```python
import torch

def _softmax(x, axis=-1):
    return torch.softmax(x, dim=axis)

def _gelu(x):
    return torch.nn.functional.gelu(x, approximate='tanh')

def _silu(x):
    return torch.nn.functional.silu(x)

def _sigmoid(x):
    return torch.sigmoid(x)

def _layernorm(x, axis=-1, eps=1e-05):
    m = x.mean(dim=axis, keepdim=True)
    v = x.var(dim=axis, keepdim=True, unbiased=False)
    return (x - m) / torch.sqrt(v + eps)

def _scatter_add(tgt, idx, src, axis=0):
    return tgt.clone().index_add_(axis, idx, src)

def _segment_sum(src, seg, n):
    out = torch.zeros((n,) + src.shape[1:], dtype=src.dtype)
    return out.index_add_(0, seg, src)

def _pad(x, pw):
    flat = tuple((v for pair in reversed(pw) for v in pair))
    return torch.nn.functional.pad(x, flat)
kernel_name = 'adaLN_cond__memory_aug_recurrent_mem_tr__swiglu_ffn__prompt'

def setup_inputs():

    _w = lambda a: (lambda t: t.to(torch.bfloat16) if t.is_floating_point() else t)(torch.as_tensor(a))

    def _A_setup():
        B, S, Dm = (1, 2048, 640)
        H, Dh = (32, 128)
        M, seg = (8, 32)
        seg = min(seg, S)
        S = S // seg * seg
        return {'x': _w((lambda *s: torch.randn(*s, dtype=torch.bfloat16) * 0.05)(B, S, Dm)), 'mem0': _w((lambda *s: torch.randn(*s, dtype=torch.bfloat16) * 0.4)(B, M, Dm)), 'Wq': _w((lambda *s: torch.randn(*s, dtype=torch.bfloat16) * 3.2)(Dm, H, Dh) * Dm ** (-0.5)), 'Wk': _w((lambda *s: torch.randn(*s, dtype=torch.bfloat16) * 3.2)(Dm, H, Dh) * Dm ** (-0.5)), 'Wv': _w((lambda *s: torch.randn(*s, dtype=torch.bfloat16) * 0.4)(Dm, H, Dh) * Dm ** (-0.5)), 'Wo': _w((lambda *s: torch.randn(*s, dtype=torch.bfloat16) * 0.4)(H, Dh, Dm) * (H * Dh) ** (-0.5)), 'seg_len': seg}
    _A_in = _A_setup()

    def _B_setup():
        B, S, D, F = (1, 2048, 640, 1728)
        return {'x': _w((lambda *s: torch.randn(*s, dtype=torch.bfloat16) * 0.05)(B, S, D)), 'w_gate': _w((lambda *s: torch.randn(*s, dtype=torch.bfloat16) * 0.05)(D, F)), 'w_up': _w((lambda *s: torch.randn(*s, dtype=torch.bfloat16) * 0.05)(D, F)), 'w_down': _w((lambda *s: torch.randn(*s, dtype=torch.bfloat16) * 0.05)(F, D))}
    _B_in = _B_setup()
    return {'A_x': _A_in['x'], 'A_mem0': _A_in['mem0'], 'A_Wq': _A_in['Wq'], 'A_Wk': _A_in['Wk'], 'A_Wv': _A_in['Wv'], 'A_Wo': _A_in['Wo'], 'A_seg_len': _A_in['seg_len'], 'B_x': _B_in['x'], 'B_w_gate': _B_in['w_gate'], 'B_w_up': _B_in['w_up'], 'B_w_down': _B_in['w_down'], 'c': _w((lambda *s: torch.randn(*s, dtype=torch.bfloat16) * 0.1)(1, 640) * 8.0), 'w_mod': _w((lambda *s: torch.randn(*s, dtype=torch.bfloat16) * 0.1)(640, 6 * 640) * 640 ** (-0.5) * 8.0)}

def reference(A_x, A_mem0, A_Wq, A_Wk, A_Wv, A_Wo, A_seg_len, B_x, B_w_gate, B_w_up, B_w_down, c, w_mod):

    def _A_ref(x, mem0, Wq, Wk, Wv, Wo, seg_len):
        B, S, Dm = x.shape
        H, Dh = (Wq.shape[1], Wq.shape[2])
        M = mem0.shape[1]
        scale = Dh ** (-0.5)
        n_seg = S // seg_len
        L = M + seg_len
        idx = torch.arange(L)
        mask = (idx[:, None] < M) | (idx[:, None] >= idx[None, :])
        mem = mem0
        outs = []
        for s in range(n_seg):
            xs = x[:, s * seg_len:(s + 1) * seg_len, :].clone()
            h = torch.concatenate([mem, xs], axis=1)
            q = torch.einsum('btd,dhe->bhte', h, Wq)
            k = torch.einsum('btd,dhe->bhte', h, Wk)
            v = torch.einsum('btd,dhe->bhte', h, Wv)
            scores = torch.einsum('bhqe,bhke->bhqk', q, k) * scale
            scores = torch.where(mask[None, None], scores, -1000000000.0)
            p = _softmax(scores, axis=-1)
            o = torch.einsum('bhqk,bhke->bhqe', p, v)
            o = torch.einsum('bhte,hed->btd', o, Wo)
            mem = o[:, :M, :].clone()
            outs.append(o[:, M:, :])
        return torch.concatenate(outs, axis=1)

    def _B_ref(x, w_gate, w_up, w_down):
        g = _silu(x @ w_gate)
        u = x @ w_up
        return g * u @ w_down
    x = A_x
    mod = (_silu(c) @ w_mod).reshape(c.shape[0], 6, 1, -1)
    sa, ca, ga, sb, cb, gb = (mod[:, 0].clone(), mod[:, 1].clone(), mod[:, 2].clone(), mod[:, 3].clone(), mod[:, 4].clone(), mod[:, 5].clone())
    h = _layernorm(x, axis=-1) * (1 + ca) + sa
    x = x + ga * _A_ref(h, A_mem0, A_Wq, A_Wk, A_Wv, A_Wo, A_seg_len)
    h = _layernorm(x, axis=-1) * (1 + cb) + sb
    x = x + gb * _B_ref(h, B_w_gate, B_w_up, B_w_down)
    return x
```

```cpp
#include <hip/hip_runtime.h>
#include <hip/hip_bf16.h>
#include <cstdio>
#include <cstdlib>

typedef unsigned short u16;
typedef __bf16 bf16x8 __attribute__((ext_vector_type(8)));
typedef __bf16 bf16x16 __attribute__((ext_vector_type(16)));
typedef __bf16 bf16x2 __attribute__((ext_vector_type(2)));
typedef float f32x2 __attribute__((ext_vector_type(2)));
typedef float f32x4 __attribute__((ext_vector_type(4)));
typedef float f32x16 __attribute__((ext_vector_type(16)));
typedef unsigned u32x2 __attribute__((ext_vector_type(2)));
typedef unsigned u32x4 __attribute__((ext_vector_type(4)));
typedef unsigned u32x8 __attribute__((ext_vector_type(8)));
#define LAS __attribute__((address_space(3)))
#define GAS __attribute__((address_space(1)))

constexpr int S = 2048, D = 640, H = 32, E = 128, HE = 4096, NSEG = 64, F = 1728;
constexpr size_t WS_CTL = 0;
constexpr size_t WS_MEM = 4096;
constexpr size_t WS_PX  = WS_MEM + 65 * 10240;
constexpr size_t WS_PM  = WS_PX + 64 * 32 * 512;
constexpr size_t WS_U   = WS_PM + 64 * 32 * 128;
constexpr size_t WS_CLR_END = WS_U + (size_t)64 * 40 * 32 * 256;
constexpr size_t WS_MOD = WS_CLR_END;
constexpr size_t WS_H1  = WS_MOD + 16384;
constexpr size_t WS_KX  = WS_H1 + (size_t)S * D * 2;
constexpr size_t WS_QX  = WS_KX + (size_t)S * HE * 2;
constexpr size_t WS_VX  = WS_QX + (size_t)S * HE * 2;
constexpr size_t WS_CF  = WS_VX + (size_t)S * HE * 2;
constexpr size_t WS_AX  = WS_CF + (size_t)64 * 40 * 32 * 1024;
constexpr size_t WS_AM  = WS_AX + (size_t)64 * 32 * 2048;
constexpr size_t WS_X1  = WS_AM + (size_t)64 * 8 * 2048;
constexpr size_t WS_H2  = WS_X1 + (size_t)S * D * 2;
constexpr size_t WS_ACT = WS_H2 + (size_t)S * D * 2;
constexpr size_t WS_DBG = WS_ACT + (size_t)S * F * 2;
constexpr size_t WS_END = WS_DBG + (2 << 20);

__device__ __forceinline__ float b2f(u16 b) { return __uint_as_float((unsigned)b << 16); }
__device__ __forceinline__ unsigned pk2(float lo, float hi) { bf16x2 v; v[0] = (__bf16)lo; v[1] = (__bf16)hi; return __builtin_bit_cast(unsigned, v); }
__device__ __forceinline__ u16 f2b(float f) { return (u16)(pk2(f, 0.f) & 0xffffu); }
__device__ __forceinline__ float rnd(float f) { return b2f(f2b(f)); }
__device__ __forceinline__ unsigned nosent(unsigned x) { return x < 0xFFFFFFFEu ? x : 0xFFFFFFFEu; }

__global__ void n_mod(const u16* c, const u16* wm, float* mod) {
    int n = blockIdx.x * 256 + threadIdx.x; float acc = 0;
    for (int k = 0; k < D; ++k) { float x = b2f(c[k]); acc += rnd(x / (1.f + __expf(-x))) * b2f(wm[(size_t)k * 3840 + n]); }
    mod[n] = rnd(acc);
}
__global__ void n_ln(const u16* x, const float* sh, const float* sc, u16* h) {
    int row = blockIdx.x, l = threadIdx.x; const u16* xr = x + (size_t)row * D;
    float s = 0; for (int i = l; i < D; i += 64) s += b2f(xr[i]);
    for (int o = 32; o; o >>= 1) s += __shfl_xor(s, o);
    float mean = s / D, v = 0; for (int i = l; i < D; i += 64) { float d = b2f(xr[i]) - mean; v += d * d; }
    for (int o = 32; o; o >>= 1) v += __shfl_xor(v, o);
    float r = rsqrtf(v / D + 1e-5f);
    for (int i = l; i < D; i += 64) h[(size_t)row * D + i] = f2b((b2f(xr[i]) - mean) * r * rnd(1.f + sc[i]) + sh[i]);
}
__global__ void n_proj(const u16* A, const u16* B, u16* C, int mode) {
    int n = blockIdx.x * 256 + threadIdx.x, m0 = blockIdx.y * 8; float acc[8] = {0,0,0,0,0,0,0,0};
    for (int k = 0; k < D; ++k) { float b = b2f(B[(size_t)k * HE + n]);
        #pragma unroll
        for (int i = 0; i < 8; ++i) acc[i] += b2f(A[(size_t)(m0 + i) * D + k]) * b; }
    for (int i = 0; i < 8; ++i) { int m = m0 + i;
        size_t o = mode == 0 ? (size_t)m * HE + n : ((((size_t)(m >> 5) * 32 + (n >> 7)) * 16 + ((n >> 3) & 15)) * 32 + (m & 31)) * 8 + (n & 7);
        C[o] = f2b(acc[i]); }
}
__global__ void n_cf(const u16* vx, const u16* Wo, u16* cf) {
    int h = blockIdx.x, seg = blockIdx.y, col = threadIdx.x;
    for (int key = 0; key < 32; ++key) { float acc = 0; const u16* v = vx + (size_t)(seg * 32 + key) * HE + h * E;
        for (int e = 0; e < E; ++e) acc += b2f(v[e]) * b2f(Wo[(size_t)(h * E + e) * D + col]);
        cf[((((size_t)seg * 40 + (col >> 4)) * 32 + h) * 64 + (key >> 3) * 16 + (col & 15)) * 8 + (key & 7)] = f2b(acc); }
}
__global__ void n_out(const u16* x, const float* ga, const u16* ax, const u16* am, const u16* cf, const u16* U, u16* x1) {
    int tok = blockIdx.x, seg = tok >> 5, tl = tok & 31, mt = tl >> 4, col = threadIdx.x; float acc = 0;
    for (int h = 0; h < H; ++h) {
        for (int key = 0; key < 32; ++key) acc += b2f(ax[((((size_t)seg * 32 + h) * 2 + mt) * 64 + (key >> 3) * 16 + (tl & 15)) * 8 + (key & 7)]) * b2f(cf[((((size_t)seg * 40 + (col >> 4)) * 32 + h) * 64 + (key >> 3) * 16 + (col & 15)) * 8 + (key & 7)]);
        for (int key = 0; key < 8; ++key) acc += b2f(am[((((size_t)seg * 8 + (h >> 2)) * 2 + mt) * 64 + (h & 3) * 16 + (tl & 15)) * 8 + key]) * b2f(U[((((size_t)seg * 40 + (col >> 4)) * 32 + h) * 16 + (col & 15)) * 8 + key]);
    }
    x1[(size_t)tok * D + col] = f2b(b2f(x[(size_t)tok * D + col]) + rnd(ga[col] * rnd(acc)));
}
__global__ void n_ffn1(const u16* h2, const u16* wg, const u16* wu, u16* act) {
    int n = blockIdx.x * 64 + threadIdx.x, m0 = blockIdx.y * 8; float g[8] = {0,0,0,0,0,0,0,0}, u[8] = {0,0,0,0,0,0,0,0};
    for (int k = 0; k < D; ++k) { float bg = b2f(wg[(size_t)k * F + n]), bu = b2f(wu[(size_t)k * F + n]);
        #pragma unroll
        for (int i = 0; i < 8; ++i) { float a = b2f(h2[(size_t)(m0 + i) * D + k]); g[i] += a * bg; u[i] += a * bu; } }
    for (int i = 0; i < 8; ++i) act[(size_t)(m0 + i) * F + n] = f2b(g[i] / (1.f + __expf(-g[i])) * u[i]);
}
__global__ void n_ffn2(const u16* act, const u16* wd, const u16* x1, const float* gb, u16* out) {
    int n = blockIdx.x * 64 + threadIdx.x, m0 = blockIdx.y * 8; float acc[8] = {0,0,0,0,0,0,0,0};
    for (int k = 0; k < F; ++k) { float b = b2f(wd[(size_t)k * D + n]);
        #pragma unroll
        for (int i = 0; i < 8; ++i) acc[i] += b2f(act[(size_t)(m0 + i) * F + k]) * b; }
    for (int i = 0; i < 8; ++i) out[(size_t)(m0 + i) * D + n] = f2b(b2f(x1[(size_t)(m0 + i) * D + n]) + rnd(gb[n] * rnd(acc[i])));
}

struct RecArgs { const u16 *mem0, *Wq, *Wk, *Wv, *Wo; unsigned char* ws; int tp[8]; };
__device__ __forceinline__ void dly(int n) { for (int i = 0; i < n; ++i) __builtin_amdgcn_s_sleep(1); }
__device__ __forceinline__ unsigned long long rtime() { unsigned long long t; asm volatile("s_memrealtime %0\n\ts_waitcnt lgkmcnt(0)" : "=s"(t) :: "memory"); return t; }
constexpr unsigned SPIN_MAX = 400000u;
#define ST(i)
#define ST_INIT
#define ST_RESET
#define ST_OUT
#define ST_T(t)
__device__ __forceinline__ u32x4 ld_sc1(__amdgpu_buffer_rsrc_t r, unsigned voff, unsigned soff) { return __builtin_amdgcn_raw_buffer_load_b128(r, voff, soff, 16); }
__device__ __forceinline__ unsigned umax3(unsigned a, unsigned b, unsigned c) { unsigned m = a > b ? a : b; return m > c ? m : c; }
__device__ __forceinline__ unsigned mx4(u32x4 v, unsigned m) { return umax3(umax3(v.x, v.y, m), v.z, v.w); }
__device__ __forceinline__ bool all_ok(unsigned m) { return __builtin_amdgcn_ballot_w64(m == 0xFFFFFFFFu) == 0ull; }
__device__ __forceinline__ f32x4 smf(u32x4 a, u32x8 b, f32x4 c, int idx) { return __builtin_amdgcn_smfmac_f32_16x16x64_bf16(__builtin_bit_cast(bf16x8, a), __builtin_bit_cast(bf16x16, b), c, idx, 0, 0); }
__device__ __forceinline__ f32x4 mf16(u32x4 a, u32x4 b, f32x4 c) { return __builtin_amdgcn_mfma_f32_16x16x32_bf16(__builtin_bit_cast(bf16x8, a), __builtin_bit_cast(bf16x8, b), c, 0, 0, 0); }
__device__ __forceinline__ f32x16 mf32(u32x4 a, u32x4 b, f32x16 c) { return __builtin_amdgcn_mfma_f32_32x32x16_bf16(__builtin_bit_cast(bf16x8, a), __builtin_bit_cast(bf16x8, b), c, 0, 0, 0); }
__device__ __forceinline__ float fmx(float a, float b) { return __builtin_elementwise_maximum(a, b); }
__device__ __forceinline__ float fold32(float a, float b) { auto r = __builtin_amdgcn_permlane32_swap(__float_as_uint(a), __float_as_uint(b), false, false); return __uint_as_float(r[0]) + __uint_as_float(r[1]); }
__device__ __forceinline__ float x32max(float a) { auto r = __builtin_amdgcn_permlane32_swap(__float_as_uint(a), __float_as_uint(a), false, false); return fmx(__uint_as_float(r[0]), __uint_as_float(r[1])); }
__device__ __forceinline__ float x32sum(float a) { return fold32(a, a); }
__device__ __forceinline__ float x16max(float a) { auto r = __builtin_amdgcn_permlane16_swap(__float_as_uint(a), __float_as_uint(a), false, false); return fmx(__uint_as_float(r[0]), __uint_as_float(r[1])); }
__device__ __forceinline__ float x16sum(float a) { auto r = __builtin_amdgcn_permlane16_swap(__float_as_uint(a), __float_as_uint(a), false, false); return __uint_as_float(r[0]) + __uint_as_float(r[1]); }
__device__ __forceinline__ float ror8(float a) { return __int_as_float(__builtin_amdgcn_update_dpp(0, __float_as_int(a), 0x128, 0xf, 0xf, true)); }
__device__ __forceinline__ int krow(int d, int gB) { return 32 * (d >> 2) + 16 * (gB >> 1) + 8 * (d & 1) + 4 * (gB & 1) + 2 * ((d >> 1) & 1); }
__device__ __forceinline__ void bar() { asm volatile("s_waitcnt lgkmcnt(0)\n\ts_barrier" ::: "memory"); }

constexpr int L_A = 0;
constexpr int L_QK = 10240;
constexpr int L_KQX = 14336;
constexpr int L_END = L_KQX + 32768;

template <bool TWO>
__device__ __forceinline__ void gather_mem(const RecArgs& a, __amdgpu_buffer_rsrc_t rs, int t, int tid, LAS unsigned char* L, bool& dead, int pre) {
    u32x4 v0, v1 = {0, 0, 0, 0};
    if (t == 0) {
        auto src = [&](int ci) { int ks = ci >> 6, l = ci & 63; return *(const u32x4*)(a.mem0 + (l & 7) * D + 64 * ks + 16 * (l >> 4) + 8 * ((l >> 3) & 1)); };
        v0 = src(tid); if (TWO) v1 = src(tid + 128);
    } else {
        const unsigned base = (unsigned)WS_MEM + t * 10240;
        dly(pre);
        for (unsigned sp = 0;; ++sp) {
            asm volatile("" ::: "memory");
            if (sp) dly(a.tp[3]);
            v0 = ld_sc1(rs, tid * 16, base); if (TWO) v1 = ld_sc1(rs, tid * 16 + 2048, base);
            unsigned m = mx4(v0, 0); if (TWO) m = mx4(v1, m);
            if (all_ok(m) || dead) break;
            if (sp > SPIN_MAX) { dead = true; break; }
        }
    }
    *(LAS u32x4*)(L + L_A + tid * 16) = v0;
    if (TWO) *(LAS u32x4*)(L + L_A + tid * 16 + 2048) = v1;
}
__device__ __forceinline__ void proj2(const u32x8 (&W)[20], LAS unsigned char* Asrc, LAS unsigned char* dstp, int idx) {
    f32x4 e = {0, 0, 0, 0}, o = {0, 0, 0, 0};
    u32x4 af[10];
    #pragma unroll
    for (int ks = 0; ks < 10; ++ks) af[ks] = *(LAS u32x4*)(Asrc + ks * 1024);
    __builtin_amdgcn_sched_barrier(0);
    #pragma unroll
    for (int ks = 0; ks < 10; ++ks) { e = smf(af[ks], W[2 * ks], e, idx); o = smf(af[ks], W[2 * ks + 1], o, idx); }
    const float e0 = fold32(e[0], e[2]), e1 = fold32(e[1], e[3]), o0 = fold32(o[0], o[2]), o1 = fold32(o[1], o[3]);
    *(LAS unsigned*)dstp = pk2(e0, o0); *(LAS unsigned*)(dstp + 16) = pk2(e1, o1);
}
__device__ __forceinline__ int proj2_dst(int wq, int lane) { const int c = lane & 15, tok = 4 * ((lane >> 4) & 1) + 2 * (lane >> 5); return ((4 * wq + (c >> 2)) * 8 + tok) * 16 + (c & 3) * 4; }
__device__ __forceinline__ void load_w_eo(u32x8 (&W)[20], const u16* mat, int cb, int lane) {
    const int c = lane & 15, gB = lane >> 4;
    #pragma unroll
    for (int ks = 0; ks < 10; ++ks) {
        #pragma unroll
        for (int d = 0; d < 8; ++d) { const int k = 64 * ks + krow(d, gB);
            const unsigned d0 = *(const unsigned*)(mat + (size_t)k * HE + cb + 2 * c), d1 = *(const unsigned*)(mat + (size_t)(k + 1) * HE + cb + 2 * c);
            W[2 * ks][d] = __builtin_amdgcn_perm(d1, d0, 0x05040100u); W[2 * ks + 1][d] = __builtin_amdgcn_perm(d1, d0, 0x07060302u); }
    }
}

#define ROLEMAP 0
constexpr float CS = 0.08838834764831845f * 1.4426950408889634f;
__global__ void __launch_bounds__(512, 2) rec_kernel(RecArgs a) {
    extern __shared__ __attribute__((aligned(16))) unsigned char smem[];
    LAS unsigned char* L = (LAS unsigned char*)smem;
    const int tid = threadIdx.x, lane = tid & 63, wave = __builtin_amdgcn_readfirstlane(tid >> 6);
    const int grp = blockIdx.x & 7, idx8 = blockIdx.x >> 3;
    int role = -1, id = 0;
    if (ROLEMAP == 0) { if (grp == 0) { role = 0; id = idx8; } else if (grp == 1) { role = 1; id = idx8; } else if (grp <= 6 && idx8 < 8) { role = 2; id = (grp - 2) * 8 + idx8; } }
    else { const int b = blockIdx.x; if (b < 32) { role = 0; id = b; } else if (b < 64) { role = 1; id = b - 32; } else if (b < 104) { role = 2; id = b - 64; } }
    if (role < 0) return;
    const __amdgpu_buffer_rsrc_t rs = __builtin_amdgcn_make_buffer_rsrc(a.ws, 0, (int)WS_END, 0x00020000);
    const int sidx = (lane & 8) ? 0xEEEE : 0x4444;
    bool dead = false;
    ST_INIT
    if (role == 0) {
        const int h = id;
        u32x8 W[20];
        load_w_eo(W, wave < 4 ? a.Wq : a.Wk, h * E + (wave & 3) * 32, lane);
        {   const u32x4* kx = (const u32x4*)(a.ws + WS_KX + (size_t)h * 8192); const u32x4* qx = (const u32x4*)(a.ws + WS_QX + (size_t)h * 8192);
            *(LAS u32x4*)(L + L_KQX + tid * 16) = kx[tid]; *(LAS u32x4*)(L + L_KQX + 8192 + tid * 16) = qx[tid]; }
        LAS unsigned char* Asrc = L + L_A + lane * 16;
        LAS unsigned char* pdst = L + L_QK + (wave >> 2) * 2048 + proj2_dst(wave & 3, lane);
        ST(0);
        if (wave == 0) {
            const int rho = lane & 15, g = lane >> 4;
            const int key = (((rho >> 2) & 1) << 3) | (((rho >> 1) & 1) << 2) | (((rho >> 3) & 1) << 1) | (rho & 1);
            LAS unsigned char* qsrc = L + L_QK + (g * 8 + (rho & 7)) * 16;
            LAS unsigned char* ksrc = L + L_KQX + (g * 32 + key) * 16;
            const bool ch = (lane & 8) != 0, g1 = lane >= 32;
            const unsigned vpx = (unsigned)WS_PX + h * 512 + ((((lane >> 3) & 1) * 2 + (g >> 1)) * 8 + (lane & 7)) * 16 + (g & 1) * 8;
            const unsigned vpm = (unsigned)WS_PM + h * 128 + (lane & 7) * 16 + (2 * (g & 1) + (g >> 1)) * 4;
            unsigned long long rt0 = 0;
            for (int t = 0; t < NSEG; ++t) {
                ST_T(t); if (t == 1) { ST_RESET; }
                if (t == 3) rt0 = rtime();
                if (t == 63 && blockIdx.x == 0 && lane == 0) *(unsigned long long*)(a.ws + WS_DBG + 1048576) = rtime() - rt0;
                gather_mem<false>(a, rs, t, tid, L, dead, a.tp[0]);
                ST(0);
                bar();
                ST(1);
                proj2(W, Asrc, pdst, sidx);
                u32x4 k0[4], k1[4];
                LAS unsigned char* kp = ksrc + (t & 1) * 16384;
                #pragma unroll
                for (int s = 0; s < 4; ++s) { k0[s] = *(LAS u32x4*)(kp + s * 2048); k1[s] = *(LAS u32x4*)(kp + s * 2048 + 256); }
                ST(2);
                bar();
                ST(3);
                u32x4 q[4], km[4];
                #pragma unroll
                for (int s = 0; s < 4; ++s) { q[s] = *(LAS u32x4*)(qsrc + s * 512); km[s] = *(LAS u32x4*)(qsrc + s * 512 + 2048); }
                __builtin_amdgcn_sched_barrier(0);
                f32x4 sm = {0, 0, 0, 0}, s0 = sm, s1 = sm;
                #pragma unroll
                for (int s = 0; s < 4; ++s) { s0 = mf16(k0[s], q[s], s0); s1 = mf16(k1[s], q[s], s1); sm = mf16(km[s], q[s], sm); }
                float v0 = ch ? s1[0] : s0[0], v1 = ch ? s1[1] : s0[1], v2 = ch ? s1[2] : s0[2], v3 = ch ? s1[3] : s0[3];
                float vm = g1 ? (ch ? sm[3] : sm[2]) : (ch ? sm[1] : sm[0]);
                float m = fmx(fmx(fmx(v0, v1), v2), fmx(v3, vm));
                m = fmx(m, ror8(m)); m = x16max(m); m = x32max(m);
                const float mc = m * CS;
                v0 = __builtin_amdgcn_exp2f(v0 * CS - mc); v1 = __builtin_amdgcn_exp2f(v1 * CS - mc); v2 = __builtin_amdgcn_exp2f(v2 * CS - mc); v3 = __builtin_amdgcn_exp2f(v3 * CS - mc); vm = __builtin_amdgcn_exp2f(vm * CS - mc);
                float sum = (v0 + v1) + (v2 + v3) + vm;
                sum += ror8(sum); sum = x16sum(sum); sum = x32sum(sum);
                const float inv = __builtin_amdgcn_rcpf(sum);
                u32x2 px; px.x = nosent(pk2(v0 * inv, v1 * inv)); px.y = nosent(pk2(v2 * inv, v3 * inv));
                vm *= inv; const float vmo = ror8(vm);
                __builtin_amdgcn_raw_buffer_store_b64(px, rs, vpx, t * 16384, 16);
                if (!ch) __builtin_amdgcn_raw_buffer_store_b32(nosent(pk2(vm, vmo)), rs, vpm, t * 4096, 16);
                ST(4);
            }
        } else if (wave == 1) {
            const int r32 = lane & 31, hf = lane >> 5;
            const int prow = (r32 & 19) | ((r32 & 4) << 1) | ((r32 & 8) >> 1);
            LAS unsigned char* qsrc = L + L_KQX + 8192 + (hf * 32 + r32) * 16;
            LAS unsigned char* ksrc = L + L_KQX + (hf * 32 + prow) * 16;
            LAS unsigned char* msrc = L + L_QK + 2048 + (hf * 8 + (prow & 7)) * 16;
            const unsigned vax = (unsigned)WS_AX + (h * 2 + (r32 >> 4)) * 1024 + (hf * 16 + (r32 & 15)) * 16;
            const unsigned vam = (unsigned)WS_AM + ((h >> 2) * 2 + (r32 >> 4)) * 1024 + ((h & 3) * 16 + (r32 & 15)) * 16;
            const int lim = r32 - 8 * hf;
            for (int t = 0; t < NSEG; ++t) {
                ST_T(t); if (t == 1) { ST_RESET; }
                gather_mem<false>(a, rs, t, tid, L, dead, 0);
                ST(0);
                bar();
                ST(1);
                proj2(W, Asrc, pdst, sidx);
                ST(2);
                bar();
                ST(3);
                f32x16 sx, sm;
                #pragma unroll
                for (int i = 0; i < 16; ++i) { sx[i] = 0; sm[i] = 0; }
                const int po = (t & 1) * 16384;
                #pragma unroll 2
                for (int s = 0; s < 8; ++s) {
                    u32x4 q = *(LAS u32x4*)(qsrc + po + s * 1024), k = *(LAS u32x4*)(ksrc + po + s * 1024), km = *(LAS u32x4*)(msrc + s * 256);
                    sx = mf32(k, q, sx); sm = mf32(km, q, sm);
                }
                float m = sm[0];
                #pragma unroll
                for (int j = 1; j < 8; ++j) m = fmx(m, sm[j]);
                #pragma unroll
                for (int i = 0; i < 16; ++i) { if (16 * (i >> 3) + (i & 7) > lim) sx[i] = -3.0e38f; m = fmx(m, sx[i]); }
                m = x32max(m);
                const float mc = m * CS; float sum = 0, summ = 0;
                #pragma unroll
                for (int i = 0; i < 16; ++i) { sx[i] = __builtin_amdgcn_exp2f(sx[i] * CS - mc); sum += sx[i]; }
                #pragma unroll
                for (int j = 0; j < 8; ++j) { sm[j] = __builtin_amdgcn_exp2f(sm[j] * CS - mc); summ += sm[j]; }
                sum = x32sum(sum) + summ;
                const float inv = __builtin_amdgcn_rcpf(sum);
                #pragma unroll
                for (int p = 0; p < 2; ++p) { u32x4 o; o.x = pk2(sx[8 * p] * inv, sx[8 * p + 1] * inv); o.y = pk2(sx[8 * p + 2] * inv, sx[8 * p + 3] * inv); o.z = pk2(sx[8 * p + 4] * inv, sx[8 * p + 5] * inv); o.w = pk2(sx[8 * p + 6] * inv, sx[8 * p + 7] * inv);
                    __builtin_amdgcn_raw_buffer_store_b128(o, rs, vax + p * 512, t * 65536, 0); }
                if (hf == 0) { u32x4 o; o.x = pk2(sm[0] * inv, sm[1] * inv); o.y = pk2(sm[2] * inv, sm[3] * inv); o.z = pk2(sm[4] * inv, sm[5] * inv); o.w = pk2(sm[6] * inv, sm[7] * inv);
                    __builtin_amdgcn_raw_buffer_store_b128(o, rs, vam, t * 16384, 0); }
                ST(4);
            }
        } else if (wave < 6) {
            const int i0 = (wave & 1) * 64 + lane;
            const unsigned vsrc = (unsigned)(wave < 4 ? WS_KX : WS_QX) + h * 8192 + i0 * 16;
            LAS unsigned char* ldst = L + L_KQX + (wave < 4 ? 0 : 8192) + i0 * 16;
            for (int t = 0; t < NSEG; ++t) {
                ST_T(t); if (t == 1) { ST_RESET; }
                gather_mem<false>(a, rs, t, tid, L, dead, a.tp[0] - 8);
                ST(0);
                bar();
                ST(1);
                proj2(W, Asrc, pdst, sidx);
                ST(2);
                bar();
                ST(3);
                if (t + 1 < NSEG) {
                    u32x4 v[4];
                    #pragma unroll
                    for (int i = 0; i < 4; ++i) v[i] = __builtin_amdgcn_raw_buffer_load_b128(rs, vsrc + i * 2048, (t + 1) * 262144, 0);
                    #pragma unroll
                    for (int i = 0; i < 4; ++i) *(LAS u32x4*)(ldst + ((t + 1) & 1) * 16384 + i * 2048) = v[i];
                }
                ST(4);
            }
        } else {
            for (int t = 0; t < NSEG; ++t) {
                ST_T(t); if (t == 1) { ST_RESET; }
                gather_mem<true>(a, rs, t, tid, L, dead, a.tp[0] + 10);
                ST(0);
                bar();
                ST(1);
                proj2(W, Asrc, pdst, sidx);
                ST(2);
                bar();
                ST(3);
            }
        }
    } else if (role == 1) {
        const int h = id;
        u32x8 W[20];
        if (wave < 4) {
            load_w_eo(W, a.Wv, h * E + wave * 32, lane);
            LAS unsigned char* Asrc = L + L_A + lane * 16;
            LAS unsigned char* pdst = L + L_QK + proj2_dst(wave, lane);
            ST(0);
            for (int t = 0; t < NSEG; ++t) {
                ST_T(t); if (t == 1) { ST_RESET; }
                gather_mem<false>(a, rs, t, tid, L, dead, a.tp[1] + 12);
                ST(0);
                bar();
                ST(1);
                proj2(W, Asrc, pdst, sidx);
                ST(2);
                bar();
                ST(3);
            }
        } else {
            const int c = lane & 15, gB = lane >> 4, b = c & 1; const unsigned sel = b ? 0x03020706u : 0x05040100u;
            #pragma unroll
            for (int tt = 0; tt < 10; ++tt)
                #pragma unroll
                for (int ks = 0; ks < 2; ++ks)
                    #pragma unroll
                    for (int d = 0; d < 8; ++d) { const int k = 64 * ks + krow(d, gB) + b;
                        const unsigned mine = *(const unsigned*)(a.Wo + (size_t)(h * E + k) * D + 16 * (10 * (wave - 4) + tt) + (c & ~1));
                        const unsigned o = (unsigned)__builtin_amdgcn_update_dpp(0, (int)mine, 0xB1, 0xf, 0xf, true);
                        W[2 * tt + ks][d] = __builtin_amdgcn_perm(o, mine, sel); }
            const unsigned vu = (unsigned)WS_U + ((10 * (wave - 4) + (lane >> 5)) * 32 + h) * 256 + (lane & 15) * 16 + ((lane >> 4) & 1) * 8;
            ST(0);
            for (int t = 0; t < NSEG; ++t) {
                ST_T(t); if (t == 1) { ST_RESET; }
                if (wave >= 6) gather_mem<true>(a, rs, t, tid, L, dead, a.tp[1]); else gather_mem<false>(a, rs, t, tid, L, dead, a.tp[1]);
                ST(0);
                bar();
                ST(1);
                bar();
                ST(3);
                const u32x4 a0 = *(LAS u32x4*)(L + L_QK + lane * 16), a1 = *(LAS u32x4*)(L + L_QK + 1024 + lane * 16);
                #pragma unroll
                for (int tp = 0; tp < 5; ++tp) {
                    f32x4 da = {0, 0, 0, 0}, db = da;
                    da = smf(a0, W[4 * tp], da, sidx); db = smf(a0, W[4 * tp + 2], db, sidx); da = smf(a1, W[4 * tp + 1], da, sidx); db = smf(a1, W[4 * tp + 3], db, sidx);
                    u32x2 o; o.x = nosent(pk2(fold32(da[0], db[0]), fold32(da[1], db[1]))); o.y = nosent(pk2(fold32(da[2], db[2]), fold32(da[3], db[3])));
                    if (a.tp[5] < 2) __builtin_amdgcn_raw_buffer_store_b64(o, rs, vu, t * 327680 + tp * 16384, 16);
                }
                ST(4);
            }
        }
    } else {
        const int c = id;
        u32x4 cf[4];
        const unsigned vcf = (unsigned)WS_CF + (c * 32 + 4 * wave) * 1024 + lane * 16;
        auto ldcf = [&](int t) {
            #pragma unroll
            for (int i = 0; i < 4; ++i) cf[i] = __builtin_amdgcn_raw_buffer_load_b128(rs, vcf + i * 1024, t * 1310720, 0); };
        ldcf(0);
        const unsigned opx = (unsigned)WS_PX + 4 * wave * 512 + lane * 16, opm = (unsigned)WS_PM + (4 * wave + (lane >> 4)) * 128 + (lane & 7) * 16, ou = (unsigned)WS_U + (c * 32 + 4 * wave) * 256 + lane * 16;
        LAS float* part = (LAS float*)(L + wave * 512) + (4 * ((lane >> 4) & 1) + 2 * (lane >> 5)) * 16 + (lane & 15);
        LAS unsigned char* rsrc = L + (((lane >> 2) & 7) * 16 + (lane >> 5) * 8 + (lane & 3) * 2) * 4;
        for (int t = 0; t < NSEG - 1; ++t) {
            ST_T(t); if (t == 1) { ST_RESET; }
            u32x4 p0, p1, pm, u;
            dly(a.tp[2]);
            for (unsigned sp = 0;; ++sp) {
                asm volatile("" ::: "memory");
                if (sp) dly(a.tp[4]);
                p0 = ld_sc1(rs, opx, t * 16384); p1 = ld_sc1(rs, opx + 1024, t * 16384); pm = ld_sc1(rs, opm, t * 4096); u = ld_sc1(rs, ou, t * 327680);
                if (all_ok(mx4(p0, mx4(p1, mx4(pm, a.tp[5] ? 0u : mx4(u, 0))))) || dead) break;
                if (sp > SPIN_MAX) { dead = true; break; }
            }
            ST(0);
            if (lane & 8) pm = (u32x4){0, 0, 0, 0};
            f32x4 acc = {0, 0, 0, 0}, acc2 = acc;
            acc = smf(p0, __builtin_shufflevector(cf[0], cf[1], 0, 1, 2, 3, 4, 5, 6, 7), acc, sidx);
            acc2 = mf16(pm, u, acc2);
            acc = smf(p1, __builtin_shufflevector(cf[2], cf[3], 0, 1, 2, 3, 4, 5, 6, 7), acc, sidx);
            acc += acc2;
            part[0] = fold32(acc[0], acc[2]); part[16] = fold32(acc[1], acc[3]);
            ST(1);
            bar();
            ST(2);
            if (wave == 0) {
                f32x2 s[8];
                #pragma unroll
                for (int w = 0; w < 8; ++w) s[w] = *(LAS f32x2*)(rsrc + w * 512);
                const f32x2 r = ((s[0] + s[1]) + (s[2] + s[3])) + ((s[4] + s[5]) + (s[6] + s[7]));
                __builtin_amdgcn_raw_buffer_store_b32(nosent(pk2(r[0], r[1])), rs, (unsigned)WS_MEM + c * 256 + lane * 4, (t + 1) * 10240, 16);
            }
            ST(3);
            if (t + 1 < NSEG - 1) ldcf(t + 1);
            ST(4);
        }
    }
    ST_OUT;
}

extern "C" void kernel_launch(void* const* d_in, const int* in_sizes, int n_in, void* d_out, int out_size, void* d_ws, size_t ws_size, hipStream_t stream) {
    const u16 *x = (const u16*)d_in[0], *mem0 = (const u16*)d_in[1], *Wq = (const u16*)d_in[2], *Wk = (const u16*)d_in[3], *Wv = (const u16*)d_in[4], *Wo = (const u16*)d_in[5];
    const u16 *wg = (const u16*)d_in[8], *wu = (const u16*)d_in[9], *wd = (const u16*)d_in[10], *c = (const u16*)d_in[11], *wm = (const u16*)d_in[12];
    unsigned char* ws = (unsigned char*)d_ws; float* mod = (float*)(ws + WS_MOD);
    hipMemsetAsync(ws, 0xFF, WS_CLR_END, stream);
    n_mod<<<15, 256, 0, stream>>>(c, wm, mod);
    n_ln<<<S, 64, 0, stream>>>(x, mod, mod + 640, (u16*)(ws + WS_H1));
    n_proj<<<dim3(16, 256), 256, 0, stream>>>((u16*)(ws + WS_H1), Wq, (u16*)(ws + WS_QX), 1);
    n_proj<<<dim3(16, 256), 256, 0, stream>>>((u16*)(ws + WS_H1), Wk, (u16*)(ws + WS_KX), 1);
    n_proj<<<dim3(16, 256), 256, 0, stream>>>((u16*)(ws + WS_H1), Wv, (u16*)(ws + WS_VX), 0);
    n_cf<<<dim3(32, 64), 640, 0, stream>>>((u16*)(ws + WS_VX), Wo, (u16*)(ws + WS_CF));
    RecArgs ra{mem0, Wq, Wk, Wv, Wo, ws, {0, 0, 0, 0, 0, 0, 0, 0}};
    hipFuncSetAttribute((const void*)rec_kernel, hipFuncAttributeMaxDynamicSharedMemorySize, 96 * 1024);
    rec_kernel<<<256, 512, 96 * 1024, stream>>>(ra);
    n_out<<<S, 640, 0, stream>>>(x, mod + 1280, (u16*)(ws + WS_AX), (u16*)(ws + WS_AM), (u16*)(ws + WS_CF), (u16*)(ws + WS_U), (u16*)(ws + WS_X1));
    n_ln<<<S, 64, 0, stream>>>((u16*)(ws + WS_X1), mod + 1920, mod + 2560, (u16*)(ws + WS_H2));
    n_ffn1<<<dim3(27, 256), 64, 0, stream>>>((u16*)(ws + WS_H2), wg, wu, (u16*)(ws + WS_ACT));
    n_ffn2<<<dim3(10, 256), 64, 0, stream>>>((u16*)(ws + WS_ACT), wd, (u16*)(ws + WS_X1), mod + 3200, (u16*)d_out);
}
```

```cpp
#include <hip/hip_runtime.h>
#include <hip/hip_bf16.h>
#include <cstdio>
#include <cstdlib>

typedef unsigned short u16;
typedef __bf16 bf16x8 __attribute__((ext_vector_type(8)));
typedef __bf16 bf16x16 __attribute__((ext_vector_type(16)));
typedef __bf16 bf16x2 __attribute__((ext_vector_type(2)));
typedef short s16x4 __attribute__((ext_vector_type(4)));
typedef float f32x2 __attribute__((ext_vector_type(2)));
typedef float f32x4 __attribute__((ext_vector_type(4)));
typedef float f32x16 __attribute__((ext_vector_type(16)));
typedef unsigned u32x2 __attribute__((ext_vector_type(2)));
typedef unsigned u32x4 __attribute__((ext_vector_type(4)));
typedef unsigned u32x8 __attribute__((ext_vector_type(8)));
typedef __amdgpu_buffer_rsrc_t rsrc_t;
#define LAS __attribute__((address_space(3)))
typedef LAS unsigned char* lptr;

constexpr int S = 2048, D = 640, H = 32, E = 128, HE = 4096, NSEG = 64, F = 1728;
constexpr unsigned WS_FH1 = 0;
constexpr unsigned WS_PQK = 1024;
constexpr unsigned WS_PVO = WS_PQK + 32 * 128;
constexpr unsigned WS_PP  = WS_PVO + 32 * 128;
constexpr unsigned WS_POUT = WS_PP + 32 * 128;
constexpr unsigned WS_PF1 = WS_POUT + 40 * 128;
constexpr unsigned WS_MOD = 24576;
constexpr unsigned WS_SLOT = 32768;
constexpr unsigned SL_PX = 0, SL_PM = 16384, SL_MEM = 20480, SL_U = 32768, SLOT = 32768 + 327680;
constexpr unsigned WS_CLR_END = WS_SLOT + 2 * SLOT;
constexpr unsigned WS_H1  = WS_SLOT + 66 * SLOT;
constexpr unsigned WS_KX  = WS_H1 + S * D * 2;
constexpr unsigned WS_QX  = WS_KX + S * HE * 2;
constexpr unsigned WS_CF  = WS_QX + S * HE * 2;
constexpr unsigned WS_AX  = WS_CF + 64u * 40 * 32 * 1024;
constexpr unsigned WS_AM  = WS_AX + 64 * 32 * 2048;
constexpr unsigned WS_X1  = WS_AM + 64 * 8 * 2048;
constexpr unsigned WS_ACT = WS_X1 + S * D * 2;
constexpr unsigned WS_DBG = WS_ACT + S * F * 2;
constexpr unsigned WS_END = WS_DBG + (2 << 20);

__device__ __forceinline__ float b2f(u16 b) { return __uint_as_float((unsigned)b << 16); }
__device__ __forceinline__ float blo(unsigned x) { return __uint_as_float(x << 16); }
__device__ __forceinline__ float bhi(unsigned x) { return __uint_as_float(x & 0xffff0000u); }
__device__ __forceinline__ unsigned pk2(float lo, float hi) { bf16x2 v; v[0] = (__bf16)lo; v[1] = (__bf16)hi; return __builtin_bit_cast(unsigned, v); }
__device__ __forceinline__ float rnd(float f) { return blo(pk2(f, 0.f)); }
__device__ __forceinline__ unsigned nosent(unsigned x) { return x < 0xFFFFFFFEu ? x : 0xFFFFFFFEu; }

struct Args { const u16 *x, *mem0, *Wq, *Wk, *Wv, *Wo, *wg, *wu, *wd, *c, *wm; u16* out; unsigned char* ws; int tp[8]; };
__device__ __forceinline__ void dly(int n) { for (int i = 0; i < n; ++i) __builtin_amdgcn_s_sleep(1); }
__device__ __forceinline__ unsigned long long rtime() { unsigned long long t; asm volatile("s_memrealtime %0\n\ts_waitcnt lgkmcnt(0)" : "=s"(t) :: "memory"); return t; }
constexpr unsigned SPIN_MAX = 200000u;
#define TL(i)
#define ST(i)
#define ST_INIT
#define ST_RESET
#define ST_OUT
#define ST_T(t)
__device__ __forceinline__ rsrc_t mkrs(const void* p, unsigned bytes) { return __builtin_amdgcn_make_buffer_rsrc((void*)p, 0, (int)bytes, 0x00020000); }
__device__ __forceinline__ u32x4 ld_sc1(rsrc_t r, unsigned voff, unsigned soff) { return __builtin_amdgcn_raw_buffer_load_b128(r, voff, soff, 16); }
__device__ __forceinline__ u32x4 ld_pl(rsrc_t r, unsigned voff, unsigned soff) { return __builtin_amdgcn_raw_buffer_load_b128(r, voff, soff, 0); }
__device__ __forceinline__ void st128(u32x4 v, rsrc_t r, unsigned voff, unsigned soff) { __builtin_amdgcn_raw_buffer_store_b128(v, r, voff, soff, 16); }
__device__ __forceinline__ void st64(u32x2 v, rsrc_t r, unsigned voff, unsigned soff) { __builtin_amdgcn_raw_buffer_store_b64(v, r, voff, soff, 16); }
__device__ __forceinline__ void st32(unsigned v, rsrc_t r, unsigned voff, unsigned soff) { __builtin_amdgcn_raw_buffer_store_b32(v, r, voff, soff, 16); }
__device__ __forceinline__ unsigned umax3(unsigned a, unsigned b, unsigned c) { unsigned m = a > b ? a : b; return m > c ? m : c; }
__device__ __forceinline__ unsigned mx4(u32x4 v, unsigned m) { return umax3(umax3(v.x, v.y, m), v.z, v.w); }
__device__ __forceinline__ bool all_ok(unsigned m) { return __builtin_amdgcn_ballot_w64(m == 0xFFFFFFFFu) == 0ull; }
__device__ __forceinline__ f32x4 smf(u32x4 a, u32x8 b, f32x4 c, int idx) { return __builtin_amdgcn_smfmac_f32_16x16x64_bf16(__builtin_bit_cast(bf16x8, a), __builtin_bit_cast(bf16x16, b), c, idx, 0, 0); }
__device__ __forceinline__ f32x4 mf16(u32x4 a, u32x4 b, f32x4 c) { return __builtin_amdgcn_mfma_f32_16x16x32_bf16(__builtin_bit_cast(bf16x8, a), __builtin_bit_cast(bf16x8, b), c, 0, 0, 0); }
__device__ __forceinline__ f32x16 mf32(u32x4 a, u32x4 b, f32x16 c) { return __builtin_amdgcn_mfma_f32_32x32x16_bf16(__builtin_bit_cast(bf16x8, a), __builtin_bit_cast(bf16x8, b), c, 0, 0, 0); }
__device__ __forceinline__ float fmx(float a, float b) { return __builtin_elementwise_maximum(a, b); }
__device__ __forceinline__ float fold32(float a, float b) { auto r = __builtin_amdgcn_permlane32_swap(__float_as_uint(a), __float_as_uint(b), false, false); return __uint_as_float(r[0]) + __uint_as_float(r[1]); }
__device__ __forceinline__ float x32max(float a) { auto r = __builtin_amdgcn_permlane32_swap(__float_as_uint(a), __float_as_uint(a), false, false); return fmx(__uint_as_float(r[0]), __uint_as_float(r[1])); }
__device__ __forceinline__ float x32sum(float a) { return fold32(a, a); }
__device__ __forceinline__ float x16max(float a) { auto r = __builtin_amdgcn_permlane16_swap(__float_as_uint(a), __float_as_uint(a), false, false); return fmx(__uint_as_float(r[0]), __uint_as_float(r[1])); }
__device__ __forceinline__ float x16sum(float a) { auto r = __builtin_amdgcn_permlane16_swap(__float_as_uint(a), __float_as_uint(a), false, false); return __uint_as_float(r[0]) + __uint_as_float(r[1]); }
template <int N> __device__ __forceinline__ float ror(float a) { return __int_as_float(__builtin_amdgcn_update_dpp(0, __float_as_int(a), 0x120 + N, 0xf, 0xf, true)); }
__device__ __forceinline__ float sum16(float a) { a += ror<8>(a); a += ror<4>(a); a += ror<2>(a); a += ror<1>(a); return a; }
__device__ __forceinline__ void bar() { asm volatile("s_waitcnt lgkmcnt(0)\n\ts_barrier" ::: "memory"); }
__device__ __forceinline__ void drain() { asm volatile("s_waitcnt vmcnt(0)" ::: "memory"); }
__device__ __forceinline__ u32x2 trd(lptr p) { return __builtin_bit_cast(u32x2, __builtin_amdgcn_ds_read_tr16_b64_v4i16((LAS s16x4*)p)); }
__device__ __forceinline__ u32x4 lrd(lptr p) { return *(LAS u32x4*)p; }
__device__ __forceinline__ void lwr(lptr p, u32x4 v) { *(LAS u32x4*)p = v; }
__device__ __forceinline__ float dot2(unsigned a, unsigned b, float c) { return __builtin_amdgcn_fdot2_f32_bf16(__builtin_bit_cast(bf16x2, a), __builtin_bit_cast(bf16x2, b), c, false); }

__device__ __forceinline__ void wait_prog(rsrc_t rs, unsigned voff, int target, int& seen, bool& dead, int slp) {
    if (seen >= target) return;
    for (unsigned sp = 0;; ++sp) {
        asm volatile("" ::: "memory");
        const int v = (int)__builtin_amdgcn_raw_buffer_load_b32(rs, voff, 0, 16);
        if (__builtin_amdgcn_ballot_w64(v < target) == 0ull) { seen = __builtin_amdgcn_ballot_w64(v < target + 4) == 0ull ? target + 4 : target; break; }
        if (dead) break;
        if (sp > SPIN_MAX) { dead = true; break; }
        dly(slp);
    }
    asm volatile("" ::: "memory");
}

constexpr int L_A = 0;
constexpr int L_QK = 10240;
constexpr int L_KQX = 14336;
constexpr int L_STG = 49152;
constexpr int L_MOD = 98304;
constexpr int L_TOT = 106496;

template <bool WO>
__device__ __forceinline__ void load_w_smf(u32x8 (&W)[20], rsrc_t rw, unsigned colbyte, int lane, lptr stg) {
    const int gB = lane >> 4, q = (lane >> 2) & 3;
    const unsigned ld = WO ? D * 2 : HE * 2;
    const unsigned voff = (WO ? 4 * (q >> 1) + 8 * (gB >> 1) + 2 * (gB & 1) + 16 * (q & 1) : 16 * (gB >> 1) + 8 * (q >> 1) + 4 * (gB & 1) + (q & 1)) * ld + colbyte + (lane & 3) * 16;
    u32x4 v[40];
    #pragma unroll
    for (int i = 0; i < 40; ++i) { const int pi = i & 3;
        const unsigned so = WO ? (64 * ((i >> 2) & 1) + 32 * (pi >> 1) + (pi & 1)) * ld + (i >> 3) * 64 : (64 * (i >> 2) + 32 * (pi >> 1) + 2 * (pi & 1)) * ld;
        v[i] = ld_pl(rw, voff, so); }
    __builtin_amdgcn_sched_barrier(0);
    lptr wp = stg + lane * 16, rp = stg + (lane >> 2) * 64 + (lane & 3) * 8;
    #pragma unroll
    for (int i = 0; i < 40; ++i) { const int pi = i & 3;
        lwr(wp + pi * 1024, v[i]);
        const u32x2 r0 = trd(rp + pi * 1024), r1 = trd(rp + pi * 1024 + 32);
        const int i0 = WO ? 4 * (i >> 3) + ((i >> 2) & 1) : 2 * (i >> 2), i1 = WO ? i0 + 2 : i0 + 1;
        W[i0][2 * pi] = r0.x; W[i0][2 * pi + 1] = r0.y; W[i1][2 * pi] = r1.x; W[i1][2 * pi + 1] = r1.y; }
}
template <bool WO>
__device__ __forceinline__ void load_w_32(u32x4 (&W)[40], rsrc_t rw, unsigned colbyte, int lane, lptr stg) {
    const int rho = lane >> 2, j = rho & 7;
    const unsigned ld = WO ? D * 2 : HE * 2;
    const unsigned voff = (WO ? 4 * (rho >> 3) + (j >> 1) + 16 * (j & 1) : rho) * ld + colbyte + (lane & 3) * 16;
    #pragma unroll
    for (int i = 0; i < 40; ++i) W[i] = ld_pl(rw, voff, WO ? (32 * ((i & 7) >> 1) + 8 * (i & 1)) * ld + (i >> 3) * 64 : 16 * i * ld);
    __builtin_amdgcn_sched_barrier(0);
    const int G = lane >> 4;
    lptr wp = stg + lane * 16, rp = stg + (8 * (G >> 1) + ((lane >> 2) & 3)) * 64 + (G & 1) * 32 + (lane & 3) * 8;
    #pragma unroll
    for (int i = 0; i < 40; ++i) { const int pi = i & 3;
        lwr(wp + pi * 1024, W[i]);
        const u32x2 r0 = trd(rp + pi * 1024), r1 = trd(rp + pi * 1024 + 256);
        W[i] = (u32x4){r0.x, r0.y, r1.x, r1.y}; }
}

template <bool TWO>
__device__ __forceinline__ void gather_mem(const Args& a, rsrc_t rs, int t, int tid, lptr L, bool& dead, int pre) {
    u32x4 v0, v1 = {0, 0, 0, 0};
    if (t == 0) {
        auto src = [&](int ci) { int ks = ci >> 6, l = ci & 63; return *(const u32x4*)(a.mem0 + (l & 7) * D + 64 * ks + 16 * (l >> 4) + 8 * ((l >> 3) & 1)); };
        v0 = src(tid); if (TWO) v1 = src(tid + 128);
    } else {
        const unsigned base = WS_SLOT + SL_MEM + (t - 1) * SLOT;
        dly(pre);
        for (unsigned sp = 0;; ++sp) {
            asm volatile("" ::: "memory");
            if (sp) dly(a.tp[3]);
            v0 = ld_sc1(rs, tid * 16, base); if (TWO) v1 = ld_sc1(rs, tid * 16 + 2048, base);
            unsigned m = mx4(v0, 0); if (TWO) m = mx4(v1, m);
            if (all_ok(m) || dead) break;
            if (sp > SPIN_MAX) { dead = true; break; }
        }
    }
    lwr(L + L_A + tid * 16, v0);
    if (TWO) lwr(L + L_A + tid * 16 + 2048, v1);
}
__device__ __forceinline__ void proj2(const u32x8 (&W)[20], lptr Asrc, lptr dstp, int idx) {
    f32x4 e = {0, 0, 0, 0}, o = {0, 0, 0, 0};
    u32x4 af[10];
    #pragma unroll
    for (int ks = 0; ks < 10; ++ks) af[ks] = lrd(Asrc + ks * 1024);
    __builtin_amdgcn_sched_barrier(0);
    #pragma unroll
    for (int ks = 0; ks < 10; ++ks) { e = smf(af[ks], W[2 * ks], e, idx); o = smf(af[ks], W[2 * ks + 1], o, idx); }
    const float e0 = fold32(e[0], e[2]), e1 = fold32(e[1], e[3]), o0 = fold32(o[0], o[2]), o1 = fold32(o[1], o[3]);
    *(LAS unsigned*)dstp = pk2(e0, o0); *(LAS unsigned*)(dstp + 16) = pk2(e1, o1);
}
__device__ __forceinline__ int proj2_dst(int wq, int lane) { const int c = lane & 15, tok = 4 * ((lane >> 4) & 1) + 2 * (lane >> 5); return ((4 * wq + (c >> 2)) * 8 + tok) * 16 + (c & 3) * 4; }

__device__ __forceinline__ void tile640(const u32x4 (&W)[40], lptr img, int lane, u32x4& o0, u32x4& o1) {
    const int tok = lane & 31, kg = lane >> 5;
    f32x16 acc;
    #pragma unroll
    for (int i = 0; i < 16; ++i) acc[i] = 0;
    lptr b[4];
    #pragma unroll
    for (int m = 0; m < 4; ++m) b[m] = img + kg * 512 + m * 1024 + ((tok ^ (2 * m + kg)) * 16);
    #pragma unroll
    for (int ks = 0; ks < 40; ++ks) acc = mf32(W[ks], lrd(b[ks & 3] + (ks >> 2) * 4096), acc);
    o0 = (u32x4){pk2(acc[0], acc[8]), pk2(acc[1], acc[9]), pk2(acc[2], acc[10]), pk2(acc[3], acc[11])};
    o1 = (u32x4){pk2(acc[4], acc[12]), pk2(acc[5], acc[13]), pk2(acc[6], acc[14]), pk2(acc[7], acc[15])};
}

constexpr float CS = 0.08838834764831845f * 1.4426950408889634f;
enum { R_P, R_V, R_C, R_OUT, R_QK, R_VO, R_F1, R_F2, R_SP };
__global__ void __launch_bounds__(512, 2) mega(Args a) {
    extern __shared__ __attribute__((aligned(16))) unsigned char smem[];
    lptr L = (lptr)smem;
    const int tid = threadIdx.x, lane = tid & 63, wave = __builtin_amdgcn_readfirstlane(tid >> 6);
    const int grp = blockIdx.x & 7, idx8 = blockIdx.x >> 3;
    int role, id, hid = -1;
    if (grp == 0) { role = R_P; id = idx8; }
    else if (grp == 1) { role = R_V; id = idx8; }
    else if (grp == 7) { if (idx8 < 27) { role = R_F1; id = idx8; hid = 80 + id; } else { role = R_F2; id = idx8 - 27; hid = 107 + id; } }
    else if (idx8 < 8) { role = R_C; id = (grp - 2) * 8 + idx8; hid = id; }
    else if (idx8 < 16) { role = R_OUT; id = (grp - 2) * 8 + idx8 - 8; hid = 40 + id; }
    else { const int wi = (idx8 - 16) * 5 + grp - 2;
        if (wi < 32) { role = R_QK; id = wi; } else if (wi < 64) { role = R_VO; id = wi - 32; } else if (wi < 79) { role = R_F2; id = wi - 59; hid = 107 + id; } else { role = R_SP; id = 0; hid = 127; } }
    const rsrc_t rs = mkrs(a.ws, WS_END);
    const int sidx = (lane & 8) ? 0xEEEE : 0x4444;
    bool dead = false;
    lptr stg = L + L_STG + wave * 4096;
    ST_INIT
    TL(0);
    if (hid >= 0) {
        const int row = hid * 16 + wave * 2 + (lane >> 5), l32 = lane & 31;
        u32x2 xv[5];
        #pragma unroll
        for (int i = 0; i < 5; ++i) xv[i] = *(const u32x2*)(a.x + row * D + (l32 + 32 * i) * 4);
        if (hid < 120) {
            u32x4 wv[5]; float sv[5];
            #pragma unroll
            for (int i = 0; i < 5; ++i) { const int k = (tid >> 2) + 128 * i; wv[i] = *(const u32x4*)(a.wm + k * 3840 + hid * 32 + (tid & 3) * 8); sv[i] = b2f(a.c[k]); }
            float acc[8] = {0, 0, 0, 0, 0, 0, 0, 0};
            #pragma unroll
            for (int i = 0; i < 5; ++i) { const float s = rnd(sv[i] / (1.f + __expf(-sv[i])));
                #pragma unroll
                for (int j = 0; j < 4; ++j) { acc[2 * j] += s * blo(wv[i][j]); acc[2 * j + 1] += s * bhi(wv[i][j]); } }
            #pragma unroll
            for (int j = 0; j < 8; ++j) { float v = acc[j]; v += ror<8>(v); v += ror<4>(v); v = x16sum(v); acc[j] = x32sum(v); }
            if (lane < 4) { *(LAS f32x4*)(L + wave * 128 + lane * 32) = (f32x4){acc[0], acc[1], acc[2], acc[3]}; *(LAS f32x4*)(L + wave * 128 + lane * 32 + 16) = (f32x4){acc[4], acc[5], acc[6], acc[7]}; }
            bar();
            if (tid < 16) { f32x2 s = {0, 0};
                #pragma unroll
                for (int w = 0; w < 8; ++w) s += *(LAS f32x2*)(L + w * 128 + tid * 8);
                st32(nosent(pk2(s[0], s[1])), rs, WS_MOD + hid * 64 + tid * 4, 0); }
        }
        float xf[20], s = 0;
        #pragma unroll
        for (int i = 0; i < 5; ++i) { xf[4 * i] = blo(xv[i].x); xf[4 * i + 1] = bhi(xv[i].x); xf[4 * i + 2] = blo(xv[i].y); xf[4 * i + 3] = bhi(xv[i].y); }
        #pragma unroll
        for (int i = 0; i < 20; ++i) s += xf[i];
        const float mean = x16sum(sum16(s)) * (1.f / D);
        float vs = 0;
        #pragma unroll
        for (int i = 0; i < 20; ++i) { const float d = xf[i] - mean; vs += d * d; }
        const float var = x16sum(sum16(vs)) * (1.f / D);
        const float mb = rnd(mean), rd = 1.f / rnd(__builtin_sqrtf(rnd(rnd(var) + 1e-5f)));
        #pragma unroll
        for (int i = 0; i < 20; ++i) xf[i] = rnd(rnd(xf[i] - mb) * rd);
        {   u32x4 mv; const unsigned mo = WS_MOD + (tid < 480 ? tid : 479) * 16;
            for (unsigned sp = 0;; ++sp) { asm volatile("" ::: "memory"); mv = ld_sc1(rs, mo, 0); if (all_ok(mx4(mv, 0))) break; if (sp > SPIN_MAX) { dead = true; break; } dly(2); }
            if (tid < 480) lwr(L + L_MOD + tid * 16, mv); }
        bar();
        #pragma unroll
        for (int i = 0; i < 5; ++i) { const u32x2 sa = *(LAS u32x2*)(L + L_MOD + (l32 + 32 * i) * 8), ca = *(LAS u32x2*)(L + L_MOD + 1280 + (l32 + 32 * i) * 8);
            u32x2 o;
            o.x = pk2(rnd(xf[4 * i] * rnd(1.f + blo(ca.x))) + blo(sa.x), rnd(xf[4 * i + 1] * rnd(1.f + bhi(ca.x))) + bhi(sa.x));
            o.y = pk2(rnd(xf[4 * i + 2] * rnd(1.f + blo(ca.y))) + blo(sa.y), rnd(xf[4 * i + 3] * rnd(1.f + bhi(ca.y))) + bhi(sa.y));
            st64(o, rs, WS_H1 + row * (D * 2) + (l32 + 32 * i) * 8, 0); }
        drain(); bar();
        if (tid == 0) st32(0u, rs, WS_FH1 + hid * 4, 0);
        TL(1);
    }
    if (role == R_P) {
        const int h = id;
        u32x8 W[20];
        load_w_smf<false>(W, mkrs(wave < 4 ? a.Wq : a.Wk, D * HE * 2), (h * E + (wave & 3) * 32) * 2, lane, stg);
        TL(1);
        lptr Asrc = L + L_A + lane * 16;
        lptr pdst = L + L_QK + (wave >> 2) * 2048 + proj2_dst(wave & 3, lane);
        ST(0);
        if (wave == 0) {
            const int rho = lane & 15, g = lane >> 4;
            const int key = (((rho >> 2) & 1) << 3) | (((rho >> 1) & 1) << 2) | (((rho >> 3) & 1) << 1) | (rho & 1);
            lptr qsrc = L + L_QK + (g * 8 + (rho & 7)) * 16;
            lptr ksrc = L + L_KQX + (g * 32 + key) * 16;
            const bool ch = (lane & 8) != 0, g1 = lane >= 32;
            const unsigned vpx = WS_SLOT + SL_PX + h * 512 + ((((lane >> 3) & 1) * 2 + (g >> 1)) * 8 + (lane & 7)) * 16 + (g & 1) * 8;
            const unsigned vpm = WS_SLOT + SL_PM + h * 128 + (lane & 7) * 16 + (2 * (g & 1) + (g >> 1)) * 4;
            unsigned long long rt0 = 0;
            for (int t = 0; t < NSEG; ++t) {
                ST_T(t); if (t == 1) { ST_RESET; }
                gather_mem<false>(a, rs, t, tid, L, dead, a.tp[0]);
                ST(0);
                bar();
                ST(1);
                proj2(W, Asrc, pdst, sidx);
                u32x4 k0[4], k1[4];
                lptr kp = ksrc + (t & 1) * 16384;
                #pragma unroll
                for (int s = 0; s < 4; ++s) { k0[s] = lrd(kp + s * 2048); k1[s] = lrd(kp + s * 2048 + 256); }
                ST(2);
                bar();
                ST(3);
                u32x4 q[4], km[4];
                #pragma unroll
                for (int s = 0; s < 4; ++s) { q[s] = lrd(qsrc + s * 512); km[s] = lrd(qsrc + s * 512 + 2048); }
                __builtin_amdgcn_sched_barrier(0);
                f32x4 sm = {0, 0, 0, 0}, s0 = sm, s1 = sm;
                #pragma unroll
                for (int s = 0; s < 4; ++s) { s0 = mf16(k0[s], q[s], s0); s1 = mf16(k1[s], q[s], s1); sm = mf16(km[s], q[s], sm); }
                float v0 = ch ? s1[0] : s0[0], v1 = ch ? s1[1] : s0[1], v2 = ch ? s1[2] : s0[2], v3 = ch ? s1[3] : s0[3];
                float vm = g1 ? (ch ? sm[3] : sm[2]) : (ch ? sm[1] : sm[0]);
                float m = fmx(fmx(fmx(v0, v1), v2), fmx(v3, vm));
                m = fmx(m, ror<8>(m)); m = x16max(m); m = x32max(m);
                const float mc = m * CS;
                v0 = __builtin_amdgcn_exp2f(v0 * CS - mc); v1 = __builtin_amdgcn_exp2f(v1 * CS - mc); v2 = __builtin_amdgcn_exp2f(v2 * CS - mc); v3 = __builtin_amdgcn_exp2f(v3 * CS - mc); vm = __builtin_amdgcn_exp2f(vm * CS - mc);
                float sum = (v0 + v1) + (v2 + v3) + vm;
                sum += ror<8>(sum); sum = x16sum(sum); sum = x32sum(sum);
                const float inv = __builtin_amdgcn_rcpf(sum);
                u32x2 px; px.x = nosent(pk2(v0 * inv, v1 * inv)); px.y = nosent(pk2(v2 * inv, v3 * inv));
                vm *= inv; const float vmo = ror<8>(vm);
                st64(px, rs, vpx, t * SLOT);
                if (!ch) st32(nosent(pk2(vm, vmo)), rs, vpm, t * SLOT);
                st64((u32x2){~0u, ~0u}, rs, vpx, (t + 2) * SLOT);
                if (!ch) st32(~0u, rs, vpm, (t + 2) * SLOT);
                ST(4);
            }
        } else if (wave == 1) {
            const int r32 = lane & 31, hf = lane >> 5;
            const int prow = (r32 & 19) | ((r32 & 4) << 1) | ((r32 & 8) >> 1);
            lptr qsrc = L + L_KQX + 8192 + (hf * 32 + r32) * 16;
            lptr ksrc = L + L_KQX + (hf * 32 + prow) * 16;
            lptr msrc = L + L_QK + 2048 + (hf * 8 + (prow & 7)) * 16;
            const unsigned vax = WS_AX + (h * 2 + (r32 >> 4)) * 1024 + (hf * 16 + (r32 & 15)) * 16;
            const unsigned vam = WS_AM + ((h >> 2) * 2 + (r32 >> 4)) * 1024 + ((h & 3) * 16 + (r32 & 15)) * 16;
            const int lim = r32 - 8 * hf;
            for (int t = 0; t < NSEG; ++t) {
                ST_T(t); if (t == 1) { ST_RESET; }
                gather_mem<false>(a, rs, t, tid, L, dead, 0);
                ST(0);
                bar();
                ST(1);
                proj2(W, Asrc, pdst, sidx);
                ST(2);
                bar();
                ST(3);
                f32x16 sx, sm;
                #pragma unroll
                for (int i = 0; i < 16; ++i) { sx[i] = 0; sm[i] = 0; }
                const int po = (t & 1) * 16384;
                #pragma unroll 2
                for (int s = 0; s < 8; ++s) {
                    u32x4 q = lrd(qsrc + po + s * 1024), k = lrd(ksrc + po + s * 1024), km = lrd(msrc + s * 256);
                    sx = mf32(k, q, sx); sm = mf32(km, q, sm);
                }
                float m = sm[0];
                #pragma unroll
                for (int j = 1; j < 8; ++j) m = fmx(m, sm[j]);
                #pragma unroll
                for (int i = 0; i < 16; ++i) { if (16 * (i >> 3) + (i & 7) > lim) sx[i] = -3.0e38f; m = fmx(m, sx[i]); }
                m = x32max(m);
                const float mc = m * CS; float sum = 0, summ = 0;
                #pragma unroll
                for (int i = 0; i < 16; ++i) { sx[i] = __builtin_amdgcn_exp2f(sx[i] * CS - mc); sum += sx[i]; }
                #pragma unroll
                for (int j = 0; j < 8; ++j) { sm[j] = __builtin_amdgcn_exp2f(sm[j] * CS - mc); summ += sm[j]; }
                sum = x32sum(sum) + summ;
                const float inv = __builtin_amdgcn_rcpf(sum);
                #pragma unroll
                for (int p = 0; p < 2; ++p) { u32x4 o; o.x = pk2(sx[8 * p] * inv, sx[8 * p + 1] * inv); o.y = pk2(sx[8 * p + 2] * inv, sx[8 * p + 3] * inv); o.z = pk2(sx[8 * p + 4] * inv, sx[8 * p + 5] * inv); o.w = pk2(sx[8 * p + 6] * inv, sx[8 * p + 7] * inv);
                    st128(o, rs, vax + p * 512, t * 65536); }
                if (hf == 0) { u32x4 o; o.x = pk2(sm[0] * inv, sm[1] * inv); o.y = pk2(sm[2] * inv, sm[3] * inv); o.z = pk2(sm[4] * inv, sm[5] * inv); o.w = pk2(sm[6] * inv, sm[7] * inv);
                    st128(o, rs, vam, t * 16384); }
                drain();
                if (lane == 0) st32((unsigned)t, rs, WS_PP + h * 128, 0);
                ST(4);
            }
        } else if (wave < 6) {
            const int i0 = (wave & 1) * 64 + lane;
            const unsigned vsrc = (wave < 4 ? WS_KX : WS_QX) + h * 8192 + i0 * 16;
            lptr ldst = L + L_KQX + (wave < 4 ? 0 : 8192) + i0 * 16;
            int seen = -1;
            auto fetch = [&](int t) {
                wait_prog(rs, WS_PQK + h * 128, t, seen, dead, 2);
                u32x4 v[4];
                #pragma unroll
                for (int i = 0; i < 4; ++i) v[i] = ld_sc1(rs, vsrc + i * 2048, t * 262144);
                #pragma unroll
                for (int i = 0; i < 4; ++i) lwr(ldst + (t & 1) * 16384 + i * 2048, v[i]); };
            fetch(0);
            for (int t = 0; t < NSEG; ++t) {
                ST_T(t); if (t == 1) { ST_RESET; }
                gather_mem<false>(a, rs, t, tid, L, dead, a.tp[0] - 8);
                ST(0);
                bar();
                ST(1);
                proj2(W, Asrc, pdst, sidx);
                ST(2);
                bar();
                ST(3);
                if (t + 1 < NSEG) fetch(t + 1);
                ST(4);
            }
        } else {
            for (int t = 0; t < NSEG; ++t) {
                ST_T(t); if (t == 1) { ST_RESET; }
                gather_mem<true>(a, rs, t, tid, L, dead, a.tp[0] + 10);
                ST(0);
                bar();
                ST(1);
                proj2(W, Asrc, pdst, sidx);
                ST(2);
                bar();
                ST(3);
            }
        }
    } else if (role == R_V) {
        const int h = id;
        u32x8 W[20];
        if (wave < 4) {
            load_w_smf<false>(W, mkrs(a.Wv, D * HE * 2), (h * E + wave * 32) * 2, lane, stg);
            TL(1);
            lptr Asrc = L + L_A + lane * 16;
            lptr pdst = L + L_QK + proj2_dst(wave, lane);
            ST(0);
            for (int t = 0; t < NSEG; ++t) {
                ST_T(t); if (t == 1) { ST_RESET; }
                gather_mem<false>(a, rs, t, tid, L, dead, a.tp[1] + 12);
                ST(0);
                bar();
                ST(1);
                proj2(W, Asrc, pdst, sidx);
                ST(2);
                bar();
                ST(3);
            }
        } else {
            load_w_smf<true>(W, mkrs(a.Wo, D * HE * 2), h * E * D * 2 + (wave - 4) * 320, lane, stg);
            TL(1);
            const unsigned vu = WS_SLOT + SL_U + ((10 * (wave - 4) + (lane >> 5)) * 32 + h) * 256 + (lane & 15) * 16 + ((lane >> 4) & 1) * 8;
            ST(0);
            for (int t = 0; t < NSEG; ++t) {
                ST_T(t); if (t == 1) { ST_RESET; }
                if (wave >= 6) gather_mem<true>(a, rs, t, tid, L, dead, a.tp[1]); else gather_mem<false>(a, rs, t, tid, L, dead, a.tp[1]);
                ST(0);
                bar();
                ST(1);
                bar();
                ST(3);
                const u32x4 a0 = lrd(L + L_QK + lane * 16), a1 = lrd(L + L_QK + 1024 + lane * 16);
                #pragma unroll
                for (int tp = 0; tp < 5; ++tp) {
                    f32x4 da = {0, 0, 0, 0}, db = da;
                    da = smf(a0, W[4 * tp], da, sidx); db = smf(a0, W[4 * tp + 2], db, sidx); da = smf(a1, W[4 * tp + 1], da, sidx); db = smf(a1, W[4 * tp + 3], db, sidx);
                    u32x2 o; o.x = nosent(pk2(fold32(da[0], db[0]), fold32(da[1], db[1]))); o.y = nosent(pk2(fold32(da[2], db[2]), fold32(da[3], db[3])));
                    st64(o, rs, vu, t * SLOT + tp * 16384);
                }
                #pragma unroll
                for (int tp = 0; tp < 5; ++tp) st64((u32x2){~0u, ~0u}, rs, vu, (t + 2) * SLOT + tp * 16384);
                ST(4);
            }
        }
    } else if (role == R_C) {
        const int c = id;
        u32x4 cf[4];
        const unsigned vcf = WS_CF + (c * 32 + 4 * wave) * 1024 + lane * 16;
        int seen = -1;
        auto ldcf = [&](int t) {
            wait_prog(rs, WS_PVO + (4 * wave + (lane & 3)) * 128, t, seen, dead, 2);
            #pragma unroll
            for (int i = 0; i < 4; ++i) cf[i] = ld_sc1(rs, vcf + i * 1024, t * 1310720); };
        ldcf(0);
        const unsigned opx = WS_SLOT + SL_PX + 4 * wave * 512 + lane * 16, opm = WS_SLOT + SL_PM + (4 * wave + (lane >> 4)) * 128 + (lane & 7) * 16, ou = WS_SLOT + SL_U + (c * 32 + 4 * wave) * 256 + lane * 16;
        LAS float* part = (LAS float*)(L + wave * 512) + (4 * ((lane >> 4) & 1) + 2 * (lane >> 5)) * 16 + (lane & 15);
        lptr rsrc = L + (((lane >> 2) & 7) * 16 + (lane >> 5) * 8 + (lane & 3) * 2) * 4;
        for (int t = 0; t < NSEG - 1; ++t) {
            ST_T(t); if (t == 1) { ST_RESET; }
            u32x4 p0, p1, pm, u;
            dly(a.tp[2]);
            for (unsigned sp = 0;; ++sp) {
                asm volatile("" ::: "memory");
                if (sp) dly(a.tp[4]);
                p0 = ld_sc1(rs, opx, t * SLOT); p1 = ld_sc1(rs, opx + 1024, t * SLOT); pm = ld_sc1(rs, opm, t * SLOT); u = ld_sc1(rs, ou, t * SLOT);
                if (all_ok(mx4(p0, mx4(p1, mx4(pm, mx4(u, 0))))) || dead) break;
                if (sp > SPIN_MAX) { dead = true; break; }
            }
            ST(0);
            if (lane & 8) pm = (u32x4){0, 0, 0, 0};
            f32x4 acc = {0, 0, 0, 0}, acc2 = acc;
            acc = smf(p0, __builtin_shufflevector(cf[0], cf[1], 0, 1, 2, 3, 4, 5, 6, 7), acc, sidx);
            acc2 = mf16(pm, u, acc2);
            acc = smf(p1, __builtin_shufflevector(cf[2], cf[3], 0, 1, 2, 3, 4, 5, 6, 7), acc, sidx);
            acc += acc2;
            part[0] = fold32(acc[0], acc[2]); part[16] = fold32(acc[1], acc[3]);
            ST(1);
            bar();
            ST(2);
            if (wave == 0) {
                f32x2 s[8];
                #pragma unroll
                for (int w = 0; w < 8; ++w) s[w] = *(LAS f32x2*)(rsrc + w * 512);
                const f32x2 r = ((s[0] + s[1]) + (s[2] + s[3])) + ((s[4] + s[5]) + (s[6] + s[7]));
                st32(nosent(pk2(r[0], r[1])), rs, WS_SLOT + SL_MEM + c * 256 + lane * 4, t * SLOT);
                st32(~0u, rs, WS_SLOT + SL_MEM + c * 256 + lane * 4, (t + 2) * SLOT);
            }
            ST(3);
            if (t + 1 < NSEG - 1) ldcf(t + 1);
            ST(4);
            if (t == 0) TL(2);
        }
    } else if (role == R_QK || role == R_VO) {
        const int h = id; const bool vo = role == R_VO, st2 = vo && wave >= 4;
        u32x4 W[40];
        if (st2) load_w_32<true>(W, mkrs(a.Wo, D * HE * 2), h * E * D * 2 + (wave - 4) * 320, lane, stg);
        else load_w_32<false>(W, mkrs(vo ? a.Wv : wave < 4 ? a.Wq : a.Wk, D * HE * 2), (h * E + (wave & 3) * 32) * 2, lane, stg);
        TL(1);
        bar();
        const unsigned vh = WS_H1 + (tid >> 4) * (D * 2) + (tid & 15) * 16;
        lptr wdst = L + (tid & 15) * 512 + (((tid >> 4) ^ (tid & 7)) * 16);
        lptr vbuf = L + 81920;
        bool allh = false;
        u32x4 hv[5];
        auto ldh = [&](int s) {
            if (!allh) for (unsigned sp = 0;; ++sp) { asm volatile("" ::: "memory");
                const u32x2 f = __builtin_amdgcn_raw_buffer_load_b64(rs, WS_FH1 + lane * 8, 0, 16);
                const unsigned long long bad = __builtin_amdgcn_ballot_w64((f.x | f.y) != 0u);
                allh = bad == 0ull;
                if (!((bad >> s) & 1ull) || dead) break;
                if (sp > SPIN_MAX) { dead = true; break; }
                dly(2); }
            asm volatile("" ::: "memory");
            #pragma unroll
            for (int i = 0; i < 5; ++i) hv[i] = ld_sc1(rs, vh + i * 256, s * (32 * D * 2)); };
        auto sth = [&](int s) {
            #pragma unroll
            for (int i = 0; i < 5; ++i) lwr(wdst + (s & 1) * 40960 + i * 8192, hv[i]); };
        ldh(0); sth(0); bar();
        if (!vo) {
            const unsigned vdst = (wave < 4 ? WS_QX : WS_KX) + h * 8192 + (wave & 3) * 2048 + lane * 16;
            for (int s = 0; s < NSEG; ++s) {
                if (s + 1 < NSEG) ldh(s + 1);
                u32x4 o0, o1;
                tile640(W, L + (s & 1) * 40960, lane, o0, o1);
                st128(o0, rs, vdst, s * 262144); st128(o1, rs, vdst + 1024, s * 262144);
                if (s + 1 < NSEG) sth(s + 1);
                drain(); bar();
                if (tid == 0) st32((unsigned)s, rs, WS_PQK + h * 128, 0);
                if (s == 0) TL(2);
            }
        } else if (!st2) {
            for (int s = 0; s <= NSEG; ++s) {
                if (s + 1 < NSEG) ldh(s + 1);
                if (s < NSEG) { u32x4 o0, o1;
                    tile640(W, L + (s & 1) * 40960, lane, o0, o1);
                    lwr(vbuf + (s & 1) * 8192 + wave * 2048 + lane * 16, o0); lwr(vbuf + (s & 1) * 8192 + wave * 2048 + 1024 + lane * 16, o1); }
                if (s + 1 < NSEG) sth(s + 1);
                bar();
                if (tid == 0 && s > 0) st32((unsigned)(s - 1), rs, WS_PVO + h * 128, 0);
                if (s == 1) TL(2);
            }
        } else {
            const int r32 = lane & 31, hf = lane >> 5;
            const int prow = (r32 & 19) | ((r32 & 4) << 1) | ((r32 & 8) >> 1);
            lptr asrc = vbuf + hf * 512 + prow * 16;
            const unsigned vdst = WS_CF + ((10 * (wave - 4) + (r32 >> 4)) * 32 + h) * 1024 + (hf * 16 + (r32 & 15)) * 16;
            for (int s = 0; s <= NSEG; ++s) {
                if (s + 1 < NSEG) ldh(s + 1);
                if (s > 0) {
                    u32x4 af[8];
                    #pragma unroll
                    for (int ks = 0; ks < 8; ++ks) af[ks] = lrd(asrc + ((s - 1) & 1) * 8192 + ks * 1024);
                    #pragma unroll
                    for (int tt = 0; tt < 5; ++tt) {
                        f32x16 acc;
                        #pragma unroll
                        for (int i = 0; i < 16; ++i) acc[i] = 0;
                        #pragma unroll
                        for (int ks = 0; ks < 8; ++ks) acc = mf32(af[ks], W[8 * tt + ks], acc);
                        #pragma unroll
                        for (int p = 0; p < 2; ++p) st128((u32x4){pk2(acc[8 * p], acc[8 * p + 1]), pk2(acc[8 * p + 2], acc[8 * p + 3]), pk2(acc[8 * p + 4], acc[8 * p + 5]), pk2(acc[8 * p + 6], acc[8 * p + 7])}, rs, vdst + p * 512, (s - 1) * 1310720 + tt * 65536);
                    }
                }
                if (s + 1 < NSEG) sth(s + 1);
                drain(); bar();
            }
        }
    } else if (role == R_OUT) {
        const int cg = id >> 1, mt = id & 1;
        const unsigned vcf = WS_CF + (cg * 64 + 4 * wave) * 1024 + lane * 16;
        const unsigned vax = WS_AX + (8 * wave + mt) * 1024 + lane * 16, vam = WS_AM + (2 * wave + mt) * 1024 + lane * 16;
        const unsigned vu = WS_SLOT + SL_U + (cg * 64 + 4 * wave) * 256 + lane * 16;
        lptr part = L + wave * 2304 + (lane & 15) * 144 + (lane >> 4) * 16;
        lptr rsrc = L + (lane >> 2) * 144 + (lane & 3) * 32;
        const unsigned vx = (mt * 16 + (lane >> 2)) * (D * 2) + cg * 64 + (lane & 3) * 16;
        const rsrc_t rx = mkrs(a.x, S * D * 2);
        const u32x4 ga = lrd(L + L_MOD + 2560 + cg * 64 + (lane & 3) * 16);
        int seenv = -1, seenp = -1;
        for (int s = 0; s < NSEG; ++s) {
            u32x4 cf[8], xr;
            wait_prog(rs, WS_PVO + (4 * wave + (lane & 3)) * 128, s, seenv, dead, 4);
            #pragma unroll
            for (int i = 0; i < 8; ++i) cf[i] = ld_sc1(rs, vcf + (i & 3) * 1024, s * 1310720 + (i >> 2) * 32768);
            xr = ld_pl(rx, vx, s * (32 * D * 2));
            wait_prog(rs, WS_PP + (4 * wave + (lane & 3)) * 128, s, seenp, dead, 4);
            u32x4 ax[4], am, u0, u1;
            #pragma unroll
            for (int i = 0; i < 4; ++i) ax[i] = ld_sc1(rs, vax + i * 2048, s * 65536);
            am = ld_sc1(rs, vam, s * 16384);
            for (unsigned sp = 0;; ++sp) { asm volatile("" ::: "memory");
                u0 = ld_sc1(rs, vu, s * SLOT); u1 = ld_sc1(rs, vu, s * SLOT + 8192);
                if (all_ok(mx4(u0, mx4(u1, 0))) || dead) break;
                if (sp > SPIN_MAX) { dead = true; break; }
                dly(2); }
            f32x4 d0 = {0, 0, 0, 0}, d1 = d0;
            #pragma unroll
            for (int i = 0; i < 4; ++i) { d0 = mf16(cf[i], ax[i], d0); d1 = mf16(cf[4 + i], ax[i], d1); }
            d0 = mf16(u0, am, d0); d1 = mf16(u1, am, d1);
            lptr pp = part + (s & 1) * 18432;
            *(LAS f32x4*)pp = d0; *(LAS f32x4*)(pp + 64) = d1;
            bar();
            if (wave == (s & 7)) {
                f32x4 r0 = {0, 0, 0, 0}, r1 = r0;
                #pragma unroll
                for (int w = 0; w < 8; ++w) { r0 += *(LAS f32x4*)(rsrc + (s & 1) * 18432 + w * 2304); r1 += *(LAS f32x4*)(rsrc + (s & 1) * 18432 + w * 2304 + 16); }
                u32x4 o;
                o.x = pk2(blo(xr.x) + rnd(blo(ga.x) * rnd(r0[0])), bhi(xr.x) + rnd(bhi(ga.x) * rnd(r0[1])));
                o.y = pk2(blo(xr.y) + rnd(blo(ga.y) * rnd(r0[2])), bhi(xr.y) + rnd(bhi(ga.y) * rnd(r0[3])));
                o.z = pk2(blo(xr.z) + rnd(blo(ga.z) * rnd(r1[0])), bhi(xr.z) + rnd(bhi(ga.z) * rnd(r1[1])));
                o.w = pk2(blo(xr.w) + rnd(blo(ga.w) * rnd(r1[2])), bhi(xr.w) + rnd(bhi(ga.w) * rnd(r1[3])));
                st128(o, rs, WS_X1 + vx, s * (32 * D * 2));
                drain();
                if (lane == 0) st32((unsigned)s, rs, WS_POUT + id * 128, 0);
            }
            if (s == 0) TL(2);
        }
    } else if (role == R_F1) {
        u32x4 W[20];
        f32x4 cs = {0, 0, 0, 0}, bs = cs;
        {   const rsrc_t rg = mkrs(a.wg, D * F * 2), ru = mkrs(a.wu, D * F * 2);
            const unsigned vw = lane * (F * 2) + (id * 64 + wave * 8) * 2;
            u32x4 g[10], u[10];
            #pragma unroll
            for (int i = 0; i < 10; ++i) { g[i] = ld_pl(rg, vw, i * 64 * F * 2); u[i] = ld_pl(ru, vw, i * 64 * F * 2); }
            __builtin_amdgcn_sched_barrier(0);
            lptr wp = stg + lane * 32, rp = stg + (8 * (lane >> 4) + ((lane >> 2) & 3)) * 32 + (lane & 3) * 8;
            const u32x4 ones = {0x3F803F80u, 0x3F803F80u, 0x3F803F80u, 0x3F803F80u};
            #pragma unroll
            for (int i = 0; i < 10; ++i) {
                lwr(wp, g[i]); lwr(wp + 16, u[i]);
                const float sc = rnd(1.f + b2f(*(LAS u16*)(L + L_MOD + 5120 + (64 * i + lane) * 2)));
                u32x4 gs, us;
                #pragma unroll
                for (int j = 0; j < 4; ++j) { gs[j] = pk2(blo(g[i][j]) * sc, bhi(g[i][j]) * sc); us[j] = pk2(blo(u[i][j]) * sc, bhi(u[i][j]) * sc); }
                lwr(wp + 2048, gs); lwr(wp + 2048 + 16, us);
                #pragma unroll
                for (int k2 = 0; k2 < 2; ++k2) {
                    const u32x2 a0 = trd(rp + k2 * 1024), a1 = trd(rp + k2 * 1024 + 128), b0 = trd(rp + 2048 + k2 * 1024), b1 = trd(rp + 2048 + k2 * 1024 + 128);
                    bs = mf16((u32x4){a0.x, a0.y, a1.x, a1.y}, lrd(L + L_MOD + 3840 + (64 * i + 32 * k2 + 8 * (lane >> 4)) * 2), bs);
                    W[2 * i + k2] = (u32x4){b0.x, b0.y, b1.x, b1.y};
                    cs = mf16(W[2 * i + k2], ones, cs); }
            } }
        float csg[4], csu[4], bg[4], bu[4];
        #pragma unroll
        for (int i = 0; i < 4; ++i) { auto r = __builtin_amdgcn_permlane32_swap(__float_as_uint(cs[i]), __float_as_uint(cs[i]), false, false); csg[i] = __uint_as_float(r[0]); csu[i] = __uint_as_float(r[1]);
            auto q = __builtin_amdgcn_permlane32_swap(__float_as_uint(bs[i]), __float_as_uint(bs[i]), false, false); bg[i] = __uint_as_float(q[0]); bu[i] = __uint_as_float(q[1]); }
        TL(2);
        const unsigned vh = WS_X1 + (tid >> 4) * (D * 2) + (tid & 15) * 16;
        lptr wdst = L + (tid & 15) * 512 + (((tid >> 4) ^ (tid & 7)) * 16);
        const int kg = lane >> 4, t16 = lane & 15;
        lptr b0 = L + kg * 512 + ((t16 ^ kg) * 16), b1 = L + 2048 + kg * 512 + ((t16 ^ (4 + kg)) * 16);
        lptr stat = L + 40960;
        const unsigned vact = WS_ACT + ((lane >> 5) * 54 + 2 * id + (wave >> 2)) * 1024 + ((wave & 3) * 16 + t16) * 16 + ((lane >> 4) & 1) * 8;
        int seen = -1;
        for (int s = 0; s < NSEG; ++s) {
            if (wave == 0) wait_prog(rs, WS_POUT + (lane < 40 ? lane : 0) * 128, s, seen, dead, 2);
            bar();
            u32x4 hv[5];
            #pragma unroll
            for (int i = 0; i < 5; ++i) hv[i] = ld_sc1(rs, vh + i * 256, s * (32 * D * 2));
            float s1 = 0, s2 = 0;
            #pragma unroll
            for (int i = 0; i < 5; ++i) { lwr(wdst + i * 8192, hv[i]);
                #pragma unroll
                for (int j = 0; j < 4; ++j) { const unsigned w = hv[i][j]; s1 = dot2(w, 0x3F803F80u, s1); s2 = dot2(w, w, s2); } }
            s1 = sum16(s1) * (1.f / D); s2 = sum16(s2) * (1.f / D);
            { const float var = __builtin_fmaxf(s2 - s1 * s1, 0.f);
              if ((tid & 15) == 0) *(LAS f32x2*)(stat + (tid >> 4) * 8) = (f32x2){rnd(s1), 1.f / rnd(__builtin_sqrtf(rnd(rnd(var) + 1e-5f)))}; }
            bar();
            f32x4 d0 = {0, 0, 0, 0}, d1 = d0;
            #pragma unroll
            for (int ks = 0; ks < 20; ++ks) { lptr p = ((ks & 1) ? b1 : b0) + (ks >> 1) * 4096; d0 = mf16(W[ks], lrd(p), d0); d1 = mf16(W[ks], lrd(p + 256), d1); }
            const f32x2 st = *(LAS f32x2*)(stat + ((lane >> 5) * 16 + t16) * 8);
            float act[4];
            #pragma unroll
            for (int i = 0; i < 4; ++i) { auto r = __builtin_amdgcn_permlane32_swap(__float_as_uint(d0[i]), __float_as_uint(d1[i]), false, false);
                const float g = rnd((__uint_as_float(r[0]) - st[0] * csg[i]) * st[1] + bg[i]), u = rnd((__uint_as_float(r[1]) - st[0] * csu[i]) * st[1] + bu[i]);
                act[i] = rnd(g * __builtin_amdgcn_rcpf(1.f + __expf(-g))) * u; }
            st64((u32x2){pk2(act[0], act[1]), pk2(act[2], act[3])}, rs, vact, s * (108 * 1024));
            drain(); bar();
            if (tid == 0) st32((unsigned)s, rs, WS_PF1 + id * 128, 0);
            if (s == 0) TL(3);
        }
    } else if (role == R_F2) {
        const int cg = id >> 1, mt = id & 1;
        const int ks0 = wave < 6 ? 7 * wave : 42 + 6 * (wave - 6);
        u32x4 W[28];
        {   const rsrc_t rw = mkrs(a.wd, F * D * 2);
            const unsigned vw = (32 * ks0 + 8 * (lane >> 4) + ((lane >> 2) & 3)) * (D * 2) + cg * 128 + (lane & 3) * 16;
            u32x4 v[28];
            #pragma unroll
            for (int i = 0; i < 28; ++i) v[i] = ld_pl(rw, vw, (i >= 24 && wave >= 6) ? 0 : (32 * (i >> 2) + 4 * (i & 1)) * (D * 2) + ((i >> 1) & 1) * 64);
            __builtin_amdgcn_sched_barrier(0);
            lptr wp = stg + lane * 16, rp = stg + (lane >> 2) * 64 + (lane & 3) * 8;
            #pragma unroll
            for (int i = 0; i < 28; ++i) { const int pi = i & 3;
                if (i >= 24 && wave >= 6) v[i] = (u32x4){0, 0, 0, 0};
                lwr(wp + pi * 1024, v[i]);
                const u32x2 r0 = trd(rp + pi * 1024), r1 = trd(rp + pi * 1024 + 32);
                const int i0 = (i & ~3) + (pi & 2);
                W[i0][2 * (pi & 1)] = r0.x; W[i0][2 * (pi & 1) + 1] = r0.y; W[i0 + 1][2 * (pi & 1)] = r1.x; W[i0 + 1][2 * (pi & 1) + 1] = r1.y; } }
        TL(2);
        bar();
        const unsigned vact = WS_ACT + (mt * 54 + ks0) * 1024 + lane * 16;
        const int pf = ((ks0 >> 1) + (lane & 3)) < 27 ? (ks0 >> 1) + (lane & 3) : 26;
        lptr part = L + wave * 4352 + (lane & 15) * 272 + (lane >> 4) * 16;
        lptr rsrc = L + (tid >> 5) * 272 + (tid & 31) * 8;
        const unsigned vx = (mt * 16 + (tid >> 5)) * (D * 2) + cg * 128 + (tid & 31) * 4;
        const unsigned gb = *(LAS unsigned*)(L + L_MOD + 6400 + cg * 128 + (tid & 31) * 4);
        const rsrc_t ro = mkrs(a.out, S * D * 2);
        int seen = -1;
        for (int s = 0; s < NSEG; ++s) {
            wait_prog(rs, WS_PF1 + pf * 128, s, seen, dead, 2);
            u32x4 af[7];
            #pragma unroll
            for (int i = 0; i < 7; ++i) af[i] = ld_sc1(rs, vact + ((i == 6 && wave >= 6) ? 0 : i * 1024), s * (108 * 1024));
            const unsigned xr = __builtin_amdgcn_raw_buffer_load_b32(rs, WS_X1 + vx, s * (32 * D * 2), 16);
            f32x4 d[4];
            #pragma unroll
            for (int j = 0; j < 4; ++j) d[j] = (f32x4){0, 0, 0, 0};
            #pragma unroll
            for (int i = 0; i < 7; ++i)
                #pragma unroll
                for (int j = 0; j < 4; ++j) d[j] = mf16(W[4 * i + j], af[i], d[j]);
            #pragma unroll
            for (int j = 0; j < 4; ++j) *(LAS f32x4*)(part + (s & 1) * 34816 + j * 64) = d[j];
            bar();
            f32x2 r = {0, 0};
            #pragma unroll
            for (int w = 0; w < 8; ++w) r += *(LAS f32x2*)(rsrc + (s & 1) * 34816 + w * 4352);
            __builtin_amdgcn_raw_buffer_store_b32(pk2(blo(xr) + rnd(blo(gb) * rnd(r[0])), bhi(xr) + rnd(bhi(gb) * rnd(r[1]))), ro, vx, s * (32 * D * 2), 0);
            if (s == 0) TL(3);
        }
    }
    TL(7);
    ST_OUT;
}

extern "C" void kernel_launch(void* const* d_in, const int* in_sizes, int n_in, void* d_out, int out_size, void* d_ws, size_t ws_size, hipStream_t stream) {
    Args a{(const u16*)d_in[0], (const u16*)d_in[1], (const u16*)d_in[2], (const u16*)d_in[3], (const u16*)d_in[4], (const u16*)d_in[5],
           (const u16*)d_in[8], (const u16*)d_in[9], (const u16*)d_in[10], (const u16*)d_in[11], (const u16*)d_in[12], (u16*)d_out, (unsigned char*)d_ws, {0, 0, 0, 0, 0, 0, 0, 0}};
    hipMemsetAsync(d_ws, 0xFF, WS_CLR_END, stream);
    hipFuncSetAttribute((const void*)mega, hipFuncAttributeMaxDynamicSharedMemorySize, L_TOT);
    mega<<<256, 512, L_TOT, stream>>>(a);
}
```

```cpp
#include <hip/hip_runtime.h>
#include <hip/hip_bf16.h>

typedef unsigned short u16;
typedef __bf16 bf16x8 __attribute__((ext_vector_type(8)));
typedef __bf16 bf16x16 __attribute__((ext_vector_type(16)));
typedef __bf16 bf16x2 __attribute__((ext_vector_type(2)));
typedef short s16x4 __attribute__((ext_vector_type(4)));
typedef float f32x2 __attribute__((ext_vector_type(2)));
typedef float f32x4 __attribute__((ext_vector_type(4)));
typedef float f32x16 __attribute__((ext_vector_type(16)));
typedef unsigned u32x2 __attribute__((ext_vector_type(2)));
typedef unsigned u32x4 __attribute__((ext_vector_type(4)));
typedef unsigned u32x8 __attribute__((ext_vector_type(8)));
typedef __amdgpu_buffer_rsrc_t rsrc_t;
#define LAS __attribute__((address_space(3)))
typedef LAS unsigned char* lptr;

constexpr int S = 2048, D = 640, H = 32, E = 128, HE = 4096, NSEG = 64, F = 1728;
constexpr unsigned WS_FH1 = 0;
constexpr unsigned WS_PQK = 1024;
constexpr unsigned WS_PVO = WS_PQK + 32 * 128;
constexpr unsigned WS_PP  = WS_PVO + 32 * 128;
constexpr unsigned WS_POUT = WS_PP + 32 * 128;
constexpr unsigned WS_PF1 = WS_POUT + 40 * 128;
constexpr unsigned WS_MOD = 24576;
constexpr unsigned WS_SLOT = 32768;
constexpr unsigned SL_PX = 0, SL_PM = 16384, SL_MEM = 20480, SL_STP = 30720, SL_U = 32768, SLOT = 32768 + 327680;
constexpr unsigned WS_CLR_END = WS_SLOT + 2 * SLOT;
constexpr unsigned WS_H1  = WS_SLOT + 66 * SLOT;
constexpr unsigned WS_KX  = WS_H1 + S * D * 2;
constexpr unsigned WS_QX  = WS_KX + S * HE * 2;
constexpr unsigned WS_CF  = WS_QX + S * HE * 2;
constexpr unsigned WS_AX  = WS_CF + 64u * 40 * 32 * 1024;
constexpr unsigned WS_AM  = WS_AX + 64 * 32 * 2048;
constexpr unsigned WS_X1  = WS_AM + 64 * 8 * 2048;
constexpr unsigned WS_ACT = WS_X1 + S * D * 2;
constexpr unsigned WS_LS  = WS_ACT + S * F * 2;
constexpr unsigned WS_DBG = WS_LS + 64 * 8192;
constexpr unsigned WS_END = WS_DBG + (2 << 20);

__device__ __forceinline__ float b2f(u16 b) { return __uint_as_float((unsigned)b << 16); }
__device__ __forceinline__ float blo(unsigned x) { return __uint_as_float(x << 16); }
__device__ __forceinline__ float bhi(unsigned x) { return __uint_as_float(x & 0xffff0000u); }
__device__ __forceinline__ unsigned pk2(float lo, float hi) { bf16x2 v; v[0] = (__bf16)lo; v[1] = (__bf16)hi; return __builtin_bit_cast(unsigned, v); }
__device__ __forceinline__ float rnd(float f) { return blo(pk2(f, 0.f)); }
__device__ __forceinline__ unsigned nosent(unsigned x) { return x < 0xFFFFFFFEu ? x : 0xFFFFFFFEu; }

struct Args { const u16 *x, *mem0, *Wq, *Wk, *Wv, *Wo, *wg, *wu, *wd, *c, *wm; u16* out; unsigned char* ws; int tp[12]; };
__device__ __forceinline__ void dly(int n) { for (int i = 0; i < n; ++i) __builtin_amdgcn_s_sleep(1); }
__device__ __forceinline__ unsigned long long rtime() { unsigned long long t; asm volatile("s_memrealtime %0\n\ts_waitcnt lgkmcnt(0)" : "=s"(t) :: "memory"); return t; }
constexpr unsigned SPIN_MAX = 200000u, OOB = 0x7FFFF000u;
#define TL(i)
#define ST(i)
#define ST_INIT
#define ST_RESET
#define ST_OUT
#define ST_T(t)
#define LG(k, val)
__device__ __forceinline__ rsrc_t mkrs(const void* p, unsigned bytes) { return __builtin_amdgcn_make_buffer_rsrc((void*)p, 0, (int)bytes, 0x00020000); }
__device__ __forceinline__ u32x4 ld_sc1(rsrc_t r, unsigned voff, unsigned soff) { return __builtin_amdgcn_raw_buffer_load_b128(r, voff, soff, 16); }
__device__ __forceinline__ u32x4 ld_pl(rsrc_t r, unsigned voff, unsigned soff) { return __builtin_amdgcn_raw_buffer_load_b128(r, voff, soff, 0); }
__device__ __forceinline__ void st128(u32x4 v, rsrc_t r, unsigned voff, unsigned soff) { __builtin_amdgcn_raw_buffer_store_b128(v, r, voff, soff, 16); asm volatile("s_nop 1" :: "v"(v)); }
__device__ __forceinline__ void st64(u32x2 v, rsrc_t r, unsigned voff, unsigned soff) { __builtin_amdgcn_raw_buffer_store_b64(v, r, voff, soff, 16); }
__device__ __forceinline__ void st32(unsigned v, rsrc_t r, unsigned voff, unsigned soff) { __builtin_amdgcn_raw_buffer_store_b32(v, r, voff, soff, 16); }
__device__ __forceinline__ unsigned umax3(unsigned a, unsigned b, unsigned c) { unsigned m = a > b ? a : b; return m > c ? m : c; }
__device__ __forceinline__ unsigned mx4(u32x4 v, unsigned m) { return umax3(umax3(v.x, v.y, m), v.z, v.w); }
__device__ __forceinline__ bool all_ok(unsigned m) { return __builtin_amdgcn_ballot_w64(m == 0xFFFFFFFFu) == 0ull; }
__device__ __forceinline__ f32x4 smf(u32x4 a, u32x8 b, f32x4 c, int idx) { return __builtin_amdgcn_smfmac_f32_16x16x64_bf16(__builtin_bit_cast(bf16x8, a), __builtin_bit_cast(bf16x16, b), c, idx, 0, 0); }
__device__ __forceinline__ f32x4 mf16(u32x4 a, u32x4 b, f32x4 c) { return __builtin_amdgcn_mfma_f32_16x16x32_bf16(__builtin_bit_cast(bf16x8, a), __builtin_bit_cast(bf16x8, b), c, 0, 0, 0); }
__device__ __forceinline__ f32x16 mf32(u32x4 a, u32x4 b, f32x16 c) { return __builtin_amdgcn_mfma_f32_32x32x16_bf16(__builtin_bit_cast(bf16x8, a), __builtin_bit_cast(bf16x8, b), c, 0, 0, 0); }
__device__ __forceinline__ float fmx(float a, float b) { return __builtin_elementwise_maximum(a, b); }
__device__ __forceinline__ float fold32(float a, float b) { auto r = __builtin_amdgcn_permlane32_swap(__float_as_uint(a), __float_as_uint(b), false, false); return __uint_as_float(r[0]) + __uint_as_float(r[1]); }
__device__ __forceinline__ float x32max(float a) { auto r = __builtin_amdgcn_permlane32_swap(__float_as_uint(a), __float_as_uint(a), false, false); return fmx(__uint_as_float(r[0]), __uint_as_float(r[1])); }
__device__ __forceinline__ float x32sum(float a) { return fold32(a, a); }
__device__ __forceinline__ float x16max(float a) { auto r = __builtin_amdgcn_permlane16_swap(__float_as_uint(a), __float_as_uint(a), false, false); return fmx(__uint_as_float(r[0]), __uint_as_float(r[1])); }
__device__ __forceinline__ float x16sum(float a) { auto r = __builtin_amdgcn_permlane16_swap(__float_as_uint(a), __float_as_uint(a), false, false); return __uint_as_float(r[0]) + __uint_as_float(r[1]); }
template <int N> __device__ __forceinline__ float ror(float a) { return __int_as_float(__builtin_amdgcn_update_dpp(0, __float_as_int(a), 0x120 + N, 0xf, 0xf, true)); }
template <int C> __device__ __forceinline__ float qp(float a) { return __int_as_float(__builtin_amdgcn_update_dpp(0, __float_as_int(a), C, 0xf, 0xf, true)); }
__device__ __forceinline__ float sum16(float a) { a += ror<8>(a); a += ror<4>(a); a += ror<2>(a); a += ror<1>(a); return a; }
__device__ __forceinline__ void bar() { asm volatile("s_waitcnt lgkmcnt(0)\n\ts_barrier" ::: "memory"); __builtin_amdgcn_s_waitcnt(0xC07F); }
__device__ __forceinline__ void drain() { asm volatile("s_waitcnt vmcnt(0)" ::: "memory"); }
__device__ __forceinline__ u32x2 trd(lptr p) { return __builtin_bit_cast(u32x2, __builtin_amdgcn_ds_read_tr16_b64_v4i16((LAS s16x4*)p)); }
__device__ __forceinline__ u32x4 lrd(lptr p) { return *(LAS u32x4*)p; }
__device__ __forceinline__ void lwr(lptr p, u32x4 v) { *(LAS u32x4*)p = v; }
__device__ __forceinline__ float dot2(unsigned a, unsigned b, float c) { return __builtin_amdgcn_fdot2_f32_bf16(__builtin_bit_cast(bf16x2, a), __builtin_bit_cast(bf16x2, b), c, false); }

__device__ __forceinline__ void peek_prog(rsrc_t rs, unsigned voff, int target, int& seen) {
    asm volatile("" ::: "memory");
    const int v = (int)__builtin_amdgcn_raw_buffer_load_b32(rs, voff, 0, 16);
    if (__builtin_amdgcn_ballot_w64(v < target) == 0ull) seen = __builtin_amdgcn_ballot_w64(v < target + 64) == 0ull ? target + 64 : __builtin_amdgcn_ballot_w64(v < target + 6) == 0ull ? target + 6 : __builtin_amdgcn_ballot_w64(v < target + 2) == 0ull ? target + 2 : target;
    asm volatile("" ::: "memory");
}
__device__ __forceinline__ void wait_prog(rsrc_t rs, unsigned voff, int target, int& seen, bool& dead, int slp) {
    if (seen >= target) return;
    for (unsigned sp = 0;; ++sp) {
        peek_prog(rs, voff, target, seen);
        if (seen >= target || dead) break;
        if (sp > SPIN_MAX) { dead = true; break; }
        dly(slp);
    }
}

constexpr int L_A = 0;
constexpr int L_QK = 10240;
constexpr int L_KQX = 14336;
constexpr int L_STG = 49152;
constexpr int L_MOD = 98304;
constexpr int L_TOT = 106496;

#define WAUX 2
template <int KIND> __device__ __forceinline__ unsigned wsoff(int r, int j) {
    const int i = 5 * r + j;
    return KIND == 0 ? (64 * (i >> 2) + 32 * ((i & 3) >> 1) + 2 * (i & 1)) * (HE * 2) : KIND == 1 ? (64 * (r >> 2) + 32 * ((r & 3) >> 1) + (r & 1)) * (D * 2) + j * 256
         : KIND == 2 ? 16 * i * (HE * 2) : (32 * (r >> 1) + 8 * (r & 1)) * (D * 2) + j * 256; }
template <int KIND> __device__ __forceinline__ void wissue(u32x4 (&v)[40], rsrc_t rw, unsigned base, int wq, int lane) {
    const int q = lane >> 4, jj = 4 * (wq & 1) + q;
    const int row = KIND == 0 ? 16 * (wq >> 1) + 8 * (q >> 1) + 4 * (wq & 1) + (q & 1) : KIND == 1 ? 4 * (q >> 1) + 8 * (wq >> 1) + 2 * (wq & 1) + 16 * (q & 1)
                  : KIND == 2 ? 4 * wq + q : 4 * (wq >> 1) + (jj >> 1) + 16 * (jj & 1);
    const unsigned voff = row * ((KIND & 1) ? D * 2 : HE * 2) + base + (lane & 15) * 16;
    #pragma unroll
    for (int i = 0; i < 40; ++i) v[i] = __builtin_amdgcn_raw_buffer_load_b128(rw, voff, wsoff<KIND>(i / 5, i % 5), WAUX);
    __builtin_amdgcn_sched_barrier(0);
}
template <bool WO>
__device__ __forceinline__ void load_w_smf(u32x8 (&W)[20], rsrc_t rw, unsigned base, int wave, int lane, lptr L) {
    const int wq = wave & 3;
    u32x4 v[40];
    wissue<WO ? 1 : 0>(v, rw, base, wq, lane);
    lptr wp = L + (wave >> 2) * 20480 + ((lane >> 2) & 3) * 1024 + (4 * wq + (lane >> 4)) * 64 + (lane & 3) * 16;
    lptr rp = L + (wave >> 2) * 20480 + (WO ? 5 * wq : wq) * 1024 + (lane >> 2) * 64 + (lane & 3) * 8;
    #pragma unroll
    for (int r = 0; r < 8; ++r) {
        #pragma unroll
        for (int j = 0; j < 5; ++j) lwr(wp + (r & 1) * 40960 + j * 4096, v[5 * r + j]);
        bar();
        #pragma unroll
        for (int j = 0; j < 5; ++j) { const int i = 5 * r + j, pi = WO ? r & 3 : i & 3;
            lptr p = rp + (r & 1) * 40960 + j * (WO ? 1024 : 4096);
            const u32x2 r0 = trd(p), r1 = trd(p + 32);
            const int i0 = WO ? 4 * j + (r >> 2) : 2 * (i >> 2), i1 = WO ? i0 + 2 : i0 + 1;
            W[i0][2 * pi] = r0.x; W[i0][2 * pi + 1] = r0.y; W[i1][2 * pi] = r1.x; W[i1][2 * pi + 1] = r1.y; }
    }
    bar();
}
template <bool WO>
__device__ __forceinline__ void load_w_32(u32x4 (&W)[40], rsrc_t rw, unsigned base, int wave, int lane, lptr L) {
    const int wq = wave & 3, G = lane >> 4;
    u32x4 v[40];
    wissue<WO ? 3 : 2>(v, rw, base, wq, lane);
    lptr wp = L + (wave >> 2) * 20480 + ((lane >> 2) & 3) * 1024 + (4 * wq + (lane >> 4)) * 64 + (lane & 3) * 16;
    lptr rp = L + (wave >> 2) * 20480 + (WO ? 5 * wq : wq) * 1024 + (8 * (G >> 1) + ((lane >> 2) & 3)) * 64 + (G & 1) * 32 + (lane & 3) * 8;
    #pragma unroll
    for (int r = 0; r < 8; ++r) {
        #pragma unroll
        for (int j = 0; j < 5; ++j) lwr(wp + (r & 1) * 40960 + j * 4096, v[5 * r + j]);
        bar();
        #pragma unroll
        for (int j = 0; j < 5; ++j) { lptr p = rp + (r & 1) * 40960 + j * (WO ? 1024 : 4096);
            const u32x2 r0 = trd(p), r1 = trd(p + 256);
            W[WO ? 8 * j + r : 5 * r + j] = (u32x4){r0.x, r0.y, r1.x, r1.y}; }
    }
    bar();
}

template <bool TWO>
__device__ __forceinline__ void gather_mem(const Args& a, rsrc_t rs, int t, int tid, lptr L, bool& dead, int pre) {
    u32x4 v0, v1 = {0, 0, 0, 0};
    if (t == 0) {
        auto src = [&](int ci) { int ks = ci >> 6, l = ci & 63; return *(const u32x4*)(a.mem0 + (2 * ((l >> 2) & 3) + (l & 1)) * D + 64 * ks + 16 * (l >> 4) + 8 * ((l >> 1) & 1)); };
        v0 = src(tid); if (TWO) v1 = src(tid + 128);
    } else {
        const unsigned base = WS_SLOT + SL_MEM + (t - 1) * SLOT;
        dly(pre);
        for (unsigned sp = 0;; ++sp) {
            asm volatile("" ::: "memory");
            if (sp) dly(a.tp[3]);
            v0 = ld_sc1(rs, tid * 16, base); if (TWO) v1 = ld_sc1(rs, tid * 16 + 2048, base);
            unsigned m = mx4(v0, 0); if (TWO) m = mx4(v1, m);
            if (all_ok(m) || dead) break;
            if (sp > SPIN_MAX) { dead = true; break; }
        }
    }
    lwr(L + L_A + tid * 16, v0);
    if (TWO) lwr(L + L_A + tid * 16 + 2048, v1);
}
__device__ __forceinline__ unsigned rt32() { return (unsigned)__builtin_amdgcn_s_memrealtime(); }
__device__ __forceinline__ void gate(unsigned target) {
    asm volatile("1:\n\ts_memrealtime vcc\n\ts_waitcnt lgkmcnt(0)\n\ts_sub_i32 vcc_lo, %0, vcc_lo\n\ts_cmp_gt_i32 vcc_lo, 0\n\ts_cbranch_scc1 1b" :: "s"(target) : "vcc", "scc", "memory"); }
template <int N> __device__ __forceinline__ int rori(int a) { return __builtin_amdgcn_update_dpp(0, a, 0x120 + N, 0xf, 0xf, true); }
__device__ __forceinline__ unsigned read_G(rsrc_t rs, int slot, int lane, bool& dead) {
    u32x2 v;
    for (unsigned sp = 0;; ++sp) { asm volatile("" ::: "memory");
        v = __builtin_amdgcn_raw_buffer_load_b64(rs, WS_SLOT + SL_STP + (lane < 40 ? lane : 0) * 32, slot * SLOT, 16);
        if (all_ok(v.x > v.y ? v.x : v.y) || dead) break;
        if (sp > SPIN_MAX) { dead = true; break; }
        dly(2); }
    const unsigned ref = rt32();
    int d = max((int)(v.x - ref), (int)(v.y - ref));
    d = max(d, rori<8>(d)); d = max(d, rori<4>(d)); d = max(d, rori<2>(d)); d = max(d, rori<1>(d));
    { auto r = __builtin_amdgcn_permlane16_swap(d, d, false, false); d = max((int)r[0], (int)r[1]); }
    { auto r = __builtin_amdgcn_permlane32_swap(d, d, false, false); d = max((int)r[0], (int)r[1]); }
    d = min(max(d, -100000), 600);
    return ref + (unsigned)__builtin_amdgcn_readfirstlane(d);
}
constexpr int L_FAIL = 47104;
#define PXL 0
#define CLRD 12
constexpr int L_DUM = 47360;
template <bool TWO>
__device__ __forceinline__ void dma_mem(rsrc_t rs, unsigned base, int tid, int wave, lptr L) {
    __builtin_amdgcn_raw_ptr_buffer_load_lds(rs, (LAS void*)(L + L_A + wave * 1024), 16, tid * 16, base, 0, 16);
    if (TWO) __builtin_amdgcn_raw_ptr_buffer_load_lds(rs, (LAS void*)(L + L_A + wave * 1024), 16, tid * 16, base, 2048, 16);
}
#define FETCH(TWO, GATE) do { if (__builtin_expect(gated && t > 1, 1)) { const unsigned b_ = WS_SLOT + SL_MEM + (t - 1) * SLOT; GATE; ST(0); bar(); ST(5); dma_mem<TWO>(rs, b_, tid, wave, L); } \
        else { gather_mem<TWO>(a, rs, t, tid, L, dead, 0); drain(); } } while (0)
#define REFETCH(TWO) do { gather_mem<TWO>(a, rs, t, tid, L, dead, 0); bar(); } while (0)
__device__ __forceinline__ void flag(unsigned m, int t, int tid, lptr L) { *(LAS int*)(L + (m == 0xFFFFFFFFu ? L_FAIL : L_DUM + (tid & 63) * 4)) = t; }
template <bool TWO>
__device__ __forceinline__ void check_own(int t, int tid, lptr L) {
    unsigned m = mx4(lrd(L + L_A + tid * 16), 0);
    if (TWO) m = mx4(lrd(L + L_A + tid * 16 + 2048), m);
    flag(m, t, tid, L);
}
__device__ __forceinline__ bool failed(int t, lptr L) { return __builtin_expect(__builtin_amdgcn_readfirstlane(*(LAS int*)(L + L_FAIL)) == t, 0); }
#define SB() __builtin_amdgcn_sched_barrier(0)
#define BIS 0
#define UBAR 3
#define PH1 2
struct Nop { __device__ void operator()() const {} };
template <int KIND, class FN = Nop>
__device__ __forceinline__ void proj2(const u32x8 (&W)[20], lptr Asrc, lptr dstp, int idx, int t, int tid, lptr L, FN mid = Nop()) {
    f32x4 e = {0, 0, 0, 0}, o = {0, 0, 0, 0};
    u32x4 af[10], own, own2;
    asm volatile("" : "+v"(e), "+v"(o));
    if (KIND == 1 || (KIND && BIS == 1)) drain();
    if (KIND) bar();
    #pragma unroll
    for (int ks = 0; ks < 4; ++ks) af[ks] = lrd(Asrc + ks * 1024);
    SB();
    #pragma unroll
    for (int ks = 0; ks < PH1; ++ks) { e = smf(af[ks], W[2 * ks], e, idx); o = smf(af[ks], W[2 * ks + 1], o, idx); }
    asm volatile("" : "+v"(e), "+v"(o));
    if (KIND > 1) drain();
    if (KIND) bar();
    if (KIND) own = lrd(L + L_A + tid * 16);
    if (KIND == 3) own2 = lrd(L + L_A + tid * 16 + 2048);
    SB();
    #pragma unroll
    for (int ks = 4; ks < 10; ++ks) af[ks] = lrd(Asrc + ks * 1024);
    SB();
    #pragma unroll
    for (int ks = PH1; ks < 4; ++ks) { e = smf(af[ks], W[2 * ks], e, idx); o = smf(af[ks], W[2 * ks + 1], o, idx); }
    SB();
    if (KIND) { asm volatile("" : "+v"(own)); unsigned m = mx4(own, 0); if (KIND == 3) m = mx4(own2, m); flag(m, t, tid, L); }
    #pragma unroll
    for (int ks = 4; ks < 10; ++ks) { e = smf(af[ks], W[2 * ks], e, idx); o = smf(af[ks], W[2 * ks + 1], o, idx); if (ks == 5) { asm volatile("" : "+v"(e), "+v"(o)); SB(); mid(); SB(); } }
    *(LAS unsigned*)dstp = pk2(e[0] + e[2], o[0] + o[2]); *(LAS unsigned*)(dstp + 16) = pk2(e[1] + e[3], o[1] + o[3]);
}
#define PROJ(KIND, GATE) do { FETCH(KIND == 3, GATE); proj2<KIND>(W, Asrc, pdst, sidx, t, tid, L); ST(2); bar(); ST(3); \
        if (failed(t, L)) { REFETCH(KIND == 3); proj2<0>(W, Asrc, pdst, sidx, t, tid, L); bar(); } } while (0)
__device__ __forceinline__ void max3x2(float a, float b, float c, float& r0, float& r1) { asm("v_maximum3_f32 %0, %2, %3, %4\n\tv_maximum3_f32 %1, %2, %3, %4\n\ts_nop 1" : "=&v"(r0), "=&v"(r1) : "v"(a), "v"(b), "v"(c)); }
__device__ __forceinline__ void addx2(float a, float b, float& r0, float& r1) { asm("v_add_f32 %0, %2, %3\n\tv_add_f32 %1, %2, %3\n\ts_nop 1" : "=&v"(r0), "=&v"(r1) : "v"(a), "v"(b)); }
__device__ __forceinline__ void sw16(float& a, float& b) { auto r = __builtin_amdgcn_permlane16_swap(__float_as_uint(a), __float_as_uint(b), false, false); a = __uint_as_float(r[0]); b = __uint_as_float(r[1]); }
__device__ __forceinline__ void sw32(float& a, float& b) { auto r = __builtin_amdgcn_permlane32_swap(__float_as_uint(a), __float_as_uint(b), false, false); a = __uint_as_float(r[0]); b = __uint_as_float(r[1]); }
__device__ __forceinline__ int proj2_dst(int wq, int lane) { const int c = lane & 15; return (2 * wq + (c >> 3)) * 256 + (4 * (lane >> 4) + 2 * ((c >> 2) & 1)) * 16 + (c & 3) * 4; }
__device__ __forceinline__ int tokoff(int tok) { return (4 * (tok >> 1) + (tok & 1)) * 16; }

template <class FN> __device__ __forceinline__ void tile640(const u32x4 (&W)[40], lptr img, int lane, u32x4& o0, u32x4& o1, FN mid) {
    const int tok = lane & 31, kg = lane >> 5;
    f32x16 acc;
    #pragma unroll
    for (int i = 0; i < 16; ++i) acc[i] = 0;
    lptr b[4];
    #pragma unroll
    for (int m = 0; m < 4; ++m) b[m] = img + kg * 512 + m * 1024 + ((tok ^ (2 * m + kg)) * 16);
    u32x4 f[4];
    #pragma unroll
    for (int m = 0; m < 4; ++m) f[m] = lrd(b[m]);
    __builtin_amdgcn_sched_barrier(0);
    #pragma unroll
    for (int ks = 0; ks < 40; ++ks) { acc = mf32(W[ks], f[ks & 3], acc); if (ks + 4 < 40) f[ks & 3] = lrd(b[ks & 3] + ((ks + 4) >> 2) * 4096); mid(ks); __builtin_amdgcn_sched_barrier(0); }
    o0 = (u32x4){pk2(acc[0], acc[8]), pk2(acc[1], acc[9]), pk2(acc[2], acc[10]), pk2(acc[3], acc[11])};
    o1 = (u32x4){pk2(acc[4], acc[12]), pk2(acc[5], acc[13]), pk2(acc[6], acc[14]), pk2(acc[7], acc[15])};
}

constexpr float CS = 0.08838834764831845f * 1.4426950408889634f;
enum { R_P, R_V, R_C, R_OUT, R_QK, R_VO, R_F1, R_F2, R_SP };
#define RMAP 2
__host__ __device__ inline void rolemap(int b, int& role, int& id) {
    const int grp = b & 7, idx8 = b >> 3;
    if (RMAP == 2) {
        if (grp < 4) {
            if (idx8 < 8) { role = R_P; id = idx8 * 4 + grp; } else if (idx8 < 16) { role = R_V; id = (idx8 - 8) * 4 + grp; } else if (idx8 < 26) { role = R_C; id = grp * 10 + idx8 - 16; }
            else if (idx8 < 31) { role = R_F2; id = (idx8 - 26) * 4 + grp; } else if (grp < 3) { role = R_QK; id = 29 + grp; } else { role = R_SP; id = 0; }
        } else if (idx8 < 10) { role = R_OUT; id = (grp - 4) * 10 + idx8; }
        else { const int wi = (idx8 - 10) * 4 + grp - 4;
            if (wi < 32) { role = R_VO; id = wi; } else if (wi < 61) { role = R_QK; id = wi - 32; } else { role = R_F1; id = wi - 61; } }
        return;
    }
    if (RMAP == 3) {
        if (idx8 < 16) { role = idx8 < 4 ? R_P : idx8 < 8 ? R_QK : idx8 < 12 ? R_V : R_VO; id = (idx8 & 3) * 8 + grp; }
        else if (idx8 < 21) { role = R_C; id = grp * 5 + idx8 - 16; }
        else if (idx8 < 25) { role = R_OUT; id = grp * 4 + idx8 - 21; }
        else if (idx8 < 27 && grp < 4) { role = R_OUT; id = 32 + grp * 2 + idx8 - 25; }
        else { const int k = idx8 < 27 ? (idx8 - 25) * 4 + grp - 4 : 8 + (idx8 - 27) * 8 + grp;
            if (k < 27) { role = R_F1; id = k; } else if (k < 47) { role = R_F2; id = k - 27; } else { role = R_SP; id = 0; } }
        return;
    }
    if (grp == 0) { role = R_P; id = idx8; return; }
    if (grp == 1) { role = R_V; id = idx8; return; }
    if (RMAP == 0) {
        if (grp == 7) { if (idx8 < 27) { role = R_F1; id = idx8; } else { role = R_F2; id = idx8 - 27; } }
        else if (idx8 < 8) { role = R_C; id = (grp - 2) * 8 + idx8; }
        else if (idx8 < 16) { role = R_OUT; id = (grp - 2) * 8 + idx8 - 8; }
        else { const int wi = (idx8 - 16) * 5 + grp - 2;
            if (wi < 32) { role = R_QK; id = wi; } else if (wi < 64) { role = R_VO; id = wi - 32; } else if (wi < 79) { role = R_F2; id = wi - 59; } else { role = R_SP; id = 0; } }
    } else {
        if (grp < 4) {
            if (idx8 < 20) { role = R_C; id = (grp - 2) * 20 + idx8; }
            else if (idx8 < 30) { role = R_F2; id = (idx8 - 20) * 2 + grp - 2; }
            else { const int k = (idx8 - 30) * 2 + grp - 2; if (k < 3) { role = R_QK; id = 29 + k; } else { role = R_SP; id = 0; } }
        } else if (idx8 < 10) { role = R_OUT; id = (grp - 4) * 10 + idx8; }
        else { const int wi = (idx8 - 10) * 4 + grp - 4;
            if (wi < 32) { role = R_VO; id = wi; } else if (wi < 61) { role = R_QK; id = wi - 32; } else { role = R_F1; id = wi - 61; } }
    }
}
__global__ void __launch_bounds__(512, 2) mega(Args a) {
    extern __shared__ __attribute__((aligned(16))) unsigned char smem[];
    lptr L = (lptr)smem;
    const int tid = threadIdx.x, lane = tid & 63, wave = __builtin_amdgcn_readfirstlane(tid >> 6);
    int role, id; rolemap(blockIdx.x, role, id);
    const int hid = role == R_C ? id : role == R_OUT ? 40 + id : role == R_F1 ? 80 + id : role == R_F2 ? 107 + id : role == R_SP ? 127 : -1;
    const rsrc_t rs = mkrs(a.ws, WS_END);
    const int sidx = (lane & 2) ? 0xEEEE : 0x4444;
    bool dead = false;
    lptr stg = L + L_STG + wave * 4096;
    ST_INIT
    TL(0);
    if (hid >= 0) {
        const int row = hid * 16 + wave * 2 + (lane >> 5), l32 = lane & 31;
        u32x2 xv[5];
        #pragma unroll
        for (int i = 0; i < 5; ++i) xv[i] = *(const u32x2*)(a.x + row * D + (l32 + 32 * i) * 4);
        if (hid < 120) {
            u32x4 wv[5]; float sv[5];
            #pragma unroll
            for (int i = 0; i < 5; ++i) { const int k = (tid >> 2) + 128 * i; wv[i] = *(const u32x4*)(a.wm + k * 3840 + hid * 32 + (tid & 3) * 8); sv[i] = b2f(a.c[k]); }
            float acc[8] = {0, 0, 0, 0, 0, 0, 0, 0};
            #pragma unroll
            for (int i = 0; i < 5; ++i) { const float s = rnd(sv[i] / (1.f + __expf(-sv[i])));
                #pragma unroll
                for (int j = 0; j < 4; ++j) { acc[2 * j] += s * blo(wv[i][j]); acc[2 * j + 1] += s * bhi(wv[i][j]); } }
            #pragma unroll
            for (int j = 0; j < 8; ++j) { float v = acc[j]; v += ror<8>(v); v += ror<4>(v); v = x16sum(v); acc[j] = x32sum(v); }
            if (lane < 4) { *(LAS f32x4*)(L + wave * 128 + lane * 32) = (f32x4){acc[0], acc[1], acc[2], acc[3]}; *(LAS f32x4*)(L + wave * 128 + lane * 32 + 16) = (f32x4){acc[4], acc[5], acc[6], acc[7]}; }
            bar();
            if (tid < 16) { f32x2 s = {0, 0};
                #pragma unroll
                for (int w = 0; w < 8; ++w) s += *(LAS f32x2*)(L + w * 128 + tid * 8);
                st32(nosent(pk2(s[0], s[1])), rs, WS_MOD + hid * 64 + tid * 4, 0); }
        }
        float xf[20], s = 0;
        #pragma unroll
        for (int i = 0; i < 5; ++i) { xf[4 * i] = blo(xv[i].x); xf[4 * i + 1] = bhi(xv[i].x); xf[4 * i + 2] = blo(xv[i].y); xf[4 * i + 3] = bhi(xv[i].y); }
        #pragma unroll
        for (int i = 0; i < 20; ++i) s += xf[i];
        const float mean = x16sum(sum16(s)) * (1.f / D);
        float vs = 0;
        #pragma unroll
        for (int i = 0; i < 20; ++i) { const float d = xf[i] - mean; vs += d * d; }
        const float var = x16sum(sum16(vs)) * (1.f / D);
        const float mb = rnd(mean), rd = 1.f / rnd(__builtin_sqrtf(rnd(rnd(var) + 1e-5f)));
        #pragma unroll
        for (int i = 0; i < 20; ++i) xf[i] = rnd(rnd(xf[i] - mb) * rd);
        {   u32x4 mv; const unsigned mo = WS_MOD + (tid < 480 ? tid : 479) * 16;
            for (unsigned sp = 0;; ++sp) { asm volatile("" ::: "memory"); mv = ld_sc1(rs, mo, 0); if (all_ok(mx4(mv, 0))) break; if (sp > SPIN_MAX) { dead = true; break; } dly(2); }
            if (tid < 480) lwr(L + L_MOD + tid * 16, mv); }
        bar();
        #pragma unroll
        for (int i = 0; i < 5; ++i) { const u32x2 sa = *(LAS u32x2*)(L + L_MOD + (l32 + 32 * i) * 8), ca = *(LAS u32x2*)(L + L_MOD + 1280 + (l32 + 32 * i) * 8);
            u32x2 o;
            o.x = pk2(rnd(xf[4 * i] * rnd(1.f + blo(ca.x))) + blo(sa.x), rnd(xf[4 * i + 1] * rnd(1.f + bhi(ca.x))) + bhi(sa.x));
            o.y = pk2(rnd(xf[4 * i + 2] * rnd(1.f + blo(ca.y))) + blo(sa.y), rnd(xf[4 * i + 3] * rnd(1.f + bhi(ca.y))) + bhi(sa.y));
            st64(o, rs, WS_H1 + row * (D * 2) + (l32 + 32 * i) * 8, 0); }
        drain(); bar();
        if (tid == 0) st32(0u, rs, WS_FH1 + hid * 4, 0);
        TL(1);
    }
    const bool gated = a.tp[0] > 0;
    const unsigned DLT = a.tp[0], D1 = a.tp[1], DU = a.tp[2], GRD = a.tp[5], PEN = a.tp[6];
    if (role == R_P) {
        const int h = id;
        u32x8 W[20];
        load_w_smf<false>(W, mkrs(wave < 4 ? a.Wq : a.Wk, D * HE * 2), h * 256, wave, lane, L);
        TL(1);
        if (tid == 0) *(LAS int*)(L + L_FAIL) = -1;
        lptr Asrc = L + L_A + lane * 16;
        lptr pdst = L + L_QK + (wave >> 2) * 2048 + proj2_dst(wave & 3, lane);
        ST(0);
        if (wave == 0) {
            const int rho = lane & 15, g = lane >> 4;
            const int key = (((rho >> 2) & 1) << 3) | (((rho >> 1) & 1) << 2) | (((rho >> 3) & 1) << 1) | (rho & 1);
            lptr qsrc = L + L_QK + (g >> 1) * 256 + (g & 1) * 32 + tokoff(rho & 7);
            lptr msrc = L + L_QK + 2048 + (g >> 1) * 256 + (g & 1) * 32 + tokoff(2 * (rho >> 2) + (rho & 1));
            lptr ksrc = L + L_KQX + (g * 32 + key) * 16;
            const bool ch = (lane & 8) != 0;
            const unsigned vpx = PXL ? WS_SLOT + SL_PX + ((lane >> 2) & 1) * 8192 + h * 256 + ((lane >> 3) & 1) * 128 + (g >> 1) * 32 + tokoff(lane & 3) + (g & 1) * 8 : WS_SLOT + SL_PX + h * 512 + ((lane >> 3) & 1) * 256 + (g >> 1) * 32 + tokoff(lane & 7) + (g & 1) * 8;
            const unsigned vpm = ch ? OOB : WS_SLOT + SL_PM + ((lane >> 2) & 1) * 2048 + (h >> 1) * 128 + tokoff(lane & 3) + (g & 1) * 32 + (h & 1) * 8 + (g >> 1) * 4;
            unsigned long long rt0 = 0; unsigned nfail = 0;
            for (int t = 0; t < NSEG; ++t) {
                ST_T(t); if (t == 2) { ST_RESET; }
                u32x4 k0[4], k1[4];
                int fl;
                auto ldk = [&]() { lptr kp = ksrc + (t & 1) * 16384;
                    #pragma unroll
                    for (int s = 0; s < 4; ++s) { k0[s] = lrd(kp + s * 2048); k1[s] = lrd(kp + s * 2048 + 256); } };
                auto scores = [&](int tt) {
                    u32x4 q[4], km[4];
                    #pragma unroll
                    for (int s = 0; s < 4; ++s) { q[s] = lrd(qsrc + s * 512); SB(); }
                    #pragma unroll
                    for (int s = 0; s < 4; ++s) { km[s] = lrd(msrc + s * 512); SB(); }
                    fl = *(LAS int*)(L + L_FAIL);
                    SB();
                    f32x4 sm = {0, 0, 0, 0}, s0 = sm, s1 = sm;
                    #pragma unroll
                    for (int s = 0; s < 4; ++s) { s0 = mf16(k0[s], q[s], s0); s1 = mf16(k1[s], q[s], s1); }
                    asm volatile("" : "+v"(s0), "+v"(s1), "+v"(km[0]), "+v"(fl));
                    #pragma unroll
                    for (int s = 0; s < 4; ++s) sm = mf16(km[s], q[s], sm);
                    const unsigned ox = fl == tt ? OOB : vpx, om = fl == tt ? OOB : vpm;
                    const float mx = fmx(fmx(fmx(fmx(s0[0], s0[1]), s0[2]), fmx(fmx(s0[3], s1[0]), s1[1])), fmx(s1[2], s1[3]));
                    float v0 = ch ? s1[0] : s0[0], v1 = ch ? s1[1] : s0[1], v2 = ch ? s1[2] : s0[2], v3 = ch ? s1[3] : s0[3];
                    SB();
                    float ma = fmx(fmx(mx, sm[0]), sm[1]), mb = fmx(fmx(mx, sm[1]), sm[0]), m2, m3;
                    float vm = ch ? sm[1] : sm[0];
                    sw16(ma, mb); max3x2(ma, mb, mb, m2, m3); sw32(m2, m3);
                    const float mc = fmx(m2, m3) * CS;
                    v0 = __builtin_amdgcn_exp2f(v0 * CS - mc); v1 = __builtin_amdgcn_exp2f(v1 * CS - mc); v2 = __builtin_amdgcn_exp2f(v2 * CS - mc); v3 = __builtin_amdgcn_exp2f(v3 * CS - mc); vm = __builtin_amdgcn_exp2f(vm * CS - mc);
                    float sa, sb2, sc, sd;
                    addx2((v0 + v1) + v2, v3 + vm, sa, sb2); sw16(sa, sb2); addx2(sa, sb2, sc, sd); sw32(sc, sd);
                    float sum = sc + sd;
                    const float vmo = ror<8>(vm);
                    sum += ror<8>(sum);
                    const float inv = __builtin_amdgcn_rcpf(sum);
                    u32x2 px; px.x = pk2(v0 * inv, v1 * inv); px.y = pk2(v2 * inv, v3 * inv);
                    st64(px, rs, ox, t * SLOT);
                    st32(pk2(vm * inv, vmo * inv), rs, om, t * SLOT); };
                FETCH(false, );
                proj2<1>(W, Asrc, pdst, sidx, t, tid, L, ldk);
                ST(2);
                bar();
                ST(3);
                if (BIS == 2) { fl = *(LAS int*)(L + L_FAIL); if (__builtin_amdgcn_readfirstlane(fl) != t) scores(t); } else scores(t);
                if (__builtin_expect(__builtin_amdgcn_readfirstlane(fl) == t, 0)) { REFETCH(false); proj2<0>(W, Asrc, pdst, sidx, t, tid, L); bar(); ldk(); scores(-2); ++nfail; }
                ST(4); LG(0, rt32());
                dly(CLRD);
                st64((u32x2){~0u, ~0u}, rs, vpx, (t + 2) * SLOT);
                st32(~0u, rs, vpm, (t + 2) * SLOT);
            }
        } else if (wave == 1) {
            const int r32 = lane & 31, hf = lane >> 5;
            const int prow = (r32 & 19) | ((r32 & 4) << 1) | ((r32 & 8) >> 1);
            lptr qsrc = L + L_KQX + 8192 + (hf * 32 + r32) * 16;
            lptr ksrc = L + L_KQX + (hf * 32 + prow) * 16;
            lptr msrc = L + L_QK + 2048 + hf * 32 + tokoff(prow & 7);
            const unsigned vax = WS_AX + (h * 2 + (r32 >> 4)) * 1024 + (hf * 16 + (r32 & 15)) * 16;
            const unsigned vam = WS_AM + ((h >> 2) * 2 + (r32 >> 4)) * 1024 + ((h & 3) * 16 + (r32 & 15)) * 16;
            const int lim = r32 - 8 * hf;
            for (int t = 0; t < NSEG; ++t) {
                ST_T(t); if (t == 2) { ST_RESET; }
                PROJ(1, );
                if (t > 0 && lane == 0) st32((unsigned)(t - 1), rs, WS_PP + h * 128, 0);
                f32x16 sx, sm;
                #pragma unroll
                for (int i = 0; i < 16; ++i) { sx[i] = 0; sm[i] = 0; }
                const int po = (t & 1) * 16384;
                #pragma unroll 2
                for (int s = 0; s < 8; ++s) {
                    u32x4 q = lrd(qsrc + po + s * 1024), k = lrd(ksrc + po + s * 1024), km = lrd(msrc + s * 256);
                    sx = mf32(k, q, sx); sm = mf32(km, q, sm);
                }
                float m = sm[0];
                #pragma unroll
                for (int j = 1; j < 8; ++j) m = fmx(m, sm[j]);
                #pragma unroll
                for (int i = 0; i < 16; ++i) { if (16 * (i >> 3) + (i & 7) > lim) sx[i] = -3.0e38f; m = fmx(m, sx[i]); }
                m = x32max(m);
                const float mc = m * CS; float sum = 0, summ = 0;
                #pragma unroll
                for (int i = 0; i < 16; ++i) { sx[i] = __builtin_amdgcn_exp2f(sx[i] * CS - mc); sum += sx[i]; }
                #pragma unroll
                for (int j = 0; j < 8; ++j) { sm[j] = __builtin_amdgcn_exp2f(sm[j] * CS - mc); summ += sm[j]; }
                sum = x32sum(sum) + summ;
                const float inv = __builtin_amdgcn_rcpf(sum);
                #pragma unroll
                for (int p = 0; p < 2; ++p) { u32x4 o; o.x = pk2(sx[8 * p] * inv, sx[8 * p + 1] * inv); o.y = pk2(sx[8 * p + 2] * inv, sx[8 * p + 3] * inv); o.z = pk2(sx[8 * p + 4] * inv, sx[8 * p + 5] * inv); o.w = pk2(sx[8 * p + 6] * inv, sx[8 * p + 7] * inv);
                    st128(o, rs, vax + p * 512, t * 65536); }
                if (hf == 0) { u32x4 o; o.x = pk2(sm[0] * inv, sm[1] * inv); o.y = pk2(sm[2] * inv, sm[3] * inv); o.z = pk2(sm[4] * inv, sm[5] * inv); o.w = pk2(sm[6] * inv, sm[7] * inv);
                    st128(o, rs, vam, t * 16384); }
                ST(4);
            }
            drain();
            if (lane == 0) st32((unsigned)(NSEG - 1), rs, WS_PP + h * 128, 0);
        } else if (wave < 6) {
            const int i0 = (wave & 1) * 64 + lane;
            const unsigned vsrc = (wave < 4 ? WS_KX : WS_QX) + h * 8192 + i0 * 16;
            lptr ldst = L + L_KQX + (wave < 4 ? 0 : 8192) + (wave & 1) * 1024;
            int seen = -1;
            auto fetch = [&](int t) {
                wait_prog(rs, WS_PQK + h * 128, t, seen, dead, 2);
                #pragma unroll
                for (int i = 0; i < 4; ++i) __builtin_amdgcn_raw_ptr_buffer_load_lds(rs, (LAS void*)(ldst + (t & 1) * 16384 + i * 2048), 16, vsrc, t * 262144 + i * 2048, 0, 16); };
            fetch(0);
            if (wave < 4) for (int t = 0; t < NSEG; ++t) {
                ST_T(t); if (t == 2) { ST_RESET; }
                PROJ(1, );
                if (seen > t) dly(a.tp[7]);
                if (t + 1 < NSEG) fetch(t + 1);
                if (seen < t + 2) peek_prog(rs, WS_PQK + h * 128, t + 2, seen);
                ST(4);
            } else for (int t = 0; t < NSEG; ++t) {
                ST_T(t); if (t == 2) { ST_RESET; }
                PROJ(2, );
                if (seen > t) dly(a.tp[7]);
                if (t + 1 < NSEG) fetch(t + 1);
                if (seen < t + 2) peek_prog(rs, WS_PQK + h * 128, t + 2, seen);
                ST(4);
            }
        } else {
            unsigned tgt = rt32();
            for (int t = 0; t < NSEG; ++t) {
                ST_T(t); if (t == 2) { ST_RESET; }
                PROJ(3, gate(tgt));
                LG(0, tgt);
                tgt = (wave == 7 && gated && t > 0) ? read_G(rs, t - 1, lane, dead) + DLT : rt32();
                ST(4);
            }
        }
    } else if (role == R_V) {
        const int h = id;
        u32x8 W[20];
        if (wave < 4) {
            load_w_smf<false>(W, mkrs(a.Wv, D * HE * 2), h * 256, wave, lane, L);
            TL(1);
            if (tid == 0) *(LAS int*)(L + L_FAIL) = -1;
            lptr Asrc = L + L_A + lane * 16;
            lptr pdst = L + L_QK + proj2_dst(wave, lane);
            unsigned tgt = rt32();
            for (int t = 0; t < NSEG; ++t) {
                ST_T(t); if (t == 2) { ST_RESET; }
                PROJ(1, gate(tgt));
                tgt = (wave == 3 && gated && t > 0) ? read_G(rs, t - 1, lane, dead) + DLT : rt32();
                ST(4);
            }
        } else {
            load_w_smf<true>(W, mkrs(a.Wo, D * HE * 2), h * E * D * 2, wave, lane, L);
            TL(1);
            const unsigned vu = WS_SLOT + SL_U + ((10 * (wave - 4) + 8 + ((lane >> 4) & 1)) * 32 + h) * 256 + (lane & 15) * 16 + (lane >> 5) * 8;
            const unsigned vw = WS_SLOT + SL_U + ((10 * (wave - 4) + (lane >> 4)) * 32 + h) * 256 + (lane & 15) * 16;
            ST(0);
            for (int t = 0; t < NSEG; ++t) {
                ST_T(t); if (t == 2) { ST_RESET; }
                f32x4 dd[10];
                int fl;
                auto zero = [&]() {
                    #pragma unroll
                    for (int j = 0; j < 10; ++j) { dd[j] = (f32x4){0, 0, 0, 0}; asm volatile("" : "+v"(dd[j])); } };
                auto ustage = [&](int tt) {
                    const u32x4 a0 = lrd(L + L_QK + lane * 16), a1 = lrd(L + L_QK + 1024 + lane * 16);
                    fl = *(LAS int*)(L + L_FAIL);
                    SB();
                    unsigned o[10], ow, ou;
                    auto fin = [&](int j) { o[j] = pk2(dd[j][0] + dd[j][2], dd[j][1] + dd[j][3]); };
                    auto s16 = [&](int j) { auto r = __builtin_amdgcn_permlane16_swap(o[j], o[j + 1], false, false); o[j] = r[0]; o[j + 1] = r[1]; };
                    auto s32 = [&](int j) { auto r = __builtin_amdgcn_permlane32_swap(o[j], o[j + 2], false, false); o[j] = r[0]; o[j + 2] = r[1]; };
                    auto m0 = [&](int j) { dd[j] = smf(a0, W[2 * j], dd[j], sidx); };
                    auto m1 = [&](int j) { dd[j] = smf(a1, W[2 * j + 1], dd[j], sidx); };
                    m0(0); m0(1); m0(2); m0(3); SB();
                    m1(0); m1(1); ow = fl == tt ? OOB : vw; ou = fl == tt ? OOB : vu; m1(2); m1(3); SB();
                    m0(4); fin(0); SB(); m0(5); fin(1); SB(); m0(6); fin(2); SB(); m0(7); s16(0); SB();
                    m1(4); fin(3); SB(); m1(5); SB(); m1(6); s16(2); SB(); m1(7); SB();
                    m0(8); s32(0); s32(1); SB(); m0(9); fin(4); SB();
                    m1(8); st128((u32x4){o[0], o[1], o[2], o[3]}, rs, ow, t * SLOT); fin(5); SB(); m1(9); fin(6); SB();
                    fin(7); s16(4); fin(8); fin(9); s16(6); s16(8); s32(4); s32(5);
                    st64((u32x2){o[8], o[9]}, rs, ou, t * SLOT);
                    st128((u32x4){o[4], o[5], o[6], o[7]}, rs, ow, t * SLOT + 32768); };
                zero();
                if (wave >= 6) { FETCH(true, ); if (BIS == 1) drain(); bar(); drain(); bar(); check_own<true>(t, tid, L); } else { FETCH(false, ); if (BIS == 1) drain(); bar(); drain(); bar(); check_own<false>(t, tid, L); }
                bar();
                ST(3);
                if (BIS == 3) { fl = *(LAS int*)(L + L_FAIL); if (__builtin_amdgcn_readfirstlane(fl) != t) ustage(t); } else ustage(t);
                if (__builtin_expect(__builtin_amdgcn_readfirstlane(fl) == t, 0)) { if (wave >= 6) REFETCH(true); else REFETCH(false);
                    zero(); bar(); ustage(-2); }
                ST(4); LG(0, rt32());
                dly(a.tp[9]);
                st128((u32x4){~0u, ~0u, ~0u, ~0u}, rs, vw, (t + 2) * SLOT); st128((u32x4){~0u, ~0u, ~0u, ~0u}, rs, vw, (t + 2) * SLOT + 32768);
                st64((u32x2){~0u, ~0u}, rs, vu, (t + 2) * SLOT);
            }
        }
    } else if (role == R_C) {
      const int c = id;
      LAS unsigned* gbox = (LAS unsigned*)(L + 65536);
      constexpr int L_CU = 66560;
      if (wave >= 4) {
        const unsigned vcf = WS_CF + c * 32768 + (wave - 4) * 8192 + lane * 16;
        int seen = -1;
        auto ldcf = [&](int t) {
            wait_prog(rs, WS_PVO + (8 * (wave - 4) + (lane & 7)) * 128, t, seen, dead, 2);
            #pragma unroll
            for (int i = 0; i < 8; ++i) __builtin_amdgcn_raw_ptr_buffer_load_lds(rs, (LAS void*)(L + (t & 1) * 32768 + (wave - 4) * 8192 + i * 1024), 16, vcf, t * 1310720 + i * 1024, 0, 16); };
        const unsigned vu = WS_SLOT + SL_U + c * 8192 + (wave - 4) * 2048 + lane * 16;
        ldcf(0); drain(); bar();
        bool pend = true;
        for (int t = 0;; ++t) {
            bar();
            __builtin_amdgcn_raw_ptr_buffer_load_lds(rs, (LAS void*)(L + L_CU + (wave - 4) * 2048), 16, vu, t * SLOT, 0, 16);
            __builtin_amdgcn_raw_ptr_buffer_load_lds(rs, (LAS void*)(L + L_CU + (wave - 4) * 2048), 16, vu, t * SLOT, 1024, 16);
            drain(); bar();
            if (t == NSEG - 2) break;
            if (pend) { ldcf(t + 1); drain(); }
            bar();
            if (seen < t + 2) { peek_prog(rs, WS_PVO + (8 * (wave - 4) + (lane & 7)) * 128, t + 2, seen); dly(a.tp[8] - 11); } else dly(a.tp[8]);
            pend = seen < t + 2;
            if (!pend && t + 2 < NSEG - 1) ldcf(t + 2);
        }
      } else if (wave >= 2) {
        const unsigned vclr = WS_SLOT + SL_MEM + c * 256 + lane * 4, vstp = lane ? OOB : WS_SLOT + SL_STP + c * 32;
        unsigned G = rt32() - 100000u;
        *gbox = G;
        bar();
        for (int t = 0;; ++t) {
            gate(G + DU);
            bar();
            bar();
            if (t == NSEG - 2) break;
            bar();
            if (wave == 2) {
                dly(CLRD);
                st32(~0u, rs, vclr, (t + 2) * SLOT); st64((u32x2){~0u, ~0u}, rs, vstp, (t + 2) * SLOT);
                if (gated) G = read_G(rs, t, lane, dead);
                drain();
                *gbox = G; }
        }
      } else {
        const int rh = wave, rho = lane & 15, kg = lane >> 4;
        u32x4 cf[32];
        auto ldcf = [&](int t) {
            #pragma unroll
            for (int i = 0; i < 32; ++i) cf[i] = lrd(L + (t & 1) * 32768 + lane * 16 + i * 1024); };
        bar();
        ldcf(0);
        const unsigned opx = PXL ? WS_SLOT + SL_PX + rh * 8192 + (2 * (rho >> 3) + (kg >> 1)) * 256 + (kg & 1) * 128 + (rho & 7) * 16 : WS_SLOT + SL_PX + (2 * (rho >> 3) + (kg >> 1)) * 512 + (kg & 1) * 256 + rh * 128 + (rho & 7) * 16;
        const unsigned opm = WS_SLOT + SL_PM + rh * 2048 + (4 * (rho >> 3) + kg) * 128 + (rho & 7) * 16;
        const unsigned ou = WS_SLOT + SL_U + c * 8192 + lane * 16;
        const unsigned vmem = (lane & 1) ? OOB : WS_SLOT + SL_MEM + c * 256 + rh * 128 + (4 * (kg & 1) + 2 * (rho >> 3) + (lane >> 5)) * 16 + (rho & 7) * 2;
        const unsigned vstp = lane ? OOB : WS_SLOT + SL_STP + c * 32 + rh * 4;
        const bool hi = lane >= 32;
        unsigned nfail = 0, nlate = 0;
        for (int t = 0; t < NSEG - 1; ++t) {
            ST_T(t); if (t == 2) { ST_RESET; }
            const unsigned sb = t * SLOT;
            u32x4 p[10], u[8];
            f32x4 ca, cb;
            unsigned res; float v;
            auto ldu = [&]() {
                #pragma unroll
                for (int i = 0; i < 8; ++i) u[i] = ld_sc1(rs, ou + (i & 3) * 1024, sb + (i >> 2) * 4096); };
            auto ldp = [&]() {
                #pragma unroll
                for (int i = 0; i < 8; ++i) { p[i] = PXL ? ld_sc1(rs, opx + (i & 3) * 1024, sb + (i >> 2) * 4096) : ld_sc1(rs, opx + (i & 1) * 2048, sb + (i >> 1) * 4096); __builtin_amdgcn_sched_barrier(0); }
                p[8] = ld_sc1(rs, opm, sb); __builtin_amdgcn_sched_barrier(0); p[9] = ld_sc1(rs, opm + 1024, sb); };
            auto comb = [&](bool first) {
                #pragma unroll
                for (int f = 0; f < 8; ++f) {
                    ca = smf(p[f], __builtin_shufflevector(cf[4 * f], cf[4 * f + 1], 0, 1, 2, 3, 4, 5, 6, 7), ca, sidx);
                    cb = smf(p[f], __builtin_shufflevector(cf[4 * f + 2], cf[4 * f + 3], 0, 1, 2, 3, 4, 5, 6, 7), cb, sidx);
                    if (first && f == UBAR) { asm volatile("" : "+v"(ca), "+v"(cb)); bar();
                        #pragma unroll
                        for (int i = 0; i < 8; ++i) u[i] = lrd(L + L_CU + lane * 16 + i * 1024); }
                    __builtin_amdgcn_sched_barrier(0); }
                #pragma unroll
                for (int f = 0; f < 2; ++f) {
                    ca = smf(p[8 + f], __builtin_shufflevector(u[4 * f], u[4 * f + 1], 0, 1, 2, 3, 4, 5, 6, 7), ca, sidx);
                    cb = smf(p[8 + f], __builtin_shufflevector(u[4 * f + 2], u[4 * f + 3], 0, 1, 2, 3, 4, 5, 6, 7), cb, sidx);
                    __builtin_amdgcn_sched_barrier(0); }
                const float t0 = hi ? cb[0] + cb[2] : ca[0] + ca[2], t1 = hi ? cb[1] + cb[3] : ca[1] + ca[3];
                v = fold32(t0, t1);
                res = pk2(v, __int_as_float(__builtin_amdgcn_update_dpp(0, __float_as_int(v), 0xB1, 0xf, 0xf, true)));
                asm volatile("" : "+v"(res)); };
            ca = (f32x4){0, 0, 0, 0}; cb = ca;
            asm volatile("" : "+v"(ca), "+v"(cb));
            bar();
            for (int i = 0; i < D1; ++i) asm volatile("s_nop 3");
            ST(5);
            ldp();
            const unsigned G = __builtin_amdgcn_readfirstlane(*gbox);
            __builtin_amdgcn_sched_barrier(0);
            comb(true);
            if (__builtin_expect(__builtin_amdgcn_ballot_w64(v != v) != 0ull, 0)) {
                for (unsigned sp = 0;; ++sp) {
                    unsigned m = 0, mu = 0;
                    #pragma unroll
                    for (int i = 0; i < 10; ++i) m = mx4(p[i], m);
                    #pragma unroll
                    for (int i = 0; i < 8; ++i) mu = mx4(u[i], mu);
                    if (all_ok(m > mu ? m : mu) || dead) break;
                    if (sp == 0) nfail += all_ok(mu) ? 1 : 0x10000;
                    if (sp > SPIN_MAX) dead = true;
                    dly(a.tp[4]);
                    asm volatile("" ::: "memory");
                    ldu(); ldp(); }
                ca = (f32x4){0, 0, 0, 0}; cb = ca;
                comb(false); }
            ST(0);
            st32(res, rs, vmem, sb);
            ST(3);
            const unsigned now = rt32();
            LG(0, now); LG(1, G);
            unsigned Sn = G + DLT;
            if ((int)(now + GRD - Sn) > 0) { Sn = (now + GRD + PEN + 3u) & ~3u; ++nlate; }
            st32(nosent(Sn), rs, vstp, sb);
            if (t == NSEG - 2) break;
            bar();
            ldcf(t + 1);
            ST(4);
            if (t == 0) TL(2);
        }
      }
    } else if (role == R_QK || role == R_VO) {
        const int h = id; const bool vo = role == R_VO, st2 = vo && wave >= 4;
        u32x4 W[40];
        if (st2) load_w_32<true>(W, mkrs(a.Wo, D * HE * 2), h * E * D * 2, wave, lane, L);
        else load_w_32<false>(W, mkrs(vo ? a.Wv : wave < 4 ? a.Wq : a.Wk, D * HE * 2), h * 256, wave, lane, L);
        TL(1);
        const unsigned vh = WS_H1 + (tid >> 4) * (D * 2) + (tid & 15) * 16;
        lptr wdst = L + (tid & 15) * 512 + (((tid >> 4) ^ (tid & 7)) * 16);
        lptr vbuf = L + 81920;
        bool allh = false;
        u32x4 hv[5];
        auto waith = [&](int s) {
            if (!allh) for (unsigned sp = 0;; ++sp) { asm volatile("" ::: "memory");
                const u32x2 f = __builtin_amdgcn_raw_buffer_load_b64(rs, WS_FH1 + lane * 8, 0, 16);
                const unsigned long long bad = __builtin_amdgcn_ballot_w64((f.x | f.y) != 0u);
                allh = bad == 0ull;
                if (!((bad >> s) & 1ull) || dead) break;
                if (sp > SPIN_MAX) { dead = true; break; }
                dly(2); }
            asm volatile("" ::: "memory"); };
        waith(0);
        #pragma unroll
        for (int i = 0; i < 5; ++i) hv[i] = ld_sc1(rs, vh + i * 256, 0);
        waith(1);
        #pragma unroll
        for (int i = 0; i < 5; ++i) { lwr(wdst + i * 8192, hv[i]); hv[i] = ld_sc1(rs, vh + i * 256, 32 * D * 2); }
        bar();
        auto stage = [&](int s, int i) { lwr(wdst + ((s + 1) & 1) * 40960 + i * 8192, hv[i]); hv[i] = ld_sc1(rs, vh + i * 256, (s + 2 < NSEG ? s + 2 : NSEG - 1) * (32 * D * 2)); };
        if (!vo) {
            const unsigned vdst = (wave < 4 ? WS_QX : WS_KX) + h * 8192 + (wave & 3) * 2048 + lane * 16;
            for (int s = 0; s < NSEG; ++s) {
                if (s == 1) { ST_RESET; }
                if (s + 2 < NSEG) waith(s + 2);
                ST(0);
                u32x4 o0, o1;
                tile640(W, L + (s & 1) * 40960, lane, o0, o1, [&](int ks) { if (ks % 6 == 2 && ks < 30) stage(s, ks / 6); });
                asm volatile("s_waitcnt vmcnt(5)" ::: "memory");
                st128(o0, rs, vdst, s * 262144); st128(o1, rs, vdst + 1024, s * 262144);
                ST(1);
                const bool now = s < 3 || s == NSEG - 1;
                if (now) drain();
                ST(3);
                bar();
                ST(4);
                if (tid == 0) st32((unsigned)(s == NSEG - 1 ? 1000 : now ? s : s - 1), rs, WS_PQK + h * 128, 0);
                if (s == 0) TL(2);
            }
        } else if (!st2) {
            for (int s = 0; s <= NSEG; ++s) {
                if (s == 1) { ST_RESET; }
                if (s + 2 < NSEG) waith(s + 2);
                ST(0);
                if (s < NSEG) { u32x4 o0, o1;
                    tile640(W, L + (s & 1) * 40960, lane, o0, o1, [&](int ks) { if (ks % 6 == 2 && ks < 30) stage(s, ks / 6); });
                    lwr(vbuf + (s & 1) * 8192 + wave * 2048 + lane * 16, o0); lwr(vbuf + (s & 1) * 8192 + wave * 2048 + 1024 + lane * 16, o1); }
                ST(1);
                bar();
                ST(4);
                const bool now = s < 4 || s == NSEG;
                if (tid == 0 && s > 0) st32((unsigned)(s == NSEG ? 1000 : now ? s - 1 : s - 2), rs, WS_PVO + h * 128, 0);
                if (s == 1) TL(2);
            }
        } else {
            const int r32 = lane & 31, hf = lane >> 5;
            const int prow = (r32 & 19) | ((r32 & 4) << 1) | ((r32 & 8) >> 1);
            lptr asrc = vbuf + hf * 512 + prow * 16;
            __builtin_amdgcn_s_setprio(1);
            const unsigned vdst = WS_CF + ((10 * (wave - 4) + (r32 >> 4)) * 32 + h) * 1024 + (hf * 16 + (r32 & 15)) * 16;
            for (int s = 0; s <= NSEG; ++s) {
                if (s == 1) { ST_RESET; }
                if (s + 2 < NSEG) waith(s + 2);
                ST(0);
                u32x4 af[8];
                #pragma unroll
                for (int ks = 0; ks < 8; ++ks) af[ks] = lrd(asrc + ((s - 1) & 1) * 8192 + ks * 1024);
                const unsigned so = (s > 0 ? s - 1 : 0) * 1310720;
                #pragma unroll
                for (int tt = 0; tt < 5; ++tt) {
                    f32x16 acc;
                    #pragma unroll
                    for (int i = 0; i < 16; ++i) acc[i] = 0;
                    if (s > 0) {
                        #pragma unroll
                        for (int ks = 0; ks < 8; ++ks) acc = mf32(af[ks], W[8 * tt + ks], acc);
                    }
                    stage(s, tt);
                    #pragma unroll
                    for (int p = 0; p < 2; ++p) st128((u32x4){pk2(acc[8 * p], acc[8 * p + 1]), pk2(acc[8 * p + 2], acc[8 * p + 3]), pk2(acc[8 * p + 4], acc[8 * p + 5]), pk2(acc[8 * p + 6], acc[8 * p + 7])}, rs, vdst + p * 512, so + tt * 65536);
                }
                asm volatile("s_waitcnt vmcnt(15)" ::: "memory");
                ST(1);
                if (s < 4 || s == NSEG) drain();
                ST(3);
                bar();
                ST(4);
            }
        }
    } else if (role == R_OUT) {
        const int cg = id >> 1, mt = id & 1;
        const unsigned vcf = WS_CF + (cg * 64 + 4 * wave) * 1024 + lane * 16;
        const unsigned vax = WS_AX + (8 * wave + mt) * 1024 + lane * 16, vam = WS_AM + (2 * wave + mt) * 1024 + lane * 16;
        const unsigned vu = WS_SLOT + SL_U + (cg * 64 + 4 * wave) * 256 + lane * 16;
        lptr part = L + wave * 2304 + (lane & 15) * 144 + (lane >> 4) * 16;
        lptr rsrc = L + (lane >> 2) * 144 + (lane & 3) * 32;
        const unsigned vx = (mt * 16 + (lane >> 2)) * (D * 2) + cg * 64 + (lane & 3) * 16;
        const rsrc_t rx = mkrs(a.x, S * D * 2);
        const u32x4 ga = lrd(L + L_MOD + 2560 + cg * 64 + (lane & 3) * 16);
        int seenv = -1, seenp = -1;
        for (int s = 0; s < NSEG; ++s) {
            u32x4 cf[8], xr;
            if (s == 2) { ST_RESET; }
            wait_prog(rs, WS_PVO + (4 * wave + (lane & 3)) * 128, s, seenv, dead, 4);
            #pragma unroll
            for (int i = 0; i < 8; ++i) cf[i] = ld_sc1(rs, vcf + (i & 3) * 1024, s * 1310720 + (i >> 2) * 32768);
            xr = ld_pl(rx, vx, s * (32 * D * 2));
            if (s > 0 && wave == ((s - 1) & 7)) { asm volatile("s_waitcnt vmcnt(9)" ::: "memory"); if (lane == 0) st32((unsigned)(s - 1), rs, WS_POUT + id * 128, 0); }
            ST(0);
            wait_prog(rs, WS_PP + (4 * wave + (lane & 3)) * 128, s, seenp, dead, 4);
            ST(1);
            u32x4 ax[4], am, u0, u1;
            #pragma unroll
            for (int i = 0; i < 4; ++i) ax[i] = ld_sc1(rs, vax + i * 2048, s * 65536);
            am = ld_sc1(rs, vam, s * 16384);
            const int pv = (int)__builtin_amdgcn_raw_buffer_load_b32(rs, seenp > s ? OOB : WS_PP + (4 * wave + (lane & 3)) * 128, 0, 16);
            for (unsigned sp = 0;; ++sp) { asm volatile("" ::: "memory");
                u0 = ld_sc1(rs, vu, s * SLOT); u1 = ld_sc1(rs, vu, s * SLOT + 8192);
                if (all_ok(mx4(u0, mx4(u1, 0))) || dead) break;
                if (sp > SPIN_MAX) { dead = true; break; }
                dly(2); }
            if (seenp <= s && __builtin_amdgcn_ballot_w64(pv <= s) == 0ull) seenp = s + 1;
            ST(2);
            f32x4 d0 = {0, 0, 0, 0}, d1 = d0;
            #pragma unroll
            for (int i = 0; i < 4; ++i) { d0 = mf16(cf[i], ax[i], d0); d1 = mf16(cf[4 + i], ax[i], d1); }
            d0 = mf16(u0, am, d0); d1 = mf16(u1, am, d1);
            lptr pp = part + (s & 1) * 18432;
            *(LAS f32x4*)pp = d0; *(LAS f32x4*)(pp + 64) = d1;
            ST(3);
            bar();
            ST(4);
            if (wave == (s & 7)) {
                f32x4 r0 = {0, 0, 0, 0}, r1 = r0;
                #pragma unroll
                for (int w = 0; w < 8; ++w) { r0 += *(LAS f32x4*)(rsrc + (s & 1) * 18432 + w * 2304); r1 += *(LAS f32x4*)(rsrc + (s & 1) * 18432 + w * 2304 + 16); }
                u32x4 o;
                o.x = pk2(blo(xr.x) + rnd(blo(ga.x) * rnd(r0[0])), bhi(xr.x) + rnd(bhi(ga.x) * rnd(r0[1])));
                o.y = pk2(blo(xr.y) + rnd(blo(ga.y) * rnd(r0[2])), bhi(xr.y) + rnd(bhi(ga.y) * rnd(r0[3])));
                o.z = pk2(blo(xr.z) + rnd(blo(ga.z) * rnd(r1[0])), bhi(xr.z) + rnd(bhi(ga.z) * rnd(r1[1])));
                o.w = pk2(blo(xr.w) + rnd(blo(ga.w) * rnd(r1[2])), bhi(xr.w) + rnd(bhi(ga.w) * rnd(r1[3])));
                st128(o, rs, WS_X1 + vx, s * (32 * D * 2));
                float s1 = 0, s2 = 0;
                #pragma unroll
                for (int j = 0; j < 4; ++j) { s1 = dot2(o[j], 0x3F803F80u, s1); s2 = dot2(o[j], o[j], s2); }
                s1 += qp<0xB1>(s1); s2 += qp<0xB1>(s2); s1 += qp<0x4E>(s1); s2 += qp<0x4E>(s2);
                st64((u32x2){__float_as_uint(s1), __float_as_uint(s2)}, rs, (lane & 3) ? OOB : WS_LS + mt * 4096 + cg * 128 + (lane >> 2) * 8, s * 8192);
                if (s == NSEG - 1) { drain(); if (lane == 0) st32((unsigned)s, rs, WS_POUT + id * 128, 0); }
            }
            ST(5);
            if (s == 0) TL(2);
        }
    } else if (role == R_F1) {
        u32x4 W[2][20];
        f32x4 cs[2], bs[2];
        const int kg = lane >> 4, t16 = lane & 15, cw = wave & 3, th = wave >> 2;
        #pragma unroll
        for (int mat = 0; mat < 2; ++mat) {
            const rsrc_t rg = mkrs(mat ? a.wu : a.wg, D * F * 2);
            const unsigned vw = lane * (F * 2) + (id * 64 + cw * 16) * 2;
            u32x4 g[10], u[10];
            #pragma unroll
            for (int i = 0; i < 10; ++i) { g[i] = ld_pl(rg, vw, i * 64 * F * 2); u[i] = ld_pl(rg, vw + 16, i * 64 * F * 2); }
            SB();
            lptr wp = stg + lane * 32, rp = stg + (8 * kg + ((lane >> 2) & 3)) * 32 + (lane & 3) * 8;
            const u32x4 ones = {0x3F803F80u, 0x3F803F80u, 0x3F803F80u, 0x3F803F80u};
            cs[mat] = (f32x4){0, 0, 0, 0}; bs[mat] = cs[mat];
            #pragma unroll
            for (int i = 0; i < 10; ++i) {
                lwr(wp, g[i]); lwr(wp + 16, u[i]);
                const float sc = rnd(1.f + b2f(*(LAS u16*)(L + L_MOD + 5120 + (64 * i + lane) * 2)));
                u32x4 gs, us;
                #pragma unroll
                for (int j = 0; j < 4; ++j) { gs[j] = pk2(blo(g[i][j]) * sc, bhi(g[i][j]) * sc); us[j] = pk2(blo(u[i][j]) * sc, bhi(u[i][j]) * sc); }
                lwr(wp + 2048, gs); lwr(wp + 2048 + 16, us);
                #pragma unroll
                for (int k2 = 0; k2 < 2; ++k2) {
                    const u32x2 a0 = trd(rp + k2 * 1024), a1 = trd(rp + k2 * 1024 + 128), b0 = trd(rp + 2048 + k2 * 1024), b1 = trd(rp + 2048 + k2 * 1024 + 128);
                    bs[mat] = mf16((u32x4){a0.x, a0.y, a1.x, a1.y}, lrd(L + L_MOD + 3840 + (64 * i + 32 * k2 + 8 * kg) * 2), bs[mat]);
                    W[mat][2 * i + k2] = (u32x4){b0.x, b0.y, b1.x, b1.y};
                    cs[mat] = mf16(W[mat][2 * i + k2], ones, cs[mat]); }
            }
            SB(); }
        TL(2);
        unsigned vh[5];
        #pragma unroll
        for (int i = 0; i < 5; ++i) { const int q = (wave * 5 + i) * 64 + lane, tok = q / 80; vh[i] = WS_X1 + ((q - tok * 80) ^ (tok & 15)) * 16 + tok * (D * 2); }
        lptr bb[4];
        #pragma unroll
        for (int m = 0; m < 4; ++m) bb[m] = L + (th * 16 + t16) * 1280 + (((4 * m + kg) ^ t16) * 16);
        lptr stat = L + 81920 + th * 4096 + kg * 640 + t16 * 8;
        const unsigned vact = WS_ACT + (th * 54 + 2 * id + (cw >> 1)) * 1024 + ((2 * (cw & 1) + (kg >> 1)) * 16 + t16) * 16 + (kg & 1) * 8;
        int seen = -1;
        const unsigned vpo = WS_POUT + (2 * (lane < 20 ? lane : 0) + th) * 128;
        auto ldh = [&](int s) {
            #pragma unroll
            for (int i = 0; i < 5; ++i) __builtin_amdgcn_raw_ptr_buffer_load_lds(rs, (LAS void*)(L + (s & 1) * 40960 + (wave * 5 + i) * 1024), 16, vh[i], s * (32 * D * 2), 0, 16);
            __builtin_amdgcn_raw_ptr_buffer_load_lds(rs, (LAS void*)(L + 81920 + (s & 1) * 8192 + wave * 1024), 16, WS_LS + tid * 16, s * 8192, 0, 16); };
        bar();
        wait_prog(rs, vpo, 0, seen, dead, 8);
        ldh(0);
        for (int s = 0; s < NSEG; ++s) {
            if (s == 2) { ST_RESET; }
            drain();
            ST(0);
            bar();
            ST(1);
            if (tid == 0 && s > 0) st32((unsigned)(s - 1), rs, WS_PF1 + id * 128, 0);
            bool got = s + 1 >= NSEG;
            int pv = (int)__builtin_amdgcn_raw_buffer_load_b32(rs, seen > s + 1 ? OOB : vpo, 0, 16);
            if (seen > s && !got) { ldh(s + 1); got = true; }
            SB();
            const int bo = (s & 1) * 40960;
            f32x2 sp[5];
            #pragma unroll
            for (int i = 0; i < 5; ++i) sp[i] = *(LAS f32x2*)(stat + (s & 1) * 8192 + i * 128);
            u32x4 f[4];
            #pragma unroll
            for (int m = 0; m < 4; ++m) f[m] = lrd(bb[m] + bo);
            SB();
            f32x4 d0 = {0, 0, 0, 0}, d1 = d0;
            float mean, rstd;
            #pragma unroll
            for (int ks = 0; ks < 20; ++ks) {
                d0 = mf16(W[0][ks], f[ks & 3], d0); d1 = mf16(W[1][ks], f[ks & 3], d1);
                if (ks + 4 < 20) f[ks & 3] = lrd(bb[ks & 3] + bo + ((ks + 4) >> 2) * 256);
                if (ks == 0) { const f32x2 t2 = ((sp[0] + sp[1]) + (sp[2] + sp[3])) + sp[4];
                    const float s1 = x32sum(x16sum(t2[0])) * (1.f / D), s2 = x32sum(x16sum(t2[1])) * (1.f / D);
                    const float var = __builtin_fmaxf(s2 - s1 * s1, 0.f);
                    mean = rnd(s1); rstd = 1.f / rnd(__builtin_sqrtf(rnd(rnd(var) + 1e-5f))); }
                else if (ks >= 12) SB(); }
            asm volatile("" : "+v"(d0), "+v"(d1), "+v"(pv));
            ST(2);
            float act[4];
            #pragma unroll
            for (int i = 0; i < 4; ++i) {
                const float g = rnd((d0[i] - mean * cs[0][i]) * rstd + bs[0][i]), u = rnd((d1[i] - mean * cs[1][i]) * rstd + bs[1][i]);
                act[i] = rnd(g * __builtin_amdgcn_rcpf(1.f + __expf(-g))) * u; }
            if (seen <= s + 1 && __builtin_amdgcn_ballot_w64(pv <= s) == 0ull) seen = __builtin_amdgcn_ballot_w64(pv < s + 4) == 0ull ? s + 4 : __builtin_amdgcn_ballot_w64(pv < s + 2) == 0ull ? s + 2 : s + 1;
            asm volatile("" ::: "memory");
            st64((u32x2){pk2(act[0], act[1]), pk2(act[2], act[3])}, rs, vact, s * (108 * 1024));
            ST(4);
            if (!got) { wait_prog(rs, vpo, s + 1, seen, dead, 2); ldh(s + 1); }
            ST(5);
            if (s == 0) TL(3);
        }
        drain(); bar();
        if (tid == 0) st32((unsigned)(NSEG - 1), rs, WS_PF1 + id * 128, 0);
    } else if (role == R_F2) {
        const int cg = id >> 1, mt = id & 1;
        const int ks0 = wave < 6 ? 7 * wave : 42 + 6 * (wave - 6);
        u32x4 W[28];
        {   const rsrc_t rw = mkrs(a.wd, F * D * 2);
            const unsigned vw = (32 * ks0 + 8 * (lane >> 4) + ((lane >> 2) & 3)) * (D * 2) + cg * 128 + (lane & 3) * 16;
            u32x4 v[28];
            #pragma unroll
            for (int i = 0; i < 28; ++i) v[i] = ld_pl(rw, vw, (i >= 24 && wave >= 6) ? 0 : (32 * (i >> 2) + 4 * (i & 1)) * (D * 2) + ((i >> 1) & 1) * 64);
            __builtin_amdgcn_sched_barrier(0);
            lptr wp = stg + lane * 16, rp = stg + (lane >> 2) * 64 + (lane & 3) * 8;
            #pragma unroll
            for (int i = 0; i < 28; ++i) { const int pi = i & 3;
                if (i >= 24 && wave >= 6) v[i] = (u32x4){0, 0, 0, 0};
                lwr(wp + pi * 1024, v[i]);
                const u32x2 r0 = trd(rp + pi * 1024), r1 = trd(rp + pi * 1024 + 32);
                const int i0 = (i & ~3) + (pi & 2);
                W[i0][2 * (pi & 1)] = r0.x; W[i0][2 * (pi & 1) + 1] = r0.y; W[i0 + 1][2 * (pi & 1)] = r1.x; W[i0 + 1][2 * (pi & 1) + 1] = r1.y; } }
        TL(2);
        bar();
        const unsigned vact = WS_ACT + (mt * 54 + ks0) * 1024 + lane * 16;
        const int pf = ((ks0 >> 1) + (lane & 3)) < 27 ? (ks0 >> 1) + (lane & 3) : 26;
        lptr part = L + wave * 4352 + (lane & 15) * 272 + (lane >> 4) * 16;
        lptr rsrc = L + (tid >> 5) * 272 + (tid & 31) * 8;
        const unsigned vx = (mt * 16 + (tid >> 5)) * (D * 2) + cg * 128 + (tid & 31) * 4;
        const unsigned gb = *(LAS unsigned*)(L + L_MOD + 6400 + cg * 128 + (tid & 31) * 4);
        const rsrc_t ro = mkrs(a.out, S * D * 2);
        int seen = -1;
        for (int s = 0; s < NSEG; ++s) {
            wait_prog(rs, WS_PF1 + pf * 128, s, seen, dead, 2);
            u32x4 af[7];
            #pragma unroll
            for (int i = 0; i < 7; ++i) af[i] = ld_sc1(rs, vact + ((i == 6 && wave >= 6) ? 0 : i * 1024), s * (108 * 1024));
            const unsigned xr = __builtin_amdgcn_raw_buffer_load_b32(rs, WS_X1 + vx, s * (32 * D * 2), 16);
            f32x4 d[4];
            #pragma unroll
            for (int j = 0; j < 4; ++j) d[j] = (f32x4){0, 0, 0, 0};
            #pragma unroll
            for (int i = 0; i < 7; ++i)
                #pragma unroll
                for (int j = 0; j < 4; ++j) d[j] = mf16(W[4 * i + j], af[i], d[j]);
            #pragma unroll
            for (int j = 0; j < 4; ++j) *(LAS f32x4*)(part + (s & 1) * 34816 + j * 64) = d[j];
            bar();
            f32x2 r = {0, 0};
            #pragma unroll
            for (int w = 0; w < 8; ++w) r += *(LAS f32x2*)(rsrc + (s & 1) * 34816 + w * 4352);
            __builtin_amdgcn_raw_buffer_store_b32(pk2(blo(xr) + rnd(blo(gb) * rnd(r[0])), bhi(xr) + rnd(bhi(gb) * rnd(r[1]))), ro, vx, s * (32 * D * 2), 0);
            if (s == 0) TL(3);
        }
    }
    TL(7);
    ST_OUT;
}

extern "C" void kernel_launch(void* const* d_in, const int* in_sizes, int n_in, void* d_out, int out_size, void* d_ws, size_t ws_size, hipStream_t stream) {
    Args a{(const u16*)d_in[0], (const u16*)d_in[1], (const u16*)d_in[2], (const u16*)d_in[3], (const u16*)d_in[4], (const u16*)d_in[5],
           (const u16*)d_in[8], (const u16*)d_in[9], (const u16*)d_in[10], (const u16*)d_in[11], (const u16*)d_in[12], (u16*)d_out, (unsigned char*)d_ws, {216, 6, 116, 0, 0, 10, 150, 0, 20, 0, 0, 0}};
    hipMemsetAsync(d_ws, 0xFF, WS_CLR_END, stream);
    hipFuncSetAttribute((const void*)mega, hipFuncAttributeMaxDynamicSharedMemorySize, L_TOT);
    mega<<<256, 512, L_TOT, stream>>>(a);
}
```

```cpp
#include <hip/hip_runtime.h>
#include <hip/hip_bf16.h>

typedef unsigned short u16;
typedef __bf16 bf16x8 __attribute__((ext_vector_type(8)));
typedef __bf16 bf16x16 __attribute__((ext_vector_type(16)));
typedef __bf16 bf16x2 __attribute__((ext_vector_type(2)));
typedef short s16x4 __attribute__((ext_vector_type(4)));
typedef float f32x2 __attribute__((ext_vector_type(2)));
typedef float f32x4 __attribute__((ext_vector_type(4)));
typedef float f32x16 __attribute__((ext_vector_type(16)));
typedef unsigned u32x2 __attribute__((ext_vector_type(2)));
typedef unsigned u32x4 __attribute__((ext_vector_type(4)));
typedef unsigned u32x8 __attribute__((ext_vector_type(8)));
typedef __amdgpu_buffer_rsrc_t rsrc_t;
#define LAS __attribute__((address_space(3)))
typedef LAS unsigned char* lptr;

constexpr int S = 2048, D = 640, H = 32, E = 128, HE = 4096, NSEG = 64, F = 1728;
constexpr unsigned WS_FH1 = 0;
constexpr unsigned WS_PQK = 1024;
constexpr unsigned WS_PVO = WS_PQK + 32 * 128;
constexpr unsigned WS_PP  = WS_PVO + 32 * 128;
constexpr unsigned WS_POUT = WS_PP + 32 * 128;
constexpr unsigned WS_PF1 = WS_POUT + 40 * 128;
constexpr unsigned WS_POL = 22528;
constexpr unsigned WS_MOD = 24576;
constexpr unsigned WS_CLR_END = 32768;
constexpr unsigned WS_SLOT = 2u << 20, SLOT = 2u << 20, CH = 16384;
constexpr unsigned SL_PX = 0, SL_PM = 2 * CH, SL_STP = 2 * CH + 4096, SL_MEM = 3 * CH, SL_U = 5 * CH, SL_END = 45 * CH, SL_CAL = 126 * CH;
#define CFN 0
constexpr unsigned WS_CF = WS_SLOT + (CFN ? 45 * CH : 0), WS_KQ = WS_SLOT + 80 * CH, WS_ACT = WS_SLOT + 112 * CH;
constexpr unsigned WS_H1  = WS_SLOT + 66 * SLOT;
constexpr unsigned WS_AX  = WS_H1 + S * D * 2;
constexpr unsigned WS_AM  = WS_AX + 64 * 32 * 2048;
constexpr unsigned WS_X1  = WS_AM + 64 * 8 * 2048;
constexpr unsigned WS_LS  = WS_X1 + S * D * 2;
constexpr unsigned WS_DBG = WS_LS + 64 * 8192;
constexpr unsigned WS_END = WS_DBG + (2 << 20);

__device__ __forceinline__ float b2f(u16 b) { return __uint_as_float((unsigned)b << 16); }
__device__ __forceinline__ float blo(unsigned x) { return __uint_as_float(x << 16); }
__device__ __forceinline__ float bhi(unsigned x) { return __uint_as_float(x & 0xffff0000u); }
__device__ __forceinline__ unsigned pk2(float lo, float hi) { bf16x2 v; v[0] = (__bf16)lo; v[1] = (__bf16)hi; return __builtin_bit_cast(unsigned, v); }
__device__ __forceinline__ float rnd(float f) { return blo(pk2(f, 0.f)); }
__device__ __forceinline__ unsigned nosent(unsigned x) { return x < 0xFFFFFFFEu ? x : 0xFFFFFFFEu; }

struct Args { const u16 *x, *mem0, *Wq, *Wk, *Wv, *Wo, *wg, *wu, *wd, *c, *wm; u16* out; unsigned char* ws; int tp[16]; };
__device__ __forceinline__ void dly(int n) { for (int i = 0; i < n; ++i) __builtin_amdgcn_s_sleep(1); }
__device__ __forceinline__ unsigned long long rtime() { unsigned long long t; asm volatile("s_memrealtime %0\n\ts_waitcnt lgkmcnt(0)" : "=s"(t) :: "memory"); return t; }
constexpr unsigned SPIN_MAX = 200000u, OOB = 0x7FFFF000u;
#define TL(i)
#define ST(i)
#define ST_INIT
#define ST_RESET
#define ST_OUT
#define ST_T(t)
#define LG(k, val)
__device__ __forceinline__ rsrc_t mkrs(const void* p, unsigned bytes) { return __builtin_amdgcn_make_buffer_rsrc((void*)p, 0, (int)bytes, 0x00020000); }
__device__ __forceinline__ u32x4 ld_sc1(rsrc_t r, unsigned voff, unsigned soff) { return __builtin_amdgcn_raw_buffer_load_b128(r, voff, soff, 16); }
__device__ __forceinline__ u32x4 ld_pl(rsrc_t r, unsigned voff, unsigned soff) { return __builtin_amdgcn_raw_buffer_load_b128(r, voff, soff, 0); }
__device__ __forceinline__ void st128(u32x4 v, rsrc_t r, unsigned voff, unsigned soff) { __builtin_amdgcn_raw_buffer_store_b128(v, r, voff, soff, 16); asm volatile("s_nop 1" :: "v"(v)); }
__device__ __forceinline__ void st64(u32x2 v, rsrc_t r, unsigned voff, unsigned soff) { __builtin_amdgcn_raw_buffer_store_b64(v, r, voff, soff, 16); }
__device__ __forceinline__ void st32(unsigned v, rsrc_t r, unsigned voff, unsigned soff) { __builtin_amdgcn_raw_buffer_store_b32(v, r, voff, soff, 16); }
__device__ __forceinline__ unsigned umax3(unsigned a, unsigned b, unsigned c) { unsigned m = a > b ? a : b; return m > c ? m : c; }
__device__ __forceinline__ unsigned mx4(u32x4 v, unsigned m) { return umax3(umax3(v.x, v.y, m), v.z, v.w); }
__device__ __forceinline__ bool all_ok(unsigned m) { return __builtin_amdgcn_ballot_w64(m == 0xFFFFFFFFu) == 0ull; }
__device__ __forceinline__ f32x4 smf(u32x4 a, u32x8 b, f32x4 c, int idx) { return __builtin_amdgcn_smfmac_f32_16x16x64_bf16(__builtin_bit_cast(bf16x8, a), __builtin_bit_cast(bf16x16, b), c, idx, 0, 0); }
__device__ __forceinline__ f32x4 mf16(u32x4 a, u32x4 b, f32x4 c) { return __builtin_amdgcn_mfma_f32_16x16x32_bf16(__builtin_bit_cast(bf16x8, a), __builtin_bit_cast(bf16x8, b), c, 0, 0, 0); }
__device__ __forceinline__ f32x16 mf32(u32x4 a, u32x4 b, f32x16 c) { return __builtin_amdgcn_mfma_f32_32x32x16_bf16(__builtin_bit_cast(bf16x8, a), __builtin_bit_cast(bf16x8, b), c, 0, 0, 0); }
__device__ __forceinline__ float fmx(float a, float b) { return __builtin_elementwise_maximum(a, b); }
__device__ __forceinline__ float fold32(float a, float b) { auto r = __builtin_amdgcn_permlane32_swap(__float_as_uint(a), __float_as_uint(b), false, false); return __uint_as_float(r[0]) + __uint_as_float(r[1]); }
__device__ __forceinline__ float x32max(float a) { auto r = __builtin_amdgcn_permlane32_swap(__float_as_uint(a), __float_as_uint(a), false, false); return fmx(__uint_as_float(r[0]), __uint_as_float(r[1])); }
__device__ __forceinline__ float x32sum(float a) { return fold32(a, a); }
__device__ __forceinline__ float x16max(float a) { auto r = __builtin_amdgcn_permlane16_swap(__float_as_uint(a), __float_as_uint(a), false, false); return fmx(__uint_as_float(r[0]), __uint_as_float(r[1])); }
__device__ __forceinline__ float x16sum(float a) { auto r = __builtin_amdgcn_permlane16_swap(__float_as_uint(a), __float_as_uint(a), false, false); return __uint_as_float(r[0]) + __uint_as_float(r[1]); }
template <int N> __device__ __forceinline__ float ror(float a) { return __int_as_float(__builtin_amdgcn_update_dpp(0, __float_as_int(a), 0x120 + N, 0xf, 0xf, true)); }
template <int C> __device__ __forceinline__ float qp(float a) { return __int_as_float(__builtin_amdgcn_update_dpp(0, __float_as_int(a), C, 0xf, 0xf, true)); }
__device__ __forceinline__ float sum16(float a) { a += ror<8>(a); a += ror<4>(a); a += ror<2>(a); a += ror<1>(a); return a; }
__device__ __forceinline__ void bar() { asm volatile("s_waitcnt lgkmcnt(0)\n\ts_barrier" ::: "memory"); __builtin_amdgcn_s_waitcnt(0xC07F); }
__device__ __forceinline__ void drain() { asm volatile("s_waitcnt vmcnt(0)" ::: "memory"); }
__device__ __forceinline__ u32x2 trd(lptr p) { return __builtin_bit_cast(u32x2, __builtin_amdgcn_ds_read_tr16_b64_v4i16((LAS s16x4*)p)); }
__device__ __forceinline__ u32x4 lrd(lptr p) { return *(LAS u32x4*)p; }
__device__ __forceinline__ void lwr(lptr p, u32x4 v) { *(LAS u32x4*)p = v; }
__device__ __forceinline__ float dot2(unsigned a, unsigned b, float c) { return __builtin_amdgcn_fdot2_f32_bf16(__builtin_bit_cast(bf16x2, a), __builtin_bit_cast(bf16x2, b), c, false); }

__device__ __forceinline__ void peek_prog(rsrc_t rs, unsigned voff, int target, int& seen) {
    asm volatile("" ::: "memory");
    const int v = (int)__builtin_amdgcn_raw_buffer_load_b32(rs, voff, 0, 16);
    if (__builtin_amdgcn_ballot_w64(v < target) == 0ull) seen = __builtin_amdgcn_ballot_w64(v < target + 64) == 0ull ? target + 64 : __builtin_amdgcn_ballot_w64(v < target + 6) == 0ull ? target + 6 : __builtin_amdgcn_ballot_w64(v < target + 2) == 0ull ? target + 2 : target;
    asm volatile("" ::: "memory");
}
__device__ __forceinline__ void wait_prog(rsrc_t rs, unsigned voff, int target, int& seen, bool& dead, int slp) {
    if (seen >= target) return;
    for (unsigned sp = 0;; ++sp) {
        peek_prog(rs, voff, target, seen);
        if (seen >= target || dead) break;
        if (sp > SPIN_MAX) { dead = true; break; }
        dly(slp);
    }
}

constexpr int L_A = 0;
constexpr int L_QK = 10240;
constexpr int L_KQX = 14336;
constexpr int L_STG = 49152;
constexpr int L_MOD = 98304;
constexpr int L_TOT = 107520;

#define WAUX 2
template <int KIND> __device__ __forceinline__ unsigned wsoff(int r, int j) {
    const int i = 5 * r + j;
    return KIND == 0 ? (64 * (i >> 2) + 32 * ((i & 3) >> 1) + 2 * (i & 1)) * (HE * 2) : KIND == 1 ? (64 * (r >> 2) + 32 * ((r & 3) >> 1) + (r & 1)) * (D * 2) + j * 256
         : KIND == 2 ? 16 * i * (HE * 2) : (32 * (r >> 1) + 8 * (r & 1)) * (D * 2) + j * 256; }
template <int KIND> __device__ __forceinline__ void wissue(u32x4 (&v)[40], rsrc_t rw, unsigned base, int wq, int lane) {
    const int q = lane >> 4, jj = 4 * (wq & 1) + q;
    const int row = KIND == 0 ? 16 * (wq >> 1) + 8 * (q >> 1) + 4 * (wq & 1) + (q & 1) : KIND == 1 ? 4 * (q >> 1) + 8 * (wq >> 1) + 2 * (wq & 1) + 16 * (q & 1)
                  : KIND == 2 ? 4 * wq + q : 4 * (wq >> 1) + (jj >> 1) + 16 * (jj & 1);
    const unsigned voff = row * ((KIND & 1) ? D * 2 : HE * 2) + base + (lane & 15) * 16;
    #pragma unroll
    for (int i = 0; i < 40; ++i) v[i] = __builtin_amdgcn_raw_buffer_load_b128(rw, voff, wsoff<KIND>(i / 5, i % 5), WAUX);
    __builtin_amdgcn_sched_barrier(0);
}
template <bool WO>
__device__ __forceinline__ void load_w_smf(u32x8 (&W)[20], rsrc_t rw, unsigned base, int wave, int lane, lptr L) {
    const int wq = wave & 3;
    u32x4 v[40];
    wissue<WO ? 1 : 0>(v, rw, base, wq, lane);
    lptr wp = L + (wave >> 2) * 20480 + ((lane >> 2) & 3) * 1024 + (4 * wq + (lane >> 4)) * 64 + (lane & 3) * 16;
    lptr rp = L + (wave >> 2) * 20480 + (WO ? 5 * wq : wq) * 1024 + (lane >> 2) * 64 + (lane & 3) * 8;
    #pragma unroll
    for (int r = 0; r < 8; ++r) {
        #pragma unroll
        for (int j = 0; j < 5; ++j) lwr(wp + (r & 1) * 40960 + j * 4096, v[5 * r + j]);
        bar();
        #pragma unroll
        for (int j = 0; j < 5; ++j) { const int i = 5 * r + j, pi = WO ? r & 3 : i & 3;
            lptr p = rp + (r & 1) * 40960 + j * (WO ? 1024 : 4096);
            const u32x2 r0 = trd(p), r1 = trd(p + 32);
            const int i0 = WO ? 4 * j + (r >> 2) : 2 * (i >> 2), i1 = WO ? i0 + 2 : i0 + 1;
            W[i0][2 * pi] = r0.x; W[i0][2 * pi + 1] = r0.y; W[i1][2 * pi] = r1.x; W[i1][2 * pi + 1] = r1.y; }
    }
    bar();
}
template <bool WO>
__device__ __forceinline__ void load_w_32(u32x4 (&W)[40], rsrc_t rw, unsigned base, int wave, int lane, lptr L) {
    const int wq = wave & 3, G = lane >> 4;
    u32x4 v[40];
    wissue<WO ? 3 : 2>(v, rw, base, wq, lane);
    lptr wp = L + (wave >> 2) * 20480 + ((lane >> 2) & 3) * 1024 + (4 * wq + (lane >> 4)) * 64 + (lane & 3) * 16;
    lptr rp = L + (wave >> 2) * 20480 + (WO ? 5 * wq : wq) * 1024 + (8 * (G >> 1) + ((lane >> 2) & 3)) * 64 + (G & 1) * 32 + (lane & 3) * 8;
    #pragma unroll
    for (int r = 0; r < 8; ++r) {
        #pragma unroll
        for (int j = 0; j < 5; ++j) lwr(wp + (r & 1) * 40960 + j * 4096, v[5 * r + j]);
        bar();
        #pragma unroll
        for (int j = 0; j < 5; ++j) { lptr p = rp + (r & 1) * 40960 + j * (WO ? 1024 : 4096);
            const u32x2 r0 = trd(p), r1 = trd(p + 256);
            W[WO ? 8 * j + r : 5 * r + j] = (u32x4){r0.x, r0.y, r1.x, r1.y}; }
    }
    bar();
}

struct Pc { unsigned g1, g2; int p1, p2; };
__device__ __forceinline__ Pc mkpc(int p1, int p2, int lane) { Pc r; r.p1 = p1; r.p2 = p2; r.g1 = WS_SLOT + SL_MEM + (p1 >> 3) * CH + (p1 & 7) * 1024 + lane * 16; r.g2 = WS_SLOT + SL_MEM + (p2 >> 3) * CH + (p2 & 7) * 1024 + lane * 16; return r; }
template <int NP>
__device__ __forceinline__ void gather_mem(const Args& a, rsrc_t rs, unsigned so, bool fresh, const Pc& pc, lptr A, bool& dead) {
    if (NP == 0) return;
    u32x4 v0 = {~0u, ~0u, ~0u, ~0u}, v1 = {0, 0, 0, 0};
    if (!fresh) { v0 = lrd(A + pc.p1 * 1024); if (NP == 2) v1 = lrd(A + pc.p2 * 1024); }
    for (unsigned sp = 0;; ++sp) {
        unsigned m = mx4(v0, 0); if (NP == 2) m = mx4(v1, m);
        if (all_ok(m) || dead) break;
        if (sp > SPIN_MAX) { dead = true; break; }
        if (sp) dly(a.tp[3]);
        asm volatile("" ::: "memory");
        if (m == 0xFFFFFFFFu) { v0 = ld_sc1(rs, pc.g1, so); if (NP == 2) v1 = ld_sc1(rs, pc.g2, so); }
    }
    lwr(A + pc.p1 * 1024, v0);
    if (NP == 2) lwr(A + pc.p2 * 1024, v1);
}
__device__ __forceinline__ void first_mem(const Args& a, int wave, int l, lptr A) {
    const u16* s = a.mem0 + (2 * ((l >> 2) & 3) + (l & 1)) * D + 16 * (l >> 4) + 8 * ((l >> 1) & 1);
    lwr(A + wave * 1024, *(const u32x4*)(s + 64 * wave));
    if (wave < 2) lwr(A + (wave + 8) * 1024, *(const u32x4*)(s + 64 * (wave + 8)));
}
__device__ __forceinline__ unsigned long long get_pol(rsrc_t rs, int lane, bool& dead) {
    unsigned v;
    for (unsigned sp = 0;; ++sp) { asm volatile("" ::: "memory");
        v = __builtin_amdgcn_raw_buffer_load_b32(rs, WS_POL + lane * 4, 0, 16);
        if (all_ok(v) || dead) break;
        if (sp > SPIN_MAX) { dead = true; break; }
        dly(8); }
    asm volatile("" ::: "memory");
    return __builtin_amdgcn_ballot_w64(v == 1u);
}
#define SO(t) (((unsigned)(t) << 21) + (((unsigned)(pol >> ((t) & 63)) & 1u) << 13))
#define FO(p) (SO(p) ^ 8192u)
#define RO(s, odd) FO(2 * ((s) & 31) + (odd))
#define CO(s, odd) (RO(s, odd) ^ (CFN ? 8192u : 0u))
__device__ __forceinline__ unsigned rt32() { return (unsigned)__builtin_amdgcn_s_memrealtime(); }
__device__ __forceinline__ void gate(unsigned target) {
    asm volatile("1:\n\ts_memrealtime vcc\n\ts_waitcnt lgkmcnt(0)\n\ts_sub_i32 vcc_lo, %0, vcc_lo\n\ts_cmp_gt_i32 vcc_lo, 0\n\ts_cbranch_scc1 1b" :: "s"(target) : "vcc", "scc", "memory"); }
template <int N> __device__ __forceinline__ int rori(int a) { return __builtin_amdgcn_update_dpp(0, a, 0x120 + N, 0xf, 0xf, true); }
__device__ __forceinline__ int min8(int d) { d = min(d, rori<4>(d)); d = min(d, rori<2>(d)); d = min(d, rori<1>(d)); return __builtin_amdgcn_readfirstlane(d); }
__device__ __forceinline__ void wait_min(rsrc_t rs, unsigned voff, int target, int& seen, bool& dead) {
    for (unsigned sp = 0; seen < target && !dead; ++sp) { asm volatile("" ::: "memory");
        int d = (int)__builtin_amdgcn_raw_buffer_load_b32(rs, voff, 0, 16);
        d = min(d, rori<8>(d)); d = min(d, rori<4>(d)); d = min(d, rori<2>(d)); d = min(d, rori<1>(d));
        { auto r = __builtin_amdgcn_permlane16_swap(d, d, false, false); d = min((int)r[0], (int)r[1]); }
        { auto r = __builtin_amdgcn_permlane32_swap(d, d, false, false); d = min((int)r[0], (int)r[1]); }
        seen = __builtin_amdgcn_readfirstlane(d);
        if (sp > SPIN_MAX) dead = true;
        if (seen < target) dly(8); }
    asm volatile("" ::: "memory");
}
__device__ __forceinline__ unsigned read_G(rsrc_t rs, unsigned so, int lane, bool& dead) {
    u32x2 v;
    for (unsigned sp = 0;; ++sp) { asm volatile("" ::: "memory");
        v = __builtin_amdgcn_raw_buffer_load_b64(rs, WS_SLOT + SL_STP + (lane < 40 ? lane : 0) * 32, so, 16);
        if (all_ok(v.x > v.y ? v.x : v.y) || dead) break;
        if (sp > SPIN_MAX) { dead = true; break; }
        dly(2); }
    const unsigned ref = rt32();
    int d = max((int)(v.x - ref), (int)(v.y - ref));
    d = max(d, rori<8>(d)); d = max(d, rori<4>(d)); d = max(d, rori<2>(d)); d = max(d, rori<1>(d));
    { auto r = __builtin_amdgcn_permlane16_swap(d, d, false, false); d = max((int)r[0], (int)r[1]); }
    { auto r = __builtin_amdgcn_permlane32_swap(d, d, false, false); d = max((int)r[0], (int)r[1]); }
    d = min(max(d, -100000), 600);
    return ref + (unsigned)__builtin_amdgcn_readfirstlane(d);
}
constexpr int L_FAIL = 63488;
constexpr int L_DUM = 63744;
#define DG(x)
__device__ __forceinline__ void dma1(rsrc_t rs, lptr d, unsigned vo, unsigned so) { __builtin_amdgcn_raw_ptr_buffer_load_lds(rs, (LAS void*)d, 16, vo, so, 0, 16); }
__device__ __forceinline__ void dma4(rsrc_t rs, lptr d, unsigned vo, unsigned so) {
    __builtin_amdgcn_raw_ptr_buffer_load_lds(rs, (LAS void*)d, 16, vo, so, 0, 16); __builtin_amdgcn_raw_ptr_buffer_load_lds(rs, (LAS void*)d, 16, vo, so, 1024, 16);
    __builtin_amdgcn_raw_ptr_buffer_load_lds(rs, (LAS void*)d, 16, vo, so, 2048, 16); __builtin_amdgcn_raw_ptr_buffer_load_lds(rs, (LAS void*)d, 16, vo, so, 3072, 16); }
template <int NP>
__device__ __forceinline__ void dma_mem(rsrc_t rs, unsigned so, const Pc& pc, lptr L) {
    if (NP > 0) __builtin_amdgcn_raw_ptr_buffer_load_lds(rs, (LAS void*)(L + L_A + pc.p1 * 1024), 16, pc.g1, so, 0, 16);
    if (NP > 1) __builtin_amdgcn_raw_ptr_buffer_load_lds(rs, (LAS void*)(L + L_A + pc.p2 * 1024), 16, pc.g2, so, 0, 16);
}
#define FETCH(NP, GATE) do { const unsigned so_ = SO(t > 0 ? t - 1 : 0); \
    if (__builtin_expect(!again && gated && t > 1, 1)) { tagc = t; GATE; ST(0); bar(); ST(5); dma_mem<NP>(rs, so_, pc, L); } \
    else { if (t > 0) gather_mem<NP>(a, rs, so_, !again, pc, Asrc, dead); tagc = -5; } } while (0)
__device__ __forceinline__ void flag(unsigned m, int t, int lane, lptr L) { *(LAS int*)(L + (m == 0xFFFFFFFFu ? L_FAIL : L_DUM + lane * 4)) = t; }
template <int NP>
__device__ __forceinline__ void check_own(int t, int lane, const Pc& pc, lptr A, lptr L) {
    unsigned m = mx4(lrd(A + pc.p1 * 1024), 0);
    if (NP == 2) m = mx4(lrd(A + pc.p2 * 1024), m);
    flag(m, t, lane, L);
}
__device__ __forceinline__ bool failed(int t, lptr L) { return __builtin_expect(__builtin_amdgcn_readfirstlane(*(LAS int*)(L + L_FAIL)) == t, 0); }
#define SB() __builtin_amdgcn_sched_barrier(0)
#define UBAR 3
#define NCAL 4
#define NB1 8
#define NB2 0
#define PH1 2
struct Nop { __device__ void operator()() const {} };
template <int KIND, class FN = Nop>
__device__ __forceinline__ void proj2(const u32x8 (&W)[20], lptr Asrc, lptr dstp, int idx, int t, const Pc& pc, int lane, lptr L, bool young, unsigned& dg_, FN mid = Nop()) {
    f32x4 e = {0, 0, 0, 0}, o = {0, 0, 0, 0};
    u32x4 af[10], own, own2;
    asm volatile("" : "+v"(e), "+v"(o));
    if (KIND == 1) { drain(); DG(rt32()); }
    if (KIND == 3) { asm volatile("s_waitcnt vmcnt(1)" ::: "memory"); DG(rt32()); }
    if (KIND == 4) { if (young) asm volatile("s_waitcnt vmcnt(8)" ::: "memory"); else drain(); DG(rt32()); }
    bar();
    #pragma unroll
    for (int ks = 0; ks < 4; ++ks) af[ks] = lrd(Asrc + ks * 1024);
    SB();
    #pragma unroll
    for (int ks = 0; ks < PH1; ++ks) { e = smf(af[ks], W[2 * ks], e, idx); o = smf(af[ks], W[2 * ks + 1], o, idx); }
    asm volatile("" : "+v"(e), "+v"(o));
    if (KIND == 2 || KIND == 3) drain();
    if (KIND == 5) asm volatile("s_waitcnt vmcnt(1)" ::: "memory");
    if (KIND == 2 || KIND == 5) DG(rt32());
    bar();
    if (KIND != 4) own = lrd(Asrc + pc.p1 * 1024);
    if (KIND == 3) own2 = lrd(Asrc + pc.p2 * 1024);
    SB();
    #pragma unroll
    for (int ks = 4; ks < 10; ++ks) af[ks] = lrd(Asrc + ks * 1024);
    SB();
    #pragma unroll
    for (int ks = PH1; ks < 4; ++ks) { e = smf(af[ks], W[2 * ks], e, idx); o = smf(af[ks], W[2 * ks + 1], o, idx); }
    SB();
    if (KIND != 4) { asm volatile("" : "+v"(own)); unsigned m = mx4(own, 0); if (KIND == 3) m = mx4(own2, m); flag(m, t, lane, L); }
    #pragma unroll
    for (int ks = 4; ks < 10; ++ks) { e = smf(af[ks], W[2 * ks], e, idx); o = smf(af[ks], W[2 * ks + 1], o, idx); if (ks == 5) { asm volatile("" : "+v"(e), "+v"(o)); SB(); mid(); SB(); } }
    *(LAS unsigned*)dstp = pk2(e[0] + e[2], o[0] + o[2]); *(LAS unsigned*)(dstp + 16) = pk2(e[1] + e[3], o[1] + o[3]);
}
#define PROJ(KIND, GATE, YOUNG) FETCH((KIND == 3 ? 2 : KIND == 4 ? 0 : 1), GATE); proj2<KIND>(W, Asrc, pdst, sidx, t, pc, lane, L, YOUNG, dg_); ST(2); bar(); ST(3); \
        again = failed(tagc, L); if (again) { --t; continue; } DGL;
__device__ __forceinline__ void max3x2(float a, float b, float c, float& r0, float& r1) { asm("v_maximum3_f32 %0, %2, %3, %4\n\tv_maximum3_f32 %1, %2, %3, %4\n\ts_nop 1" : "=&v"(r0), "=&v"(r1) : "v"(a), "v"(b), "v"(c)); }
__device__ __forceinline__ void addx2(float a, float b, float& r0, float& r1) { asm("v_add_f32 %0, %2, %3\n\tv_add_f32 %1, %2, %3\n\ts_nop 1" : "=&v"(r0), "=&v"(r1) : "v"(a), "v"(b)); }
__device__ __forceinline__ void sw16(float& a, float& b) { auto r = __builtin_amdgcn_permlane16_swap(__float_as_uint(a), __float_as_uint(b), false, false); a = __uint_as_float(r[0]); b = __uint_as_float(r[1]); }
__device__ __forceinline__ void sw32(float& a, float& b) { auto r = __builtin_amdgcn_permlane32_swap(__float_as_uint(a), __float_as_uint(b), false, false); a = __uint_as_float(r[0]); b = __uint_as_float(r[1]); }
__device__ __forceinline__ int proj2_dst(int wq, int lane) { const int c = lane & 15; return (2 * wq + (c >> 3)) * 256 + (4 * (lane >> 4) + 2 * ((c >> 2) & 1)) * 16 + (c & 3) * 4; }
__device__ __forceinline__ int tokoff(int tok) { return (4 * (tok >> 1) + (tok & 1)) * 16; }

template <class FN> __device__ __forceinline__ void tile640(const u32x4 (&W)[40], lptr img, int lane, u32x4& o0, u32x4& o1, FN mid) {
    const int tok = lane & 31, kg = lane >> 5;
    f32x16 acc;
    #pragma unroll
    for (int i = 0; i < 16; ++i) acc[i] = 0;
    lptr b[4];
    #pragma unroll
    for (int m = 0; m < 4; ++m) b[m] = img + kg * 512 + m * 1024 + ((tok ^ (2 * m + kg)) * 16);
    u32x4 f[4];
    #pragma unroll
    for (int m = 0; m < 4; ++m) f[m] = lrd(b[m]);
    __builtin_amdgcn_sched_barrier(0);
    #pragma unroll
    for (int ks = 0; ks < 40; ++ks) { acc = mf32(W[ks], f[ks & 3], acc); if (ks + 4 < 40) f[ks & 3] = lrd(b[ks & 3] + ((ks + 4) >> 2) * 4096); mid(ks); __builtin_amdgcn_sched_barrier(0); }
    o0 = (u32x4){pk2(acc[0], acc[8]), pk2(acc[1], acc[9]), pk2(acc[2], acc[10]), pk2(acc[3], acc[11])};
    o1 = (u32x4){pk2(acc[4], acc[12]), pk2(acc[5], acc[13]), pk2(acc[6], acc[14]), pk2(acc[7], acc[15])};
}

constexpr float CS = 0.08838834764831845f * 1.4426950408889634f;
enum { R_P, R_V, R_C, R_OUT, R_QK, R_VO, R_F1, R_F2, R_SP };
#define FSW 1
#define RMAP 2
__host__ __device__ inline void rolemap(int b, int& role, int& id) {
    const int grp = b & 7, idx8 = b >> 3;
    if (RMAP == 2) {
        if (grp < 4) {
            if (idx8 < 8) { role = R_P; id = idx8 * 4 + grp; } else if (idx8 < 16) { role = R_V; id = (idx8 - 8) * 4 + grp; } else if (idx8 < 26) { role = R_C; id = grp * 10 + idx8 - 16; }
            else if (FSW) { const int k = (idx8 - 26) * 4 + grp; if (k < 23) { role = R_F1; id = k; } else { role = R_SP; id = 0; } }
            else if (idx8 < 31) { role = R_F2; id = (idx8 - 26) * 4 + grp; } else if (grp < 3) { role = R_F1; id = 24 + grp; } else { role = R_SP; id = 0; }
        } else if (idx8 < 10) { role = R_OUT; id = (grp - 4) * 10 + idx8; }
        else { const int wi = (idx8 - 10) * 4 + grp - 4;
            if (wi < 32) { role = R_VO; id = wi; } else if (wi < 64) { role = R_QK; id = wi - 32; } else if (!FSW) { role = R_F1; id = wi - 64; }
            else if (wi < 84) { role = R_F2; id = wi - 64; } else { role = R_F1; id = wi - 61; } }
        return;
    }
    if (RMAP == 3) {
        if (idx8 < 16) { role = idx8 < 4 ? R_P : idx8 < 8 ? R_QK : idx8 < 12 ? R_V : R_VO; id = (idx8 & 3) * 8 + grp; }
        else if (idx8 < 21) { role = R_C; id = grp * 5 + idx8 - 16; }
        else if (idx8 < 25) { role = R_OUT; id = grp * 4 + idx8 - 21; }
        else if (idx8 < 27 && grp < 4) { role = R_OUT; id = 32 + grp * 2 + idx8 - 25; }
        else { const int k = idx8 < 27 ? (idx8 - 25) * 4 + grp - 4 : 8 + (idx8 - 27) * 8 + grp;
            if (k < 27) { role = R_F1; id = k; } else if (k < 47) { role = R_F2; id = k - 27; } else { role = R_SP; id = 0; } }
        return;
    }
    if (grp == 0) { role = R_P; id = idx8; return; }
    if (grp == 1) { role = R_V; id = idx8; return; }
    if (RMAP == 0) {
        if (grp == 7) { if (idx8 < 27) { role = R_F1; id = idx8; } else { role = R_F2; id = idx8 - 27; } }
        else if (idx8 < 8) { role = R_C; id = (grp - 2) * 8 + idx8; }
        else if (idx8 < 16) { role = R_OUT; id = (grp - 2) * 8 + idx8 - 8; }
        else { const int wi = (idx8 - 16) * 5 + grp - 2;
            if (wi < 32) { role = R_QK; id = wi; } else if (wi < 64) { role = R_VO; id = wi - 32; } else if (wi < 79) { role = R_F2; id = wi - 59; } else { role = R_SP; id = 0; } }
    } else {
        if (grp < 4) {
            if (idx8 < 20) { role = R_C; id = (grp - 2) * 20 + idx8; }
            else if (idx8 < 30) { role = R_F2; id = (idx8 - 20) * 2 + grp - 2; }
            else { const int k = (idx8 - 30) * 2 + grp - 2; if (k < 3) { role = R_QK; id = 29 + k; } else { role = R_SP; id = 0; } }
        } else if (idx8 < 10) { role = R_OUT; id = (grp - 4) * 10 + idx8; }
        else { const int wi = (idx8 - 10) * 4 + grp - 4;
            if (wi < 32) { role = R_VO; id = wi; } else if (wi < 61) { role = R_QK; id = wi - 32; } else { role = R_F1; id = wi - 61; } }
    }
}
__global__ void __launch_bounds__(512, 2) mega(Args a) {
    extern __shared__ __attribute__((aligned(16))) unsigned char smem[];
    lptr L = (lptr)smem;
    const int tid = threadIdx.x, lane = tid & 63, wave = __builtin_amdgcn_readfirstlane(tid >> 6);
    int role, id; rolemap(blockIdx.x, role, id);
    const int hid = role == R_C ? id : role == R_OUT ? 40 + id : role == R_F1 ? 80 + id : role == R_F2 ? 107 + id : role == R_SP ? 127 : -1;
    const rsrc_t rs = mkrs(a.ws, WS_END);
    const int sidx = (lane & 2) ? 0xEEEE : 0x4444;
    bool dead = false;
    lptr stg = L + L_STG + wave * 4096;
    ST_INIT
    TL(0);
    if (hid >= 0) {
        const int row = hid * 16 + wave * 2 + (lane >> 5), l32 = lane & 31;
        if (hid < 120) {
            #pragma unroll
            for (int i = 0; i < 2; ++i) { const unsigned u = (i * 120 + hid) * 8192 + tid * 16;
                st128((u32x4){~0u, ~0u, ~0u, ~0u}, rs, u < SL_END ? u : u < 2 * SL_END ? u - SL_END + SLOT : u < 2 * SL_END + 3 * CH ? u - 2 * SL_END + 2 * SLOT + 2 * CH : OOB, WS_SLOT); } }
        u32x2 xv[5];
        #pragma unroll
        for (int i = 0; i < 5; ++i) xv[i] = *(const u32x2*)(a.x + row * D + (l32 + 32 * i) * 4);
        if (hid < 120) {
            u32x4 wv[5]; float sv[5];
            #pragma unroll
            for (int i = 0; i < 5; ++i) { const int k = (tid >> 2) + 128 * i; wv[i] = *(const u32x4*)(a.wm + k * 3840 + hid * 32 + (tid & 3) * 8); sv[i] = b2f(a.c[k]); }
            float acc[8] = {0, 0, 0, 0, 0, 0, 0, 0};
            #pragma unroll
            for (int i = 0; i < 5; ++i) { const float s = rnd(sv[i] / (1.f + __expf(-sv[i])));
                #pragma unroll
                for (int j = 0; j < 4; ++j) { acc[2 * j] += s * blo(wv[i][j]); acc[2 * j + 1] += s * bhi(wv[i][j]); } }
            #pragma unroll
            for (int j = 0; j < 8; ++j) { float v = acc[j]; v += ror<8>(v); v += ror<4>(v); v = x16sum(v); acc[j] = x32sum(v); }
            if (lane < 4) { *(LAS f32x4*)(L + wave * 128 + lane * 32) = (f32x4){acc[0], acc[1], acc[2], acc[3]}; *(LAS f32x4*)(L + wave * 128 + lane * 32 + 16) = (f32x4){acc[4], acc[5], acc[6], acc[7]}; }
            drain(); bar();
            if (tid < 16) { f32x2 s = {0, 0};
                #pragma unroll
                for (int w = 0; w < 8; ++w) s += *(LAS f32x2*)(L + w * 128 + tid * 8);
                st32(nosent(pk2(s[0], s[1])), rs, WS_MOD + hid * 64 + tid * 4, 0); }
        }
        float xf[20], s = 0;
        #pragma unroll
        for (int i = 0; i < 5; ++i) { xf[4 * i] = blo(xv[i].x); xf[4 * i + 1] = bhi(xv[i].x); xf[4 * i + 2] = blo(xv[i].y); xf[4 * i + 3] = bhi(xv[i].y); }
        #pragma unroll
        for (int i = 0; i < 20; ++i) s += xf[i];
        const float mean = x16sum(sum16(s)) * (1.f / D);
        float vs = 0;
        #pragma unroll
        for (int i = 0; i < 20; ++i) { const float d = xf[i] - mean; vs += d * d; }
        const float var = x16sum(sum16(vs)) * (1.f / D);
        const float mb = rnd(mean), rd = 1.f / rnd(__builtin_sqrtf(rnd(rnd(var) + 1e-5f)));
        #pragma unroll
        for (int i = 0; i < 20; ++i) xf[i] = rnd(rnd(xf[i] - mb) * rd);
        {   u32x4 mv; const unsigned mo = WS_MOD + (tid < 480 ? tid : 479) * 16;
            for (unsigned sp = 0;; ++sp) { asm volatile("" ::: "memory"); mv = ld_sc1(rs, mo, 0); if (all_ok(mx4(mv, 0))) break; if (sp > SPIN_MAX) { dead = true; break; } dly(2); }
            if (tid < 480) lwr(L + L_MOD + tid * 16, mv); }
        bar();
        #pragma unroll
        for (int i = 0; i < 5; ++i) { const u32x2 sa = *(LAS u32x2*)(L + L_MOD + (l32 + 32 * i) * 8), ca = *(LAS u32x2*)(L + L_MOD + 1280 + (l32 + 32 * i) * 8);
            u32x2 o;
            o.x = pk2(rnd(xf[4 * i] * rnd(1.f + blo(ca.x))) + blo(sa.x), rnd(xf[4 * i + 1] * rnd(1.f + bhi(ca.x))) + bhi(sa.x));
            o.y = pk2(rnd(xf[4 * i + 2] * rnd(1.f + blo(ca.y))) + blo(sa.y), rnd(xf[4 * i + 3] * rnd(1.f + bhi(ca.y))) + bhi(sa.y));
            st64(o, rs, WS_H1 + row * (D * 2) + (l32 + 32 * i) * 8, 0); }
        drain(); bar();
        if (tid == 0) st32(0u, rs, WS_FH1 + hid * 4, 0);
        TL(1);
        if (hid < 32) {
            const unsigned vo = lane < 32 ? WS_SLOT + SL_CAL + (wave & 1) * 4096 + ((wave >> 1) & 1) * CH + lane * 4 : OOB;
            unsigned m[2] = {~0u, ~0u};
            for (int r = 0; r < NCAL; ++r)
                #pragma unroll
                for (int q = 0; q < 2; ++q) { unsigned long long t0, t1;
                    asm volatile("s_waitcnt vmcnt(0)\n\ts_memtime %0\n\ts_waitcnt lgkmcnt(0)" : "=s"(t0) :: "memory");
                    st32(0u, rs, vo, (hid * 2 + (wave >> 2)) * SLOT + q * 8192);
                    asm volatile("s_waitcnt vmcnt(0)\n\ts_memtime %0\n\ts_waitcnt lgkmcnt(0)" : "=s"(t1) :: "memory");
                    m[q] = min(m[q], (unsigned)(t1 - t0)); }
            *(LAS u32x2*)(L + wave * 8) = (u32x2){m[0], m[1]};
            bar();
            if (wave == 0) { const u32x2 mm = *(LAS u32x2*)(L + (lane & 7) * 8);
                const unsigned bm = (unsigned)__builtin_amdgcn_ballot_w64(mm.y < mm.x);
                if (lane == 0) st64((u32x2){__builtin_popcount(bm & 7u) > 1 ? 1u : 0u, __builtin_popcount(bm & 0x70u) > 1 ? 1u : 0u}, rs, WS_POL + hid * 8, 0); }
            bar();
            TL(4);
        }
    }
    const bool gated = a.tp[0] > 0;
    const unsigned DLT = a.tp[0], D1 = a.tp[1], DU = a.tp[2];
    int tagc = 0; bool again = false; unsigned nfail = 0, dg_ = 0;
#define DGL
    if (role == R_P) {
        const int h = id;
        u32x8 W[20];
        load_w_smf<false>(W, mkrs(wave < 4 ? a.Wq : a.Wk, D * HE * 2), h * 256, wave, lane, L);
        TL(1);
        if (tid == 0) *(LAS int*)(L + L_FAIL) = -1;
        lptr Asrc = L + L_A + lane * 16;
        lptr pdst = L + L_QK + (wave >> 2) * 2048 + proj2_dst(wave & 3, lane);
        first_mem(a, wave, lane, Asrc);
        const unsigned long long pol = get_pol(rs, lane, dead);
        asm volatile("" : "+v"(pdst));
        ST(0);
        if (wave == 0) {
            const int rho = lane & 15, g = lane >> 4;
            const Pc pc = mkpc(4, 4, lane);
            const int key = (((rho >> 2) & 1) << 3) | (((rho >> 1) & 1) << 2) | (((rho >> 3) & 1) << 1) | (rho & 1);
            lptr qsrc = L + L_QK + (g >> 1) * 256 + (g & 1) * 32 + tokoff(rho & 7);
            lptr msrc = L + L_QK + 2048 + (g >> 1) * 256 + (g & 1) * 32 + tokoff(2 * (rho >> 2) + (rho & 1));
            lptr ksrc = L + L_KQX + (g * 32 + key) * 16;
            asm volatile("" : "+v"(qsrc), "+v"(msrc));
            const bool ch = (lane & 8) != 0;
            const unsigned vpx = WS_SLOT + SL_PX + (h >> 4) * CH + (h & 15) * 512 + ((lane >> 3) & 1) * 256 + (g >> 1) * 32 + tokoff(lane & 7) + (g & 1) * 8;
            const unsigned vpm = ch ? OOB : WS_SLOT + SL_PM + ((lane >> 2) & 1) * 2048 + (h >> 1) * 128 + tokoff(lane & 3) + (g & 1) * 32 + (h & 1) * 8 + (g >> 1) * 4;
            unsigned long long rt0 = 0;
            for (int t = 0; t < NSEG; ++t) {
                ST_T(t); if (t == 2) { ST_RESET; }
                u32x4 k0[4], k1[4];
                int fl;
                unsigned so = SO(t);
                lptr kp = ksrc + (t % 3) * 16384;
                asm volatile("" : "+s"(so), "+v"(kp));
                auto ldk = [&]() {
                    #pragma unroll
                    for (int s = 0; s < 4; ++s) { k0[s] = lrd(kp + s * 2048); k1[s] = lrd(kp + s * 2048 + 256); } };
                auto scores = [&]() -> bool {
                    u32x4 q[4], km[4];
                    #pragma unroll
                    for (int s = 0; s < 4; ++s) { q[s] = lrd(qsrc + s * 512); SB(); }
                    #pragma unroll
                    for (int s = 0; s < 4; ++s) { km[s] = lrd(msrc + s * 512); SB(); }
                    fl = *(LAS int*)(L + L_FAIL);
                    SB();
                    f32x4 sm = {0, 0, 0, 0}, s0 = sm, s1 = sm;
                    #pragma unroll
                    for (int s = 0; s < 4; ++s) { s0 = mf16(k0[s], q[s], s0); s1 = mf16(k1[s], q[s], s1); }
                    asm volatile("" : "+v"(s0), "+v"(s1), "+v"(km[0]), "+v"(fl));
                    #pragma unroll
                    for (int s = 0; s < 4; ++s) sm = mf16(km[s], q[s], sm);
                    asm volatile("" : "+v"(sm));
                    if (__builtin_expect(__builtin_amdgcn_readfirstlane(fl) == tagc, 0)) return true;
                    const float mx = fmx(fmx(fmx(fmx(s0[0], s0[1]), s0[2]), fmx(fmx(s0[3], s1[0]), s1[1])), fmx(s1[2], s1[3]));
                    float v0 = ch ? s1[0] : s0[0], v1 = ch ? s1[1] : s0[1], v2 = ch ? s1[2] : s0[2], v3 = ch ? s1[3] : s0[3];
                    SB();
                    float ma = fmx(fmx(mx, sm[0]), sm[1]), mb = fmx(fmx(mx, sm[1]), sm[0]), m2, m3;
                    float vm = ch ? sm[1] : sm[0];
                    sw16(ma, mb); max3x2(ma, mb, mb, m2, m3); sw32(m2, m3);
                    const float mc = fmx(m2, m3) * CS;
                    v0 = __builtin_amdgcn_exp2f(v0 * CS - mc); v1 = __builtin_amdgcn_exp2f(v1 * CS - mc); v2 = __builtin_amdgcn_exp2f(v2 * CS - mc); v3 = __builtin_amdgcn_exp2f(v3 * CS - mc); vm = __builtin_amdgcn_exp2f(vm * CS - mc);
                    float sa, sb2, sc, sd;
                    addx2((v0 + v1) + v2, v3 + vm, sa, sb2); sw16(sa, sb2); addx2(sa, sb2, sc, sd); sw32(sc, sd);
                    float sum = sc + sd;
                    const float vmo = ror<8>(vm);
                    sum += ror<8>(sum);
                    const float inv = __builtin_amdgcn_rcpf(sum);
                    u32x2 px; px.x = pk2(v0 * inv, v1 * inv); px.y = pk2(v2 * inv, v3 * inv);
                    st64(px, rs, vpx, so);
                    st32(pk2(vm * inv, vmo * inv), rs, vpm, so);
                    return false; };
                FETCH(1, );
                proj2<2>(W, Asrc, pdst, sidx, t, pc, lane, L, false, dg_, ldk);
                ST(2);
                bar();
                ST(3);
                again = scores();
                if (again) { --t; ++nfail; continue; }
                ST(4); LG(0, rt32()); DGL;
            }
        } else if (wave == 1) {
            const int r32 = lane & 31, hf = lane >> 5;
            const int prow = (r32 & 19) | ((r32 & 4) << 1) | ((r32 & 8) >> 1);
            lptr qsrc = L + L_KQX + 8192 + (hf * 32 + r32) * 16;
            lptr ksrc = L + L_KQX + (hf * 32 + prow) * 16;
            lptr msrc = L + L_QK + 2048 + hf * 32 + tokoff(prow & 7);
            const unsigned vax = WS_AX + (h * 2 + (r32 >> 4)) * 1024 + (hf * 16 + (r32 & 15)) * 16;
            const unsigned vam = WS_AM + ((h >> 2) * 2 + (r32 >> 4)) * 1024 + ((h & 3) * 16 + (r32 & 15)) * 16;
            const int lim = r32 - 8 * hf;
            const Pc pc = mkpc(5, 5, lane);
            for (int t = 0; t < NSEG; ++t) {
                ST_T(t); if (t == 2) { ST_RESET; }
                FETCH(1, );
                if (tagc < 0) drain(); else asm volatile("s_waitcnt vmcnt(1)" ::: "memory");
                st32((unsigned)(t - 1), rs, lane ? OOB : WS_PP + h * 128, 0);
                proj2<5>(W, Asrc, pdst, sidx, t, pc, lane, L, false, dg_); bar();
                again = failed(tagc, L); if (again) { --t; continue; } DGL;
                f32x16 sx, sm;
                #pragma unroll
                for (int i = 0; i < 16; ++i) { sx[i] = 0; sm[i] = 0; }
                const int po = (t % 3) * 16384;
                #pragma unroll 2
                for (int s = 0; s < 8; ++s) {
                    u32x4 q = lrd(qsrc + po + s * 1024), k = lrd(ksrc + po + s * 1024), km = lrd(msrc + s * 256);
                    sx = mf32(k, q, sx); sm = mf32(km, q, sm);
                }
                float m = sm[0];
                #pragma unroll
                for (int j = 1; j < 8; ++j) m = fmx(m, sm[j]);
                #pragma unroll
                for (int i = 0; i < 16; ++i) { if (16 * (i >> 3) + (i & 7) > lim) sx[i] = -3.0e38f; m = fmx(m, sx[i]); }
                m = x32max(m);
                const float mc = m * CS; float sum = 0, summ = 0;
                #pragma unroll
                for (int i = 0; i < 16; ++i) { sx[i] = __builtin_amdgcn_exp2f(sx[i] * CS - mc); sum += sx[i]; }
                #pragma unroll
                for (int j = 0; j < 8; ++j) { sm[j] = __builtin_amdgcn_exp2f(sm[j] * CS - mc); summ += sm[j]; }
                sum = x32sum(sum) + summ;
                const float inv = __builtin_amdgcn_rcpf(sum);
                #pragma unroll
                for (int p = 0; p < 2; ++p) { u32x4 o; o.x = pk2(sx[8 * p] * inv, sx[8 * p + 1] * inv); o.y = pk2(sx[8 * p + 2] * inv, sx[8 * p + 3] * inv); o.z = pk2(sx[8 * p + 4] * inv, sx[8 * p + 5] * inv); o.w = pk2(sx[8 * p + 6] * inv, sx[8 * p + 7] * inv);
                    st128(o, rs, vax + p * 512, t * 65536); }
                if (hf == 0) { u32x4 o; o.x = pk2(sm[0] * inv, sm[1] * inv); o.y = pk2(sm[2] * inv, sm[3] * inv); o.z = pk2(sm[4] * inv, sm[5] * inv); o.w = pk2(sm[6] * inv, sm[7] * inv);
                    st128(o, rs, vam, t * 16384); }
                ST(4);
            }
            drain();
            if (lane == 0) st32((unsigned)(NSEG - 1), rs, WS_PP + h * 128, 0);
        } else if (wave < 4) {
            const unsigned vsrc = WS_KQ + h * CH + lane * 16;
            lptr ldst = L + L_KQX + (wave - 2) * 8192;
            int seen = -1, nf = 1, pv = -1;
            const Pc pc = mkpc(0, 0, lane);
            auto fetch = [&](int t) {
                wait_prog(rs, WS_PQK + h * 128, t, seen, dead, 2);
                const unsigned so = RO(t, wave - 2);
                dma4(rs, ldst + (t % 3) * 16384, vsrc, so); dma4(rs, ldst + (t % 3) * 16384 + 4096, vsrc, so + 4096); };
            fetch(0);
            for (int t = 0; t < NSEG; ++t) {
                ST_T(t); if (t == 2) { ST_RESET; }
                PROJ(4, , nf > t + 1);
                seen = max(seen, __builtin_amdgcn_readfirstlane(pv));
                asm volatile("" ::: "memory");
                pv = (int)__builtin_amdgcn_raw_buffer_load_b32(rs, seen >= NSEG ? OOB : WS_PQK + h * 128, 0, 16);
                if (nf == t + 1 && nf < NSEG) { fetch(nf); ++nf; }
                if (nf == t + 2 && nf < NSEG && seen >= nf) { const unsigned so = RO(nf, wave - 2);
                    for (int i = 0; i < 8; ++i) { dma1(rs, ldst + (nf % 3) * 16384 + i * 1024, vsrc, so + i * 1024); dly(a.tp[7]); }
                    ++nf; }
                ST(4);
            }
        } else {
            unsigned tgt = rt32();
            const Pc pc = mkpc(wave - 4, wave + 2, lane);
            const unsigned vcx = WS_SLOT + SL_PX + (h >> 4) * CH + (h & 15) * 512 + lane * 8, vcm = lane < 16 ? WS_SLOT + SL_PM + (lane >> 3) * 2048 + (h >> 1) * 128 + (lane & 7) * 16 + (h & 1) * 8 : OOB;
            for (int t = 0; t < NSEG; ++t) {
                ST_T(t); if (t == 2) { ST_RESET; }
                PROJ(3, gate(tgt), false);
                if (wave == 7) LG(0, tgt);
                if (wave == 6) { st64((u32x2){~0u, ~0u}, rs, vcx, SO(t + 2)); st64((u32x2){~0u, ~0u}, rs, vcm, SO(t + 2)); }
                tgt = (wave == 7 && gated && t > 0) ? read_G(rs, SO(t - 1), lane, dead) + DLT : rt32();
                ST(4);
            }
        }
    } else if (role == R_V) {
        const int h = id;
        u32x8 W[20];
        if (wave < 4) {
            load_w_smf<false>(W, mkrs(a.Wv, D * HE * 2), h * 256, wave, lane, L);
            TL(1);
            if (tid == 0) *(LAS int*)(L + L_FAIL) = -1;
            lptr pdst = L + L_QK + proj2_dst(wave, lane);
            const Pc pc = mkpc(wave, wave, lane);
            lptr Asrc = L + L_A + lane * 16;
            first_mem(a, wave, lane, Asrc);
            const unsigned long long pol = get_pol(rs, lane, dead);
            unsigned tgt = rt32();
            for (int t = 0; t < NSEG; ++t) {
                ST_T(t); if (t == 2) { ST_RESET; }
                PROJ(1, gate(tgt), false);
                tgt = (wave == 3 && gated && t > 0) ? read_G(rs, SO(t - 1), lane, dead) + DLT : rt32();
                ST(4);
            }
        } else {
            load_w_smf<true>(W, mkrs(a.Wo, D * HE * 2), h * E * D * 2, wave, lane, L);
            TL(1);
            const unsigned vu = WS_SLOT + SL_U + (10 * (wave - 4) + 8 + ((lane >> 4) & 1)) * CH + h * 256 + (lane & 15) * 16 + (lane >> 5) * 8;
            const unsigned vw = WS_SLOT + SL_U + (10 * (wave - 4) + (lane >> 4)) * CH + h * 256 + (lane & 15) * 16;
            const Pc pc = mkpc(wave, wave + 2, lane);
            lptr Asrc = L + L_A + lane * 16;
            first_mem(a, wave, lane, Asrc);
            const unsigned long long pol = get_pol(rs, lane, dead);
            ST(0);
            for (int t = 0; t < NSEG; ++t) {
                ST_T(t); if (t == 2) { ST_RESET; }
                f32x4 dd[10];
                int fl;
                unsigned so = SO(t), so2 = so + 4 * CH;
                asm volatile("" : "+s"(so), "+s"(so2));
                auto zero = [&]() {
                    #pragma unroll
                    for (int j = 0; j < 10; ++j) { dd[j] = (f32x4){0, 0, 0, 0}; asm volatile("" : "+v"(dd[j])); } };
                auto ustage = [&]() -> bool {
                    const u32x4 a0 = lrd(L + L_QK + lane * 16); SB();
                    const u32x4 a1 = lrd(L + L_QK + 1024 + lane * 16); SB();
                    fl = *(LAS int*)(L + L_FAIL);
                    SB();
                    unsigned o[10];
                    auto fin = [&](int j) { o[j] = pk2(dd[j][0] + dd[j][2], dd[j][1] + dd[j][3]); };
                    auto s16 = [&](int j) { auto r = __builtin_amdgcn_permlane16_swap(o[j], o[j + 1], false, false); o[j] = r[0]; o[j + 1] = r[1]; };
                    auto s32 = [&](int j) { auto r = __builtin_amdgcn_permlane32_swap(o[j], o[j + 2], false, false); o[j] = r[0]; o[j + 2] = r[1]; };
                    auto m0 = [&](int j) { dd[j] = smf(a0, W[2 * j], dd[j], sidx); };
                    auto m1 = [&](int j) { dd[j] = smf(a1, W[2 * j + 1], dd[j], sidx); };
                    m0(0); m0(1); m0(2); m0(3); SB();
                    m1(0); SB(); const int flu = __builtin_amdgcn_readfirstlane(fl); m1(1); SB(); m1(2); m1(3); asm volatile("" : "+v"(dd[0]), "+v"(dd[1]), "+v"(dd[2]), "+v"(dd[3])); SB();
                    if (__builtin_expect(flu == tagc, 0)) return true;
                    m0(4); fin(0); SB(); m0(5); fin(1); SB(); m0(6); fin(2); SB(); m0(7); s16(0); SB();
                    m1(4); fin(3); SB(); m1(5); SB(); m1(6); s16(2); SB(); m1(7); SB();
                    m0(8); s32(0); s32(1); SB(); m0(9); fin(4); SB();
                    m1(8); st128((u32x4){o[0], o[1], o[2], o[3]}, rs, vw, so); fin(5); SB(); m1(9); fin(6); SB();
                    fin(7); s16(4); fin(8); fin(9); s16(6); s16(8); s32(4); s32(5);
                    st64((u32x2){o[8], o[9]}, rs, vu, so);
                    st128((u32x4){o[4], o[5], o[6], o[7]}, rs, vw, so2);
                    return false; };
                zero();
                if (wave >= 6) { FETCH(2, ); bar(); drain(); bar(); check_own<2>(t, lane, pc, Asrc, L); } else { FETCH(1, ); bar(); drain(); bar(); check_own<1>(t, lane, pc, Asrc, L); }
                bar();
                ST(3);
                again = ustage();
                if (again) { --t; continue; }
                ST(4); LG(0, rt32());
                dly(a.tp[9]);
                const unsigned sc = SO(t + 2);
                st128((u32x4){~0u, ~0u, ~0u, ~0u}, rs, vw, sc); dly(a.tp[13]); st128((u32x4){~0u, ~0u, ~0u, ~0u}, rs, vw, sc + 4 * CH); dly(a.tp[13]);
                st64((u32x2){~0u, ~0u}, rs, vu, sc);
            }
        }
    } else if (role == R_C) {
      const int c = id;
      LAS unsigned* gbox = (LAS unsigned*)(L + 98304);
      constexpr int L_CU = 98560;
      const unsigned long long pol = get_pol(rs, lane, dead);
      const unsigned cmem = WS_SLOT + SL_MEM + (c >> 5) * CH + (c & 31) * 256;
      if (wave >= 4) {
        const unsigned vcf = WS_CF + ((c % 20) * 4 + wave - 4) * CH + lane * 16;
        int seen = -1, nf = 1;
        const unsigned vpv = WS_PVO + (8 * (wave - 4) + (lane & 7)) * 128;
        lptr cdst = L + (wave - 4) * 8192;
        auto ldcf = [&](int t) {
            wait_prog(rs, vpv, t, seen, dead, 2);
            const unsigned so = CO(t, c >= 20);
            dma4(rs, cdst + (t % 3) * 32768, vcf, so); dma4(rs, cdst + (t % 3) * 32768 + 4096, vcf, so + 4096); };
        const unsigned vclr = wave != 7 ? OOB : lane < 32 ? cmem + lane * 8 : lane == 32 ? WS_SLOT + SL_STP + c * 32 : OOB;
        ldcf(0); drain(); bar();
        for (int t = 0;; ++t) {
            if (seen < nf) peek_prog(rs, vpv, nf, seen);
            if (nf == t + 1 && nf < NSEG - 1 && seen >= nf) { ldcf(nf); ++nf; }
            if (t > 0) st64((u32x2){~0u, ~0u}, rs, vclr, SO(t + 2));
            if (nf == t + 2 && nf < NSEG - 1 && seen >= nf) {
                const unsigned so = CO(nf, c >= 20);
                lptr d = cdst + (nf % 3) * 32768;
                int pv = (int)__builtin_amdgcn_raw_buffer_load_b32(rs, seen >= NSEG ? OOB : vpv, 0, 16);
                dly(a.tp[14] + (wave - 4) * a.tp[15]);
                #pragma unroll
                for (int i = 0; i < NB1; ++i) { dma1(rs, d + i * 1024, vcf, so + i * 1024); if (i + 1 < NB1) dly(a.tp[8]); }
                DG(rt32());
                bar();
                #pragma unroll
                for (int i = NB1; i < NB1 + NB2; ++i) { dma1(rs, d + i * 1024, vcf, so + i * 1024); if (i + 1 < NB1 + NB2) dly(4); }
                bar();
                #pragma unroll
                for (int i = NB1 + NB2; i < 8; ++i) dma1(rs, d + i * 1024, vcf, so + i * 1024);
                asm volatile("" : "+v"(pv));
                seen = max(seen, min8(pv));
                asm volatile("s_waitcnt vmcnt(8)" ::: "memory");
                ++nf;
            } else {
                DG(rt32());
                bar();
                bar();
                if (t == NSEG - 2) break;
                if (nf == t + 1) { ldcf(nf); ++nf; }
                drain();
            }
            bar();
            DGL;
        }
      } else if (wave >= 2) {
        const unsigned vu = WS_SLOT + SL_U + c * CH + (wave - 2) * 4096 + lane * 16;
        unsigned G = rt32() - 100000u;
        *gbox = G;
        bar();
        for (int t = 0;; ++t) {
            const unsigned so = SO(t);
            unsigned d2_ = 0;
            DG(rt32());
            gate(G + DU);
            bar();
            dma4(rs, L + L_CU + (wave - 2) * 4096, vu, so);
            drain();
            bar();
            if (t == NSEG - 2) break;
            bar();
            if (wave == 2 && gated) {
                dly(a.tp[11]);
                G = read_G(rs, so, lane, dead);
                *gbox = G; }
        }
      } else {
        const int rh = wave, rho = lane & 15, kg = lane >> 4;
        u32x4 cf[32];
        auto ldcf = [&](int t) {
            #pragma unroll
            for (int i = 0; i < 32; ++i) cf[i] = lrd(L + (t % 3) * 32768 + lane * 16 + i * 1024); };
        bar();
        ldcf(0);
        const unsigned opx = WS_SLOT + SL_PX + (2 * (rho >> 3) + (kg >> 1)) * 512 + (kg & 1) * 256 + rh * 128 + (rho & 7) * 16;
        const unsigned opm = WS_SLOT + SL_PM + rh * 2048 + (4 * (rho >> 3) + kg) * 128 + (rho & 7) * 16;
        const unsigned ou = WS_SLOT + SL_U + c * CH + lane * 16;
        const unsigned vmem = (lane & 1) ? OOB : cmem + rh * 128 + (4 * (kg & 1) + 2 * (rho >> 3) + (lane >> 5)) * 16 + (rho & 7) * 2;
        const unsigned vstp = lane ? OOB : WS_SLOT + SL_STP + c * 32 + rh * 4;
        const bool hi = lane >= 32;
        unsigned nlate = 0;
        for (int t = 0; t < NSEG - 1; ++t) {
            ST_T(t); if (t == 2) { ST_RESET; }
            const unsigned sb = SO(t);
            u32x4 p[10], u[8];
            f32x4 ca, cb;
            unsigned res; float v;
            auto ldu = [&]() {
                #pragma unroll
                for (int i = 0; i < 8; ++i) u[i] = ld_sc1(rs, ou + (i & 3) * 1024, sb + (i >> 2) * 4096); };
            auto ldp = [&]() {
                #pragma unroll
                for (int i = 0; i < 8; ++i) { p[i] = ld_sc1(rs, opx + (i & 1) * 2048, sb + (i >> 2) * CH + ((i >> 1) & 1) * 4096); __builtin_amdgcn_sched_barrier(0); }
                p[8] = ld_sc1(rs, opm, sb); __builtin_amdgcn_sched_barrier(0); p[9] = ld_sc1(rs, opm + 1024, sb); };
            auto comb = [&](bool first) {
                #pragma unroll
                for (int f = 0; f < 8; ++f) {
                    ca = smf(p[f], __builtin_shufflevector(cf[4 * f], cf[4 * f + 1], 0, 1, 2, 3, 4, 5, 6, 7), ca, sidx);
                    cb = smf(p[f], __builtin_shufflevector(cf[4 * f + 2], cf[4 * f + 3], 0, 1, 2, 3, 4, 5, 6, 7), cb, sidx);
                    if (first && f == UBAR) { asm volatile("" : "+v"(ca), "+v"(cb)); bar();
                        #pragma unroll
                        for (int i = 0; i < 8; ++i) u[i] = lrd(L + L_CU + lane * 16 + i * 1024); }
                    __builtin_amdgcn_sched_barrier(0); }
                #pragma unroll
                for (int f = 0; f < 2; ++f) {
                    ca = smf(p[8 + f], __builtin_shufflevector(u[4 * f], u[4 * f + 1], 0, 1, 2, 3, 4, 5, 6, 7), ca, sidx);
                    cb = smf(p[8 + f], __builtin_shufflevector(u[4 * f + 2], u[4 * f + 3], 0, 1, 2, 3, 4, 5, 6, 7), cb, sidx);
                    __builtin_amdgcn_sched_barrier(0); }
                const float t0 = hi ? cb[0] + cb[2] : ca[0] + ca[2], t1 = hi ? cb[1] + cb[3] : ca[1] + ca[3];
                v = fold32(t0, t1);
                res = pk2(v, __int_as_float(__builtin_amdgcn_update_dpp(0, __float_as_int(v), 0xB1, 0xf, 0xf, true)));
                asm volatile("" : "+v"(res)); };
            ca = (f32x4){0, 0, 0, 0}; cb = ca;
            asm volatile("" : "+v"(ca), "+v"(cb));
            bar();
            for (int i = 0; i < D1; ++i) asm volatile("s_nop 3");
            ST(5);
            ldp();
            const unsigned G = __builtin_amdgcn_readfirstlane(*gbox);
            __builtin_amdgcn_sched_barrier(0);
            comb(true);
            if (__builtin_expect(__builtin_amdgcn_ballot_w64(v != v) != 0ull, 0)) {
                for (unsigned sp = 0;; ++sp) {
                    unsigned m = 0, mu = 0;
                    #pragma unroll
                    for (int i = 0; i < 10; ++i) m = mx4(p[i], m);
                    #pragma unroll
                    for (int i = 0; i < 8; ++i) mu = mx4(u[i], mu);
                    if (all_ok(m > mu ? m : mu) || dead) break;
                    if (sp == 0) nfail += all_ok(mu) ? 1 : 0x10000;
                    if (sp > SPIN_MAX) dead = true;
                    if (sp) dly(a.tp[4]);
                    asm volatile("" ::: "memory");
                    if (mu == 0xFFFFFFFFu) ldu();
                    if (m == 0xFFFFFFFFu) ldp(); }
                ca = (f32x4){0, 0, 0, 0}; cb = ca;
                comb(false); }
            ST(0);
            st32(res, rs, vmem, sb);
            ST(3);
            const unsigned now = rt32();
            LG(0, now); LG(1, G);
            unsigned Sn = G + DLT;
            const int lt = (int)(now - Sn);
            if (lt > a.tp[5]) { ++nlate; Sn += max((lt + a.tp[6] + 3) & ~3, a.tp[12]); if (t == 0) Sn &= ~3u; }
            st32(nosent(Sn), rs, vstp, sb);
            if (t == NSEG - 2) break;
            bar();
            ldcf(t + 1);
            ST(4);
            if (t == 0) TL(2);
        }
      }
    } else if (role == R_QK || role == R_VO) {
        const int h = id; const bool vo = role == R_VO, st2 = vo && wave >= 4;
        u32x4 W[40];
        if (st2) load_w_32<true>(W, mkrs(a.Wo, D * HE * 2), h * E * D * 2, wave, lane, L);
        else load_w_32<false>(W, mkrs(vo ? a.Wv : wave < 4 ? a.Wq : a.Wk, D * HE * 2), h * 256, wave, lane, L);
        TL(1);
        const unsigned vh = WS_H1 + (tid >> 4) * (D * 2) + (tid & 15) * 16;
        lptr wdst = L + (tid & 15) * 512 + (((tid >> 4) ^ (tid & 7)) * 16);
        lptr vbuf = L + 81920;
        bool allh = false;
        int seen = -1;
        u32x4 hv[5];
        auto waith = [&](int s) {
            if (!allh) for (unsigned sp = 0;; ++sp) { asm volatile("" ::: "memory");
                const u32x2 f = __builtin_amdgcn_raw_buffer_load_b64(rs, WS_FH1 + lane * 8, 0, 16);
                const unsigned long long bad = __builtin_amdgcn_ballot_w64((f.x | f.y) != 0u);
                allh = bad == 0ull;
                if (!((bad >> s) & 1ull) || dead) break;
                if (sp > SPIN_MAX) { dead = true; break; }
                dly(2); }
            asm volatile("" ::: "memory"); };
        waith(0);
        #pragma unroll
        for (int i = 0; i < 5; ++i) hv[i] = ld_sc1(rs, vh + i * 256, 0);
        waith(1);
        #pragma unroll
        for (int i = 0; i < 5; ++i) { lwr(wdst + i * 8192, hv[i]); hv[i] = ld_sc1(rs, vh + i * 256, 32 * D * 2); }
        bar();
        const unsigned long long pol = get_pol(rs, lane, dead);
        auto stage = [&](int s, int i) { lwr(wdst + ((s + 1) & 1) * 40960 + i * 8192, hv[i]); hv[i] = ld_sc1(rs, vh + i * 256, (s + 2 < NSEG ? s + 2 : NSEG - 1) * (32 * D * 2)); };
        if (!vo) {
            const unsigned vdst = WS_KQ + h * CH + (wave & 3) * 2048 + lane * 16;
            for (int s = 0; s < NSEG; ++s) {
                if (s == 1) { ST_RESET; }
                if (s + 2 < NSEG) waith(s + 2);
                ST(0);
                u32x4 o0, o1;
                tile640(W, L + (s & 1) * 40960, lane, o0, o1, [&](int ks) { if (ks % 6 == 2 && ks < 30) stage(s, ks / 6); });
                asm volatile("s_waitcnt vmcnt(5)" ::: "memory");
                const unsigned so = RO(s, wave < 4);
                st128(o0, rs, vdst, so); st128(o1, rs, vdst + 1024, so);
                if (wave == 0) wait_min(rs, WS_PP + h * 128, s - 31, seen, dead);
                ST(1);
                const bool now = s < 3 || s == NSEG - 1;
                if (now) drain();
                ST(3);
                bar();
                ST(4);
                if (tid == 0) st32((unsigned)(s == NSEG - 1 ? 1000 : now ? s : s - 1), rs, WS_PQK + h * 128, 0);
                if (s == 0) TL(2);
            }
        } else if (!st2) {
            for (int s = 0; s <= NSEG; ++s) {
                if (s == 1) { ST_RESET; }
                if (s + 2 < NSEG) waith(s + 2);
                ST(0);
                if (s < NSEG) { u32x4 o0, o1;
                    tile640(W, L + (s & 1) * 40960, lane, o0, o1, [&](int ks) { if (ks % 6 == 2 && ks < 30) stage(s, ks / 6); });
                    lwr(vbuf + (s & 1) * 8192 + wave * 2048 + lane * 16, o0); lwr(vbuf + (s & 1) * 8192 + wave * 2048 + 1024 + lane * 16, o1);
                    if (wave == 1 && s >= 32) wait_min(rs, WS_POUT + (lane < 40 ? lane : 0) * 128, s - 31, seen, dead); }
                ST(1);
                bar();
                ST(4);
                const bool now = s < 4 || s == NSEG;
                if (tid == 0 && s > 0) st32((unsigned)(s == NSEG ? 1000 : now ? s - 1 : s - 2), rs, WS_PVO + h * 128, 0);
                if (s == 1) TL(2);
            }
        } else {
            const int r32 = lane & 31, hf = lane >> 5;
            const int prow = (r32 & 19) | ((r32 & 4) << 1) | ((r32 & 8) >> 1);
            lptr asrc = vbuf + hf * 512 + prow * 16;
            __builtin_amdgcn_s_setprio(1);
            const unsigned vdst = WS_CF + ((10 * (wave & 1) + (r32 >> 4)) * 4 + (h >> 3)) * CH + (h & 7) * 1024 + (hf * 16 + (r32 & 15)) * 16;
            for (int s = 0; s <= NSEG; ++s) {
                if (s == 1) { ST_RESET; }
                if (s + 2 < NSEG) waith(s + 2);
                ST(0);
                u32x4 af[8];
                #pragma unroll
                for (int ks = 0; ks < 8; ++ks) af[ks] = lrd(asrc + ((s - 1) & 1) * 8192 + ks * 1024);
                const unsigned so = CO(s > 0 ? s - 1 : 0, wave >= 6);
                #pragma unroll
                for (int tt = 0; tt < 5; ++tt) {
                    f32x16 acc;
                    #pragma unroll
                    for (int i = 0; i < 16; ++i) acc[i] = 0;
                    if (s > 0) {
                        #pragma unroll
                        for (int ks = 0; ks < 8; ++ks) acc = mf32(af[ks], W[8 * tt + ks], acc);
                    }
                    stage(s, tt);
                    #pragma unroll
                    for (int p = 0; p < 2; ++p) st128((u32x4){pk2(acc[8 * p], acc[8 * p + 1]), pk2(acc[8 * p + 2], acc[8 * p + 3]), pk2(acc[8 * p + 4], acc[8 * p + 5]), pk2(acc[8 * p + 6], acc[8 * p + 7])}, rs, vdst + p * 512, so + tt * (8 * CH));
                }
                asm volatile("s_waitcnt vmcnt(15)" ::: "memory");
                ST(1);
                if (s < 4 || s == NSEG) drain();
                ST(3);
                bar();
                ST(4);
            }
        }
    } else if (role == R_OUT) {
        const int cg = id >> 1, mt = id & 1;
        const unsigned vcf = WS_CF + ((cg % 10) * 8 + (wave >> 1)) * CH + (wave & 1) * 4096 + lane * 16;
        const unsigned vax = WS_AX + (8 * wave + mt) * 1024 + lane * 16, vam = WS_AM + (2 * wave + mt) * 1024 + lane * 16;
        const unsigned vu = WS_SLOT + SL_U + 2 * cg * CH + wave * 1024 + lane * 16;
        const unsigned long long pol = get_pol(rs, lane, dead);
        lptr part = L + wave * 2304 + (lane & 15) * 144 + (lane >> 4) * 16;
        lptr rsrc = L + (lane >> 2) * 144 + (lane & 3) * 32;
        const unsigned vx = (mt * 16 + (lane >> 2)) * (D * 2) + cg * 64 + (lane & 3) * 16;
        const rsrc_t rx = mkrs(a.x, S * D * 2);
        const u32x4 ga = lrd(L + L_MOD + 2560 + cg * 64 + (lane & 3) * 16);
        int seenv = -1, seenp = -1;
        for (int s = 0; s < NSEG; ++s) {
            u32x4 cf[8], xr;
            if (s == 2) { ST_RESET; }
            wait_prog(rs, WS_PVO + (4 * wave + (lane & 3)) * 128, s, seenv, dead, 4);
            #pragma unroll
            for (int i = 0; i < 8; ++i) cf[i] = ld_sc1(rs, vcf + (i & 3) * 1024, CO(s, cg >= 10) + (i >> 2) * (4 * CH));
            xr = ld_pl(rx, vx, s * (32 * D * 2));
            if (s > 0 && wave == ((s - 1) & 7)) { asm volatile("s_waitcnt vmcnt(9)" ::: "memory"); if (lane == 0) st32((unsigned)(s - 1), rs, WS_POUT + id * 128, 0); }
            ST(0);
            wait_prog(rs, WS_PP + (4 * wave + (lane & 3)) * 128, s, seenp, dead, 4);
            ST(1);
            u32x4 ax[4], am, u0, u1;
            #pragma unroll
            for (int i = 0; i < 4; ++i) ax[i] = ld_sc1(rs, vax + i * 2048, s * 65536);
            am = ld_sc1(rs, vam, s * 16384);
            const int pv = (int)__builtin_amdgcn_raw_buffer_load_b32(rs, seenp > s ? OOB : WS_PP + (4 * wave + (lane & 3)) * 128, 0, 16);
            for (unsigned sp = 0;; ++sp) { asm volatile("" ::: "memory");
                u0 = ld_sc1(rs, vu, SO(s)); u1 = ld_sc1(rs, vu, SO(s) + CH);
                if (all_ok(mx4(u0, mx4(u1, 0))) || dead) break;
                if (sp > SPIN_MAX) { dead = true; break; }
                dly(2); }
            if (seenp <= s && __builtin_amdgcn_ballot_w64(pv <= s) == 0ull) seenp = s + 1;
            ST(2);
            f32x4 d0 = {0, 0, 0, 0}, d1 = d0;
            #pragma unroll
            for (int i = 0; i < 4; ++i) { d0 = mf16(cf[i], ax[i], d0); d1 = mf16(cf[4 + i], ax[i], d1); }
            d0 = mf16(u0, am, d0); d1 = mf16(u1, am, d1);
            lptr pp = part + (s & 1) * 18432;
            *(LAS f32x4*)pp = d0; *(LAS f32x4*)(pp + 64) = d1;
            ST(3);
            bar();
            ST(4);
            if (wave == (s & 7)) {
                f32x4 r0 = {0, 0, 0, 0}, r1 = r0;
                #pragma unroll
                for (int w = 0; w < 8; ++w) { r0 += *(LAS f32x4*)(rsrc + (s & 1) * 18432 + w * 2304); r1 += *(LAS f32x4*)(rsrc + (s & 1) * 18432 + w * 2304 + 16); }
                u32x4 o;
                o.x = pk2(blo(xr.x) + rnd(blo(ga.x) * rnd(r0[0])), bhi(xr.x) + rnd(bhi(ga.x) * rnd(r0[1])));
                o.y = pk2(blo(xr.y) + rnd(blo(ga.y) * rnd(r0[2])), bhi(xr.y) + rnd(bhi(ga.y) * rnd(r0[3])));
                o.z = pk2(blo(xr.z) + rnd(blo(ga.z) * rnd(r1[0])), bhi(xr.z) + rnd(bhi(ga.z) * rnd(r1[1])));
                o.w = pk2(blo(xr.w) + rnd(blo(ga.w) * rnd(r1[2])), bhi(xr.w) + rnd(bhi(ga.w) * rnd(r1[3])));
                st128(o, rs, WS_X1 + vx, s * (32 * D * 2));
                float s1 = 0, s2 = 0;
                #pragma unroll
                for (int j = 0; j < 4; ++j) { s1 = dot2(o[j], 0x3F803F80u, s1); s2 = dot2(o[j], o[j], s2); }
                s1 += qp<0xB1>(s1); s2 += qp<0xB1>(s2); s1 += qp<0x4E>(s1); s2 += qp<0x4E>(s2);
                st64((u32x2){__float_as_uint(s1), __float_as_uint(s2)}, rs, (lane & 3) ? OOB : WS_LS + mt * 4096 + cg * 128 + (lane >> 2) * 8, s * 8192);
                if (s == NSEG - 1) { drain(); if (lane == 0) st32((unsigned)s, rs, WS_POUT + id * 128, 0); }
            }
            ST(5);
            if (s == 0) TL(2);
        }
    } else if (role == R_F1) {
        u32x4 W[2][20];
        f32x4 cs[2], bs[2];
        const int kg = lane >> 4, t16 = lane & 15, cw = wave & 3, th = wave >> 2;
        #pragma unroll
        for (int mat = 0; mat < 2; ++mat) {
            const rsrc_t rg = mkrs(mat ? a.wu : a.wg, D * F * 2);
            const unsigned vw = lane * (F * 2) + (id * 64 + cw * 16) * 2;
            u32x4 g[10], u[10];
            #pragma unroll
            for (int i = 0; i < 10; ++i) { g[i] = ld_pl(rg, vw, i * 64 * F * 2); u[i] = ld_pl(rg, vw + 16, i * 64 * F * 2); }
            SB();
            lptr wp = stg + lane * 32, rp = stg + (8 * kg + ((lane >> 2) & 3)) * 32 + (lane & 3) * 8;
            const u32x4 ones = {0x3F803F80u, 0x3F803F80u, 0x3F803F80u, 0x3F803F80u};
            cs[mat] = (f32x4){0, 0, 0, 0}; bs[mat] = cs[mat];
            #pragma unroll
            for (int i = 0; i < 10; ++i) {
                lwr(wp, g[i]); lwr(wp + 16, u[i]);
                const float sc = rnd(1.f + b2f(*(LAS u16*)(L + L_MOD + 5120 + (64 * i + lane) * 2)));
                u32x4 gs, us;
                #pragma unroll
                for (int j = 0; j < 4; ++j) { gs[j] = pk2(blo(g[i][j]) * sc, bhi(g[i][j]) * sc); us[j] = pk2(blo(u[i][j]) * sc, bhi(u[i][j]) * sc); }
                lwr(wp + 2048, gs); lwr(wp + 2048 + 16, us);
                #pragma unroll
                for (int k2 = 0; k2 < 2; ++k2) {
                    const u32x2 a0 = trd(rp + k2 * 1024), a1 = trd(rp + k2 * 1024 + 128), b0 = trd(rp + 2048 + k2 * 1024), b1 = trd(rp + 2048 + k2 * 1024 + 128);
                    bs[mat] = mf16((u32x4){a0.x, a0.y, a1.x, a1.y}, lrd(L + L_MOD + 3840 + (64 * i + 32 * k2 + 8 * kg) * 2), bs[mat]);
                    W[mat][2 * i + k2] = (u32x4){b0.x, b0.y, b1.x, b1.y};
                    cs[mat] = mf16(W[mat][2 * i + k2], ones, cs[mat]); }
            }
            SB(); }
        TL(2);
        unsigned vh[5];
        #pragma unroll
        for (int i = 0; i < 5; ++i) { const int q = (wave * 5 + i) * 64 + lane, tok = q / 80; vh[i] = WS_X1 + ((q - tok * 80) ^ (tok & 15)) * 16 + tok * (D * 2); }
        lptr bb[4];
        #pragma unroll
        for (int m = 0; m < 4; ++m) bb[m] = L + (th * 16 + t16) * 1280 + (((4 * m + kg) ^ t16) * 16);
        lptr stat = L + 81920 + th * 4096 + kg * 640 + t16 * 8;
        const int ab = th * 54 + 2 * id + (cw >> 1);
        const unsigned vact = WS_ACT + (ab >> 3) * CH + (ab & 7) * 1024 + ((2 * (cw & 1) + (kg >> 1)) * 16 + t16) * 16 + (kg & 1) * 8;
        const unsigned long long pol = get_pol(rs, lane, dead);
        int seen = -1;
        const unsigned vpo = WS_POUT + (2 * (lane < 20 ? lane : 0) + th) * 128;
        auto ldh = [&](int s) {
            #pragma unroll
            for (int i = 0; i < 5; ++i) __builtin_amdgcn_raw_ptr_buffer_load_lds(rs, (LAS void*)(L + (s & 1) * 40960 + (wave * 5 + i) * 1024), 16, vh[i], s * (32 * D * 2), 0, 16);
            __builtin_amdgcn_raw_ptr_buffer_load_lds(rs, (LAS void*)(L + 81920 + (s & 1) * 8192 + wave * 1024), 16, WS_LS + tid * 16, s * 8192, 0, 16); };
        bar();
        wait_prog(rs, vpo, 0, seen, dead, 8);
        ldh(0);
        for (int s = 0; s < NSEG; ++s) {
            if (s == 2) { ST_RESET; }
            drain();
            ST(0);
            bar();
            ST(1);
            if (tid == 0 && s > 0) st32((unsigned)(s - 1), rs, WS_PF1 + id * 128, 0);
            bool got = s + 1 >= NSEG;
            int pv = (int)__builtin_amdgcn_raw_buffer_load_b32(rs, seen > s + 1 ? OOB : vpo, 0, 16);
            if (seen > s && !got) { ldh(s + 1); got = true; }
            SB();
            const int bo = (s & 1) * 40960;
            f32x2 sp[5];
            #pragma unroll
            for (int i = 0; i < 5; ++i) sp[i] = *(LAS f32x2*)(stat + (s & 1) * 8192 + i * 128);
            u32x4 f[4];
            #pragma unroll
            for (int m = 0; m < 4; ++m) f[m] = lrd(bb[m] + bo);
            SB();
            f32x4 d0 = {0, 0, 0, 0}, d1 = d0;
            float mean, rstd;
            #pragma unroll
            for (int ks = 0; ks < 20; ++ks) {
                d0 = mf16(W[0][ks], f[ks & 3], d0); d1 = mf16(W[1][ks], f[ks & 3], d1);
                if (ks + 4 < 20) f[ks & 3] = lrd(bb[ks & 3] + bo + ((ks + 4) >> 2) * 256);
                if (ks == 0) { const f32x2 t2 = ((sp[0] + sp[1]) + (sp[2] + sp[3])) + sp[4];
                    const float s1 = x32sum(x16sum(t2[0])) * (1.f / D), s2 = x32sum(x16sum(t2[1])) * (1.f / D);
                    const float var = __builtin_fmaxf(s2 - s1 * s1, 0.f);
                    mean = rnd(s1); rstd = 1.f / rnd(__builtin_sqrtf(rnd(rnd(var) + 1e-5f))); }
                else if (ks >= 12) SB(); }
            asm volatile("" : "+v"(d0), "+v"(d1), "+v"(pv));
            ST(2);
            float act[4];
            #pragma unroll
            for (int i = 0; i < 4; ++i) {
                const float g = rnd((d0[i] - mean * cs[0][i]) * rstd + bs[0][i]), u = rnd((d1[i] - mean * cs[1][i]) * rstd + bs[1][i]);
                act[i] = rnd(g * __builtin_amdgcn_rcpf(1.f + __expf(-g))) * u; }
            if (seen <= s + 1 && __builtin_amdgcn_ballot_w64(pv <= s) == 0ull) seen = __builtin_amdgcn_ballot_w64(pv < s + 4) == 0ull ? s + 4 : __builtin_amdgcn_ballot_w64(pv < s + 2) == 0ull ? s + 2 : s + 1;
            asm volatile("" ::: "memory");
            st64((u32x2){pk2(act[0], act[1]), pk2(act[2], act[3])}, rs, vact, FO(s));
            ST(4);
            if (!got) { wait_prog(rs, vpo, s + 1, seen, dead, 2); ldh(s + 1); }
            ST(5);
            if (s == 0) TL(3);
        }
        drain(); bar();
        if (tid == 0) st32((unsigned)(NSEG - 1), rs, WS_PF1 + id * 128, 0);
    } else if (role == R_F2) {
        const int cg = id >> 1, mt = id & 1;
        const int ks0 = wave < 6 ? 7 * wave : 42 + 6 * (wave - 6);
        u32x4 W[28];
        {   const rsrc_t rw = mkrs(a.wd, F * D * 2);
            const unsigned vw = (32 * ks0 + 8 * (lane >> 4) + ((lane >> 2) & 3)) * (D * 2) + cg * 128 + (lane & 3) * 16;
            u32x4 v[28];
            #pragma unroll
            for (int i = 0; i < 28; ++i) v[i] = ld_pl(rw, vw, (i >= 24 && wave >= 6) ? 0 : (32 * (i >> 2) + 4 * (i & 1)) * (D * 2) + ((i >> 1) & 1) * 64);
            __builtin_amdgcn_sched_barrier(0);
            lptr wp = stg + lane * 16, rp = stg + (lane >> 2) * 64 + (lane & 3) * 8;
            #pragma unroll
            for (int i = 0; i < 28; ++i) { const int pi = i & 3;
                if (i >= 24 && wave >= 6) v[i] = (u32x4){0, 0, 0, 0};
                lwr(wp + pi * 1024, v[i]);
                const u32x2 r0 = trd(rp + pi * 1024), r1 = trd(rp + pi * 1024 + 32);
                const int i0 = (i & ~3) + (pi & 2);
                W[i0][2 * (pi & 1)] = r0.x; W[i0][2 * (pi & 1) + 1] = r0.y; W[i0 + 1][2 * (pi & 1)] = r1.x; W[i0 + 1][2 * (pi & 1) + 1] = r1.y; } }
        TL(2);
        bar();
        const int ab = mt * 54 + ks0;
        const unsigned vact = WS_ACT + lane * 16;
        const unsigned long long pol = get_pol(rs, lane, dead);
        const int pf = ((ks0 >> 1) + (lane & 3)) < 27 ? (ks0 >> 1) + (lane & 3) : 26;
        lptr part = L + wave * 4352 + (lane & 15) * 272 + (lane >> 4) * 16;
        lptr rsrc = L + (tid >> 5) * 272 + (tid & 31) * 8;
        const unsigned vx = (mt * 16 + (tid >> 5)) * (D * 2) + cg * 128 + (tid & 31) * 4;
        const unsigned gb = *(LAS unsigned*)(L + L_MOD + 6400 + cg * 128 + (tid & 31) * 4);
        const rsrc_t ro = mkrs(a.out, S * D * 2);
        int seen = -1;
        for (int s = 0; s < NSEG; ++s) {
            wait_prog(rs, WS_PF1 + pf * 128, s, seen, dead, 2);
            u32x4 af[7];
            #pragma unroll
            for (int i = 0; i < 7; ++i) { const int b = ab + ((i == 6 && wave >= 6) ? 0 : i); af[i] = ld_sc1(rs, vact, FO(s) + (b >> 3) * CH + (b & 7) * 1024); }
            const unsigned xr = __builtin_amdgcn_raw_buffer_load_b32(rs, WS_X1 + vx, s * (32 * D * 2), 16);
            f32x4 d[4];
            #pragma unroll
            for (int j = 0; j < 4; ++j) d[j] = (f32x4){0, 0, 0, 0};
            #pragma unroll
            for (int i = 0; i < 7; ++i)
                #pragma unroll
                for (int j = 0; j < 4; ++j) d[j] = mf16(W[4 * i + j], af[i], d[j]);
            #pragma unroll
            for (int j = 0; j < 4; ++j) *(LAS f32x4*)(part + (s & 1) * 34816 + j * 64) = d[j];
            bar();
            f32x2 r = {0, 0};
            #pragma unroll
            for (int w = 0; w < 8; ++w) r += *(LAS f32x2*)(rsrc + (s & 1) * 34816 + w * 4352);
            __builtin_amdgcn_raw_buffer_store_b32(pk2(blo(xr) + rnd(blo(gb) * rnd(r[0])), bhi(xr) + rnd(bhi(gb) * rnd(r[1]))), ro, vx, s * (32 * D * 2), 0);
            if (s == 0) TL(3);
        }
    }
    TL(7);
    ST_OUT;
}

extern "C" void kernel_launch(void* const* d_in, const int* in_sizes, int n_in, void* d_out, int out_size, void* d_ws, size_t ws_size, hipStream_t stream) {
    unsigned char* wsb = (unsigned char*)(((size_t)d_ws + 0x1FFFFF) & ~(size_t)0x1FFFFF);
    Args a{(const u16*)d_in[0], (const u16*)d_in[1], (const u16*)d_in[2], (const u16*)d_in[3], (const u16*)d_in[4], (const u16*)d_in[5],
           (const u16*)d_in[8], (const u16*)d_in[9], (const u16*)d_in[10], (const u16*)d_in[11], (const u16*)d_in[12], (u16*)d_out, wsb, {196, 0, 116, 0, 0, -4, 72, 3, 1, 12, 0, 6, 112, 4, 0, 0}};
    hipMemsetAsync(wsb, 0xFF, WS_CLR_END, stream);
    hipFuncSetAttribute((const void*)mega, hipFuncAttributeMaxDynamicSharedMemorySize, L_TOT);
    mega<<<256, 512, L_TOT, stream>>>(a);
}
```

```cpp
#include <hip/hip_runtime.h>
#include <hip/hip_bf16.h>

typedef unsigned short u16;
typedef __bf16 bf16x8 __attribute__((ext_vector_type(8)));
typedef __bf16 bf16x16 __attribute__((ext_vector_type(16)));
typedef __bf16 bf16x2 __attribute__((ext_vector_type(2)));
typedef short s16x4 __attribute__((ext_vector_type(4)));
typedef float f32x2 __attribute__((ext_vector_type(2)));
typedef float f32x4 __attribute__((ext_vector_type(4)));
typedef float f32x16 __attribute__((ext_vector_type(16)));
typedef unsigned u32x2 __attribute__((ext_vector_type(2)));
typedef unsigned u32x4 __attribute__((ext_vector_type(4)));
typedef unsigned u32x8 __attribute__((ext_vector_type(8)));
typedef __amdgpu_buffer_rsrc_t rsrc_t;
#define LAS __attribute__((address_space(3)))
typedef LAS unsigned char* lptr;

constexpr int S = 2048, D = 640, H = 32, E = 128, HE = 4096, NSEG = 64, F = 1728;
constexpr unsigned WS_FH1 = 0;
#define FS 32
constexpr unsigned WS_FLG = 65536, FSEG = 192 * FS;
constexpr unsigned WS_PVO = WS_FLG, WS_PQK = WS_FLG + 32 * FS, WS_PP = WS_FLG + 64 * FS, WS_POUT = WS_FLG + 96 * FS, WS_PF2 = WS_FLG + 136 * FS, WS_PF1 = WS_FLG + 160 * FS;
#define RC 16
#define RS 16
constexpr int NPG = 2 * RC > RS ? 2 * RC : RS;
constexpr unsigned WS_POL = 22528;
constexpr unsigned WS_MOD = 24576;
constexpr unsigned WS_CLR_END = 32768;
constexpr unsigned WS_SLOT = 2u << 20, SLOT = 2u << 20, CH = 16384;
constexpr unsigned SL_PX = 0, SL_PM = 2 * CH, SL_STP = 2 * CH + 4096, SL_MEM = 3 * CH, SL_U = 5 * CH, SL_END = 45 * CH, SL_CAL = 126 * CH;
#define CFN 0
constexpr unsigned WS_CF = WS_SLOT + (CFN ? 45 * CH : 0), WS_KQ = WS_SLOT + 80 * CH, WS_ACT = WS_SLOT + 112 * CH;
constexpr unsigned WS_H1  = WS_SLOT + NPG * SLOT;
constexpr unsigned WS_AX  = WS_H1 + S * D * 2;
constexpr unsigned WS_AM  = WS_AX + RS * 32 * 2048;
constexpr unsigned WS_X1  = WS_AM + RS * 8 * 2048;
constexpr unsigned WS_LS  = WS_X1 + RS * 32 * D * 2;
constexpr unsigned WS_DBG = WS_LS + RS * 10240;
constexpr unsigned WS_END = WS_DBG + (2 << 20);

__device__ __forceinline__ float b2f(u16 b) { return __uint_as_float((unsigned)b << 16); }
__device__ __forceinline__ float blo(unsigned x) { return __uint_as_float(x << 16); }
__device__ __forceinline__ float bhi(unsigned x) { return __uint_as_float(x & 0xffff0000u); }
__device__ __forceinline__ unsigned pk2(float lo, float hi) { bf16x2 v; v[0] = (__bf16)lo; v[1] = (__bf16)hi; return __builtin_bit_cast(unsigned, v); }
__device__ __forceinline__ float rnd(float f) { return blo(pk2(f, 0.f)); }
__device__ __forceinline__ unsigned nosent(unsigned x) { return x < 0xFFFFFFFEu ? x : 0xFFFFFFFEu; }

struct Args { const u16 *x, *mem0, *Wq, *Wk, *Wv, *Wo, *wg, *wu, *wd, *c, *wm; u16* out; unsigned char* ws; int tp[24]; };
__device__ __forceinline__ void dly(int n) { for (int i = 0; i < n; ++i) __builtin_amdgcn_s_sleep(1); }
__device__ __forceinline__ unsigned long long rtime() { unsigned long long t; asm volatile("s_memrealtime %0\n\ts_waitcnt lgkmcnt(0)" : "=s"(t) :: "memory"); return t; }
constexpr unsigned SPIN_MAX = 200000u, OOB = 0x7FFFF000u;
#define TL(i)
#define ST(i)
#define ST_INIT
#define ST_RESET
#define ST_OUT
#define ST_T(t)
#define LG(k, val)
__device__ __forceinline__ rsrc_t mkrs(const void* p, unsigned bytes) { return __builtin_amdgcn_make_buffer_rsrc((void*)p, 0, (int)bytes, 0x00020000); }
__device__ __forceinline__ u32x4 ld_sc1(rsrc_t r, unsigned voff, unsigned soff) { return __builtin_amdgcn_raw_buffer_load_b128(r, voff, soff, 16); }
__device__ __forceinline__ u32x4 ld_pl(rsrc_t r, unsigned voff, unsigned soff) { return __builtin_amdgcn_raw_buffer_load_b128(r, voff, soff, 0); }
__device__ __forceinline__ void st128(u32x4 v, rsrc_t r, unsigned voff, unsigned soff) { __builtin_amdgcn_raw_buffer_store_b128(v, r, voff, soff, 16); asm volatile("s_nop 1" :: "v"(v)); }
__device__ __forceinline__ void st64(u32x2 v, rsrc_t r, unsigned voff, unsigned soff) { __builtin_amdgcn_raw_buffer_store_b64(v, r, voff, soff, 16); }
__device__ __forceinline__ void st32(unsigned v, rsrc_t r, unsigned voff, unsigned soff) { __builtin_amdgcn_raw_buffer_store_b32(v, r, voff, soff, 16); }
__device__ __forceinline__ unsigned umax3(unsigned a, unsigned b, unsigned c) { unsigned m = a > b ? a : b; return m > c ? m : c; }
__device__ __forceinline__ unsigned mx4(u32x4 v, unsigned m) { return umax3(umax3(v.x, v.y, m), v.z, v.w); }
__device__ __forceinline__ bool all_ok(unsigned m) { return __builtin_amdgcn_ballot_w64(m == 0xFFFFFFFFu) == 0ull; }
__device__ __forceinline__ f32x4 smf(u32x4 a, u32x8 b, f32x4 c, int idx) { return __builtin_amdgcn_smfmac_f32_16x16x64_bf16(__builtin_bit_cast(bf16x8, a), __builtin_bit_cast(bf16x16, b), c, idx, 0, 0); }
__device__ __forceinline__ f32x4 mf16(u32x4 a, u32x4 b, f32x4 c) { return __builtin_amdgcn_mfma_f32_16x16x32_bf16(__builtin_bit_cast(bf16x8, a), __builtin_bit_cast(bf16x8, b), c, 0, 0, 0); }
__device__ __forceinline__ f32x16 mf32(u32x4 a, u32x4 b, f32x16 c) { return __builtin_amdgcn_mfma_f32_32x32x16_bf16(__builtin_bit_cast(bf16x8, a), __builtin_bit_cast(bf16x8, b), c, 0, 0, 0); }
__device__ __forceinline__ float fmx(float a, float b) { return __builtin_elementwise_maximum(a, b); }
__device__ __forceinline__ float fold32(float a, float b) { auto r = __builtin_amdgcn_permlane32_swap(__float_as_uint(a), __float_as_uint(b), false, false); return __uint_as_float(r[0]) + __uint_as_float(r[1]); }
__device__ __forceinline__ float x32max(float a) { auto r = __builtin_amdgcn_permlane32_swap(__float_as_uint(a), __float_as_uint(a), false, false); return fmx(__uint_as_float(r[0]), __uint_as_float(r[1])); }
__device__ __forceinline__ float x32sum(float a) { return fold32(a, a); }
__device__ __forceinline__ float x16max(float a) { auto r = __builtin_amdgcn_permlane16_swap(__float_as_uint(a), __float_as_uint(a), false, false); return fmx(__uint_as_float(r[0]), __uint_as_float(r[1])); }
__device__ __forceinline__ float x16sum(float a) { auto r = __builtin_amdgcn_permlane16_swap(__float_as_uint(a), __float_as_uint(a), false, false); return __uint_as_float(r[0]) + __uint_as_float(r[1]); }
template <int N> __device__ __forceinline__ float ror(float a) { return __int_as_float(__builtin_amdgcn_update_dpp(0, __float_as_int(a), 0x120 + N, 0xf, 0xf, true)); }
template <int C> __device__ __forceinline__ float qp(float a) { return __int_as_float(__builtin_amdgcn_update_dpp(0, __float_as_int(a), C, 0xf, 0xf, true)); }
__device__ __forceinline__ float sum16(float a) { a += ror<8>(a); a += ror<4>(a); a += ror<2>(a); a += ror<1>(a); return a; }
__device__ __forceinline__ void bar() { asm volatile("s_waitcnt lgkmcnt(0)\n\ts_barrier" ::: "memory"); __builtin_amdgcn_s_waitcnt(0xC07F); }
__device__ __forceinline__ void drain() { asm volatile("s_waitcnt vmcnt(0)" ::: "memory"); }
__device__ __forceinline__ u32x2 trd(lptr p) { return __builtin_bit_cast(u32x2, __builtin_amdgcn_ds_read_tr16_b64_v4i16((LAS s16x4*)p)); }
__device__ __forceinline__ u32x4 lrd(lptr p) { return *(LAS u32x4*)p; }
__device__ __forceinline__ void lwr(lptr p, u32x4 v) { *(LAS u32x4*)p = v; }
__device__ __forceinline__ float dot2(unsigned a, unsigned b, float c) { return __builtin_amdgcn_fdot2_f32_bf16(__builtin_bit_cast(bf16x2, a), __builtin_bit_cast(bf16x2, b), c, false); }

__device__ __forceinline__ unsigned ld_fl(rsrc_t rs, unsigned voff, int seg) { asm volatile("" ::: "memory"); return __builtin_amdgcn_raw_buffer_load_b32(rs, voff, seg * FSEG, 16); }
__device__ __forceinline__ bool fl_ok(unsigned v) { const bool r = __builtin_amdgcn_ballot_w64(v != 0u) == 0ull; asm volatile("" ::: "memory"); return r; }
__device__ __forceinline__ void wait_fl(rsrc_t rs, unsigned voff, int seg, int& seen, bool& dead, int slp) {
    if (seen >= seg) return;
    for (unsigned sp = 0;; ++sp) {
        if (fl_ok(ld_fl(rs, voff, seg)) || dead) break;
        if (sp > SPIN_MAX) { dead = true; break; }
        dly(slp);
    }
    seen = seg;
}
__device__ __forceinline__ void wait_ring(rsrc_t rs, unsigned voff, int seg, int& seen, bool& dead) {
    if (seen >= seg) return;
    wait_fl(rs, voff, seg, seen, dead, 8);
}

constexpr int L_A = 0;
constexpr int L_QK = 10240;
constexpr int L_KQX = 14336;
constexpr int L_STG = 49152;
constexpr int L_MOD = 98304;
constexpr int L_TOT = 120320;

#define WAUX 2
template <int KIND> __device__ __forceinline__ unsigned wsoff(int r, int j) {
    const int i = 5 * r + j;
    return KIND == 0 ? (64 * (i >> 2) + 32 * ((i & 3) >> 1) + 2 * (i & 1)) * (HE * 2) : KIND == 1 ? (64 * (r >> 2) + 32 * ((r & 3) >> 1) + (r & 1)) * (D * 2) + j * 256
         : KIND == 2 ? 16 * i * (HE * 2) : (32 * (r >> 1) + 8 * (r & 1)) * (D * 2) + j * 256; }
template <int KIND> __device__ __forceinline__ void wissue(u32x4 (&v)[40], rsrc_t rw, unsigned base, int wq, int lane) {
    const int q = lane >> 4, jj = 4 * (wq & 1) + q;
    const int row = KIND == 0 ? 16 * (wq >> 1) + 8 * (q >> 1) + 4 * (wq & 1) + (q & 1) : KIND == 1 ? 4 * (q >> 1) + 8 * (wq >> 1) + 2 * (wq & 1) + 16 * (q & 1)
                  : KIND == 2 ? 4 * wq + q : 4 * (wq >> 1) + (jj >> 1) + 16 * (jj & 1);
    const unsigned voff = row * ((KIND & 1) ? D * 2 : HE * 2) + base + (lane & 15) * 16;
    #pragma unroll
    for (int i = 0; i < 40; ++i) v[i] = __builtin_amdgcn_raw_buffer_load_b128(rw, voff, wsoff<KIND>(i / 5, i % 5), WAUX);
    __builtin_amdgcn_sched_barrier(0);
}
template <bool WO>
__device__ __forceinline__ void load_w_smf(u32x8 (&W)[20], rsrc_t rw, unsigned base, int wave, int lane, lptr L) {
    const int wq = wave & 3;
    u32x4 v[40];
    wissue<WO ? 1 : 0>(v, rw, base, wq, lane);
    lptr wp = L + (wave >> 2) * 20480 + ((lane >> 2) & 3) * 1024 + (4 * wq + (lane >> 4)) * 64 + (lane & 3) * 16;
    lptr rp = L + (wave >> 2) * 20480 + (WO ? 5 * wq : wq) * 1024 + (lane >> 2) * 64 + (lane & 3) * 8;
    #pragma unroll
    for (int r = 0; r < 8; ++r) {
        #pragma unroll
        for (int j = 0; j < 5; ++j) lwr(wp + (r & 1) * 40960 + j * 4096, v[5 * r + j]);
        bar();
        #pragma unroll
        for (int j = 0; j < 5; ++j) { const int i = 5 * r + j, pi = WO ? r & 3 : i & 3;
            lptr p = rp + (r & 1) * 40960 + j * (WO ? 1024 : 4096);
            const u32x2 r0 = trd(p), r1 = trd(p + 32);
            const int i0 = WO ? 4 * j + (r >> 2) : 2 * (i >> 2), i1 = WO ? i0 + 2 : i0 + 1;
            W[i0][2 * pi] = r0.x; W[i0][2 * pi + 1] = r0.y; W[i1][2 * pi] = r1.x; W[i1][2 * pi + 1] = r1.y; }
    }
    bar();
}
template <bool WO>
__device__ __forceinline__ void load_w_32(u32x4 (&W)[40], rsrc_t rw, unsigned base, int wave, int lane, lptr L) {
    const int wq = wave & 3, G = lane >> 4;
    u32x4 v[40];
    wissue<WO ? 3 : 2>(v, rw, base, wq, lane);
    lptr wp = L + (wave >> 2) * 20480 + ((lane >> 2) & 3) * 1024 + (4 * wq + (lane >> 4)) * 64 + (lane & 3) * 16;
    lptr rp = L + (wave >> 2) * 20480 + (WO ? 5 * wq : wq) * 1024 + (8 * (G >> 1) + ((lane >> 2) & 3)) * 64 + (G & 1) * 32 + (lane & 3) * 8;
    #pragma unroll
    for (int r = 0; r < 8; ++r) {
        #pragma unroll
        for (int j = 0; j < 5; ++j) lwr(wp + (r & 1) * 40960 + j * 4096, v[5 * r + j]);
        bar();
        #pragma unroll
        for (int j = 0; j < 5; ++j) { lptr p = rp + (r & 1) * 40960 + j * (WO ? 1024 : 4096);
            const u32x2 r0 = trd(p), r1 = trd(p + 256);
            W[WO ? 8 * j + r : 5 * r + j] = (u32x4){r0.x, r0.y, r1.x, r1.y}; }
    }
    bar();
}

struct Pc { unsigned g1, g2; int p1, p2; };
__device__ __forceinline__ Pc mkpc(int p1, int p2, int lane) { Pc r; r.p1 = p1; r.p2 = p2; r.g1 = WS_SLOT + SL_MEM + (p1 >> 3) * CH + (p1 & 7) * 1024 + lane * 16; r.g2 = WS_SLOT + SL_MEM + (p2 >> 3) * CH + (p2 & 7) * 1024 + lane * 16; return r; }
template <int NP>
__device__ __forceinline__ void gather_mem(const Args& a, rsrc_t rs, unsigned so, bool fresh, const Pc& pc, lptr A, bool& dead) {
    if (NP == 0) return;
    u32x4 v0 = {~0u, ~0u, ~0u, ~0u}, v1 = {0, 0, 0, 0};
    if (!fresh) { v0 = lrd(A + pc.p1 * 1024); if (NP == 2) v1 = lrd(A + pc.p2 * 1024); }
    for (unsigned sp = 0;; ++sp) {
        unsigned m = mx4(v0, 0); if (NP == 2) m = mx4(v1, m);
        if (all_ok(m) || dead) break;
        if (sp > SPIN_MAX) { dead = true; break; }
        asm volatile("" ::: "memory");
        if (m == 0xFFFFFFFFu) { v0 = ld_sc1(rs, pc.g1, so); if (NP == 2) v1 = ld_sc1(rs, pc.g2, so); }
    }
    lwr(A + pc.p1 * 1024, v0);
    if (NP == 2) lwr(A + pc.p2 * 1024, v1);
}
__device__ __forceinline__ void first_mem(const Args& a, int wave, int l, lptr A) {
    const u16* s = a.mem0 + (2 * ((l >> 2) & 3) + (l & 1)) * D + 16 * (l >> 4) + 8 * ((l >> 1) & 1);
    lwr(A + wave * 1024, *(const u32x4*)(s + 64 * wave));
    if (wave < 2) lwr(A + (wave + 8) * 1024, *(const u32x4*)(s + 64 * (wave + 8)));
}
__device__ __forceinline__ unsigned long long get_pol(rsrc_t rs, int lane, bool& dead) {
    unsigned v;
    for (unsigned sp = 0;; ++sp) { asm volatile("" ::: "memory");
        v = __builtin_amdgcn_raw_buffer_load_b32(rs, lane < NPG ? WS_POL + lane * 4 : OOB, 0, 16);
        if (all_ok(v) || dead) break;
        if (sp > SPIN_MAX) { dead = true; break; }
        dly(8); }
    asm volatile("" ::: "memory");
    return __builtin_amdgcn_ballot_w64(v == 1u);
}
#define PG(p) (((unsigned)(p) << 21) + (((unsigned)(pol >> (p)) & 1u) << 13))
#define SO(t) PG((t) & (RS - 1))
#define FO(t) (SO(t) ^ 8192u)
#define RO(s, odd) (PG(2 * ((unsigned)(s) % RC) + (odd)) ^ 8192u)
#define RI(s) ((s) & (RS - 1))
#define CO(s, odd) (RO(s, odd) ^ (CFN ? 8192u : 0u))
__device__ __forceinline__ unsigned rt32() { return (unsigned)__builtin_amdgcn_s_memrealtime(); }
__device__ __forceinline__ void gate(unsigned target) {
    asm volatile("1:\n\ts_memrealtime vcc\n\ts_waitcnt lgkmcnt(0)\n\ts_sub_i32 vcc_lo, %0, vcc_lo\n\ts_cmp_gt_i32 vcc_lo, 0\n\ts_cbranch_scc1 1b" :: "s"(target) : "vcc", "scc", "memory"); }
template <int N> __device__ __forceinline__ int rori(int a) { return __builtin_amdgcn_update_dpp(0, a, 0x120 + N, 0xf, 0xf, true); }
__device__ __forceinline__ unsigned read_G(rsrc_t rs, unsigned so, int lane, bool& dead) {
    u32x2 v;
    for (unsigned sp = 0;; ++sp) { asm volatile("" ::: "memory");
        v = __builtin_amdgcn_raw_buffer_load_b64(rs, WS_SLOT + SL_STP + (lane < 40 ? lane : 0) * 32, so, 16);
        if (all_ok(v.x > v.y ? v.x : v.y) || dead) break;
        if (sp > SPIN_MAX) { dead = true; break; }
        dly(2); }
    const unsigned ref = rt32();
    int d = max((int)(v.x - ref), (int)(v.y - ref));
    d = max(d, rori<8>(d)); d = max(d, rori<4>(d)); d = max(d, rori<2>(d)); d = max(d, rori<1>(d));
    { auto r = __builtin_amdgcn_permlane16_swap(d, d, false, false); d = max((int)r[0], (int)r[1]); }
    { auto r = __builtin_amdgcn_permlane32_swap(d, d, false, false); d = max((int)r[0], (int)r[1]); }
    d = min(max(d, -100000), 600);
    return ref + (unsigned)__builtin_amdgcn_readfirstlane(d);
}
constexpr int L_FAIL = 63488;
constexpr int L_DUM = 63744;
#define LS2(x0, x1)
#define DG(x)
__device__ __forceinline__ void dma1(rsrc_t rs, lptr d, unsigned vo, unsigned so) { __builtin_amdgcn_raw_ptr_buffer_load_lds(rs, (LAS void*)d, 16, vo, so, 0, 16); }
__device__ __forceinline__ void dma4(rsrc_t rs, lptr d, unsigned vo, unsigned so) {
    __builtin_amdgcn_raw_ptr_buffer_load_lds(rs, (LAS void*)d, 16, vo, so, 0, 16); __builtin_amdgcn_raw_ptr_buffer_load_lds(rs, (LAS void*)d, 16, vo, so, 1024, 16);
    __builtin_amdgcn_raw_ptr_buffer_load_lds(rs, (LAS void*)d, 16, vo, so, 2048, 16); __builtin_amdgcn_raw_ptr_buffer_load_lds(rs, (LAS void*)d, 16, vo, so, 3072, 16); }
template <int NP>
__device__ __forceinline__ void dma_mem(rsrc_t rs, unsigned so, const Pc& pc, lptr L) {
    if (NP > 0) __builtin_amdgcn_raw_ptr_buffer_load_lds(rs, (LAS void*)(L + L_A + pc.p1 * 1024), 16, pc.g1, so, 0, 16);
    if (NP > 1) __builtin_amdgcn_raw_ptr_buffer_load_lds(rs, (LAS void*)(L + L_A + pc.p2 * 1024), 16, pc.g2, so, 0, 16);
}
#define FETCH(NP, GATE) do { const unsigned so_ = SO(t > 0 ? t - 1 : 0); \
    if (__builtin_expect(!again && gated && t > 1, 1)) { tagc = t; GATE; ST(0); bar(); ST(5); dma_mem<NP>(rs, so_, pc, L); } \
    else { if (t > 0) gather_mem<NP>(a, rs, so_, !again, pc, Asrc, dead); tagc = -5; } } while (0)
__device__ __forceinline__ void flag(unsigned m, int t, int lane, lptr L) { *(LAS int*)(L + (m == 0xFFFFFFFFu ? L_FAIL : L_DUM + lane * 4)) = t; }
template <int NP>
__device__ __forceinline__ void check_own(int t, int lane, const Pc& pc, lptr A, lptr L) {
    unsigned m = mx4(lrd(A + pc.p1 * 1024), 0);
    if (NP == 2) m = mx4(lrd(A + pc.p2 * 1024), m);
    flag(m, t, lane, L);
}
__device__ __forceinline__ bool failed(int t, lptr L) { return __builtin_expect(__builtin_amdgcn_readfirstlane(*(LAS int*)(L + L_FAIL)) == t, 0); }
#define SB() __builtin_amdgcn_sched_barrier(0)
#define VPT 1
#define UBAR 3
#define NCAL 4
#define NB1 8
#define NB2 0
#define PH1 2
template <bool B> struct Bc { static constexpr bool value = B; };
struct Nop { __device__ void operator()() const {} };
template <int KIND, class FN = Nop>
__device__ __forceinline__ void proj2(const u32x8 (&W)[20], lptr Asrc, lptr dstp, int idx, int t, const Pc& pc, int lane, lptr L, bool young, unsigned& dg_, FN mid = Nop()) {
    f32x4 e = {0, 0, 0, 0}, o = {0, 0, 0, 0};
    u32x4 af[10], own, own2;
    asm volatile("" : "+v"(e), "+v"(o));
    if (KIND == 1) { drain(); DG(rt32()); }
    if (KIND == 3) { asm volatile("s_waitcnt vmcnt(1)" ::: "memory"); DG(rt32()); }
    if (KIND == 4) { if (young) asm volatile("s_waitcnt vmcnt(8)" ::: "memory"); else drain(); DG(rt32()); }
    bar();
    #pragma unroll
    for (int ks = 0; ks < 4; ++ks) af[ks] = lrd(Asrc + ks * 1024);
    SB();
    #pragma unroll
    for (int ks = 0; ks < PH1; ++ks) { e = smf(af[ks], W[2 * ks], e, idx); o = smf(af[ks], W[2 * ks + 1], o, idx); }
    asm volatile("" : "+v"(e), "+v"(o));
    if (KIND == 2 || KIND == 3) drain();
    if (KIND == 5) asm volatile("s_waitcnt vmcnt(1)" ::: "memory");
    if (KIND == 2 || KIND == 5) DG(rt32());
    bar();
    if (KIND != 4) own = lrd(Asrc + pc.p1 * 1024);
    if (KIND == 3) own2 = lrd(Asrc + pc.p2 * 1024);
    SB();
    #pragma unroll
    for (int ks = 4; ks < 10; ++ks) af[ks] = lrd(Asrc + ks * 1024);
    SB();
    #pragma unroll
    for (int ks = PH1; ks < 4; ++ks) { e = smf(af[ks], W[2 * ks], e, idx); o = smf(af[ks], W[2 * ks + 1], o, idx); }
    SB();
    if (KIND != 4) { asm volatile("" : "+v"(own)); unsigned m = mx4(own, 0); if (KIND == 3) m = mx4(own2, m); flag(m, t, lane, L); }
    #pragma unroll
    for (int ks = 4; ks < 10; ++ks) { e = smf(af[ks], W[2 * ks], e, idx); o = smf(af[ks], W[2 * ks + 1], o, idx); if (ks == 5) { asm volatile("" : "+v"(e), "+v"(o)); SB(); mid(); SB(); } }
    *(LAS unsigned*)dstp = pk2(e[0] + e[2], o[0] + o[2]); *(LAS unsigned*)(dstp + 16) = pk2(e[1] + e[3], o[1] + o[3]);
}
#define PROJ(KIND, GATE, YOUNG) FETCH((KIND == 3 ? 2 : KIND == 4 ? 0 : 1), GATE); proj2<KIND>(W, Asrc, pdst, sidx, t, pc, lane, L, YOUNG, dg_); ST(2); bar(); ST(3); \
        again = failed(tagc, L); if (again) { --t; continue; } DGL;
__device__ __forceinline__ void max3x2(float a, float b, float c, float& r0, float& r1) { asm("v_maximum3_f32 %0, %2, %3, %4\n\tv_maximum3_f32 %1, %2, %3, %4\n\ts_nop 1" : "=&v"(r0), "=&v"(r1) : "v"(a), "v"(b), "v"(c)); }
__device__ __forceinline__ void addx2(float a, float b, float& r0, float& r1) { asm("v_add_f32 %0, %2, %3\n\tv_add_f32 %1, %2, %3\n\ts_nop 1" : "=&v"(r0), "=&v"(r1) : "v"(a), "v"(b)); }
__device__ __forceinline__ void sw16(float& a, float& b) { auto r = __builtin_amdgcn_permlane16_swap(__float_as_uint(a), __float_as_uint(b), false, false); a = __uint_as_float(r[0]); b = __uint_as_float(r[1]); }
__device__ __forceinline__ void sw32(float& a, float& b) { auto r = __builtin_amdgcn_permlane32_swap(__float_as_uint(a), __float_as_uint(b), false, false); a = __uint_as_float(r[0]); b = __uint_as_float(r[1]); }
__device__ __forceinline__ int proj2_dst(int wq, int lane) { const int c = lane & 15; return (2 * wq + (c >> 3)) * 256 + (4 * (lane >> 4) + 2 * ((c >> 2) & 1)) * 16 + (c & 3) * 4; }
__device__ __forceinline__ int tokoff(int tok) { return (4 * (tok >> 1) + (tok & 1)) * 16; }

template <class FN> __device__ __forceinline__ void tile640(const u32x4 (&W)[40], lptr img, int lane, u32x4& o0, u32x4& o1, FN mid) {
    const int tok = lane & 31, kg = lane >> 5;
    f32x16 acc;
    #pragma unroll
    for (int i = 0; i < 16; ++i) acc[i] = 0;
    lptr b[4];
    #pragma unroll
    for (int m = 0; m < 4; ++m) b[m] = img + kg * 512 + m * 1024 + ((tok ^ (2 * m + kg)) * 16);
    u32x4 f[4];
    #pragma unroll
    for (int m = 0; m < 4; ++m) f[m] = lrd(b[m]);
    __builtin_amdgcn_sched_barrier(0);
    #pragma unroll
    for (int ks = 0; ks < 40; ++ks) { acc = mf32(W[ks], f[ks & 3], acc); if (ks + 4 < 40) f[ks & 3] = lrd(b[ks & 3] + ((ks + 4) >> 2) * 4096); mid(ks); __builtin_amdgcn_sched_barrier(0); }
    o0 = (u32x4){pk2(acc[0], acc[8]), pk2(acc[1], acc[9]), pk2(acc[2], acc[10]), pk2(acc[3], acc[11])};
    o1 = (u32x4){pk2(acc[4], acc[12]), pk2(acc[5], acc[13]), pk2(acc[6], acc[14]), pk2(acc[7], acc[15])};
}

constexpr float CS = 0.08838834764831845f * 1.4426950408889634f;
enum { R_P, R_V, R_C, R_OUT, R_QK, R_VO, R_F1, R_F2, R_SP };
#define FSW 1
#define HM 1
#define RMAP 2
__host__ __device__ inline void rolemap(int b, int& role, int& id) {
    const int grp = b & 7, idx8 = b >> 3;
    if (RMAP == 2) {
        if (grp < 4) {
            if (idx8 < 8) { role = R_P; id = HM ? grp * 8 + idx8 : idx8 * 4 + grp; } else if (idx8 < 16) { role = R_V; id = HM ? grp * 8 + idx8 - 8 : (idx8 - 8) * 4 + grp; } else if (idx8 < 26) { role = R_C; id = grp * 10 + idx8 - 16; }
            else if (FSW) { const int k = (idx8 - 26) * 4 + grp; if (k < 23) { role = R_F1; id = k; } else { role = R_SP; id = 0; } }
            else if (idx8 < 31) { role = R_F2; id = (idx8 - 26) * 4 + grp; } else if (grp < 3) { role = R_F1; id = 24 + grp; } else { role = R_SP; id = 0; }
        } else if (idx8 < 10) { role = R_OUT; id = (grp - 4) * 10 + idx8; }
        else { const int wi = (idx8 - 10) * 4 + grp - 4;
            if (wi < 32) { role = R_VO; id = HM ? (wi & 3) * 8 + (wi >> 2) : wi; } else if (wi < 64) { role = R_QK; id = HM ? (wi & 3) * 8 + ((wi - 32) >> 2) : wi - 32; } else if (!FSW) { role = R_F1; id = wi - 64; }
            else if (wi < 84) { role = R_F2; id = wi - 64; } else { role = R_F1; id = wi - 61; } }
        return;
    }
    if (RMAP == 3) {
        if (idx8 < 16) { role = idx8 < 4 ? R_P : idx8 < 8 ? R_QK : idx8 < 12 ? R_V : R_VO; id = (idx8 & 3) * 8 + grp; }
        else if (idx8 < 21) { role = R_C; id = grp * 5 + idx8 - 16; }
        else if (idx8 < 25) { role = R_OUT; id = grp * 4 + idx8 - 21; }
        else if (idx8 < 27 && grp < 4) { role = R_OUT; id = 32 + grp * 2 + idx8 - 25; }
        else { const int k = idx8 < 27 ? (idx8 - 25) * 4 + grp - 4 : 8 + (idx8 - 27) * 8 + grp;
            if (k < 27) { role = R_F1; id = k; } else if (k < 47) { role = R_F2; id = k - 27; } else { role = R_SP; id = 0; } }
        return;
    }
    if (grp == 0) { role = R_P; id = idx8; return; }
    if (grp == 1) { role = R_V; id = idx8; return; }
    if (RMAP == 0) {
        if (grp == 7) { if (idx8 < 27) { role = R_F1; id = idx8; } else { role = R_F2; id = idx8 - 27; } }
        else if (idx8 < 8) { role = R_C; id = (grp - 2) * 8 + idx8; }
        else if (idx8 < 16) { role = R_OUT; id = (grp - 2) * 8 + idx8 - 8; }
        else { const int wi = (idx8 - 16) * 5 + grp - 2;
            if (wi < 32) { role = R_QK; id = wi; } else if (wi < 64) { role = R_VO; id = wi - 32; } else if (wi < 79) { role = R_F2; id = wi - 59; } else { role = R_SP; id = 0; } }
    } else {
        if (grp < 4) {
            if (idx8 < 20) { role = R_C; id = (grp - 2) * 20 + idx8; }
            else if (idx8 < 30) { role = R_F2; id = (idx8 - 20) * 2 + grp - 2; }
            else { const int k = (idx8 - 30) * 2 + grp - 2; if (k < 3) { role = R_QK; id = 29 + k; } else { role = R_SP; id = 0; } }
        } else if (idx8 < 10) { role = R_OUT; id = (grp - 4) * 10 + idx8; }
        else { const int wi = (idx8 - 10) * 4 + grp - 4;
            if (wi < 32) { role = R_VO; id = wi; } else if (wi < 61) { role = R_QK; id = wi - 32; } else { role = R_F1; id = wi - 61; } }
    }
}
__global__ void __launch_bounds__(512, 2) mega(Args a) {
    extern __shared__ __attribute__((aligned(16))) unsigned char smem[];
    lptr L = (lptr)smem;
    const int tid = threadIdx.x, lane = tid & 63, wave = __builtin_amdgcn_readfirstlane(tid >> 6);
    int role, id; rolemap(blockIdx.x, role, id);
    const int hid = role == R_C ? id : role == R_OUT ? 40 + id : role == R_F1 ? 80 + id : role == R_F2 ? 107 + id : role == R_SP ? 127 : -1;
    const rsrc_t rs = mkrs(a.ws, WS_END);
    const int sidx = (lane & 2) ? 0xEEEE : 0x4444;
    bool dead = false;
    lptr stg = L + L_STG + wave * 4096;
    ST_INIT
    TL(0);
    const unsigned tstart = rt32();
    if (hid >= 0) {
        const int row = hid * 16 + wave * 2 + (lane >> 5), l32 = lane & 31;
        if (hid < 120) {
            #pragma unroll
            for (int i = 0; i < 2; ++i) { const unsigned u = (i * 120 + hid) * 8192 + tid * 16;
                st128((u32x4){~0u, ~0u, ~0u, ~0u}, rs, u < SL_END ? u : u < 2 * SL_END ? u - SL_END + SLOT : u < 2 * SL_END + 3 * CH ? u - 2 * SL_END + 2 * SLOT + 2 * CH : OOB, WS_SLOT); } }
        if (hid < 96 && tid < 128) st32(~0u, rs, WS_FLG + (hid * 128 + tid) * FS, 0);
        u32x2 xv[5];
        #pragma unroll
        for (int i = 0; i < 5; ++i) xv[i] = *(const u32x2*)(a.x + row * D + (l32 + 32 * i) * 4);
        if (hid < 120) {
            u32x4 wv[5]; float sv[5];
            #pragma unroll
            for (int i = 0; i < 5; ++i) { const int k = (tid >> 2) + 128 * i; wv[i] = *(const u32x4*)(a.wm + k * 3840 + hid * 32 + (tid & 3) * 8); sv[i] = b2f(a.c[k]); }
            float acc[8] = {0, 0, 0, 0, 0, 0, 0, 0};
            #pragma unroll
            for (int i = 0; i < 5; ++i) { const float s = rnd(sv[i] / (1.f + __expf(-sv[i])));
                #pragma unroll
                for (int j = 0; j < 4; ++j) { acc[2 * j] += s * blo(wv[i][j]); acc[2 * j + 1] += s * bhi(wv[i][j]); } }
            #pragma unroll
            for (int j = 0; j < 8; ++j) { float v = acc[j]; v += ror<8>(v); v += ror<4>(v); v = x16sum(v); acc[j] = x32sum(v); }
            if (lane < 4) { *(LAS f32x4*)(L + wave * 128 + lane * 32) = (f32x4){acc[0], acc[1], acc[2], acc[3]}; *(LAS f32x4*)(L + wave * 128 + lane * 32 + 16) = (f32x4){acc[4], acc[5], acc[6], acc[7]}; }
            drain(); bar();
            if (tid < 16) { f32x2 s = {0, 0};
                #pragma unroll
                for (int w = 0; w < 8; ++w) s += *(LAS f32x2*)(L + w * 128 + tid * 8);
                st32(nosent(pk2(s[0], s[1])), rs, WS_MOD + hid * 64 + tid * 4, 0); }
        }
        float xf[20], s = 0;
        #pragma unroll
        for (int i = 0; i < 5; ++i) { xf[4 * i] = blo(xv[i].x); xf[4 * i + 1] = bhi(xv[i].x); xf[4 * i + 2] = blo(xv[i].y); xf[4 * i + 3] = bhi(xv[i].y); }
        #pragma unroll
        for (int i = 0; i < 20; ++i) s += xf[i];
        const float mean = x16sum(sum16(s)) * (1.f / D);
        float vs = 0;
        #pragma unroll
        for (int i = 0; i < 20; ++i) { const float d = xf[i] - mean; vs += d * d; }
        const float var = x16sum(sum16(vs)) * (1.f / D);
        const float mb = rnd(mean), rd = 1.f / rnd(__builtin_sqrtf(rnd(rnd(var) + 1e-5f)));
        #pragma unroll
        for (int i = 0; i < 20; ++i) xf[i] = rnd(rnd(xf[i] - mb) * rd);
        {   u32x4 mv; const unsigned mo = WS_MOD + (tid < 480 ? tid : 479) * 16;
            for (unsigned sp = 0;; ++sp) { asm volatile("" ::: "memory"); mv = ld_sc1(rs, mo, 0); if (all_ok(mx4(mv, 0))) break; if (sp > SPIN_MAX) { dead = true; break; } dly(2); }
            if (tid < 480) lwr(L + L_MOD + tid * 16, mv); }
        bar();
        #pragma unroll
        for (int i = 0; i < 5; ++i) { const u32x2 sa = *(LAS u32x2*)(L + L_MOD + (l32 + 32 * i) * 8), ca = *(LAS u32x2*)(L + L_MOD + 1280 + (l32 + 32 * i) * 8);
            u32x2 o;
            o.x = pk2(rnd(xf[4 * i] * rnd(1.f + blo(ca.x))) + blo(sa.x), rnd(xf[4 * i + 1] * rnd(1.f + bhi(ca.x))) + bhi(sa.x));
            o.y = pk2(rnd(xf[4 * i + 2] * rnd(1.f + blo(ca.y))) + blo(sa.y), rnd(xf[4 * i + 3] * rnd(1.f + bhi(ca.y))) + bhi(sa.y));
            st64(o, rs, WS_H1 + row * (D * 2) + (l32 + 32 * i) * 8, 0); }
        drain(); bar();
        if (tid == 0) st32(0u, rs, WS_FH1 + hid * 4, 0);
        TL(1);
        if (hid < NPG / 2) {
            const unsigned vo = lane < 32 ? WS_SLOT + SL_CAL + (wave & 1) * 4096 + ((wave >> 1) & 1) * CH + lane * 4 : OOB;
            unsigned m[2] = {~0u, ~0u};
            for (int r = 0; r < NCAL; ++r)
                #pragma unroll
                for (int q = 0; q < 2; ++q) { unsigned long long t0, t1;
                    asm volatile("s_waitcnt vmcnt(0)\n\ts_memtime %0\n\ts_waitcnt lgkmcnt(0)" : "=s"(t0) :: "memory");
                    st32(0u, rs, vo, (hid * 2 + (wave >> 2)) * SLOT + q * 8192);
                    asm volatile("s_waitcnt vmcnt(0)\n\ts_memtime %0\n\ts_waitcnt lgkmcnt(0)" : "=s"(t1) :: "memory");
                    m[q] = min(m[q], (unsigned)(t1 - t0)); }
            *(LAS u32x2*)(L + wave * 8) = (u32x2){m[0], m[1]};
            bar();
            if (wave == 0) { const u32x2 mm = *(LAS u32x2*)(L + (lane & 7) * 8);
                const unsigned bm = (unsigned)__builtin_amdgcn_ballot_w64(mm.y < mm.x);
                if (lane == 0) st64((u32x2){__builtin_popcount(bm & 7u) > 1 ? 1u : 0u, __builtin_popcount(bm & 0x70u) > 1 ? 1u : 0u}, rs, WS_POL + hid * 8, 0); }
            bar();
            TL(4);
        }
    }
    const bool gated = a.tp[0] > 0;
    const unsigned DLT = a.tp[0], DU = a.tp[2];
    int tagc = 0; bool again = false; unsigned nfail = 0, dg_ = 0;
#define DGL
    if (role == R_P) {
        const int h = id;
        u32x8 W[20];
        load_w_smf<false>(W, mkrs(wave < 4 ? a.Wq : a.Wk, D * HE * 2), h * 256, wave, lane, L);
        TL(1);
        if (tid == 0) *(LAS int*)(L + L_FAIL) = -1;
        lptr Asrc = L + L_A + lane * 16;
        lptr pdst = L + L_QK + (wave >> 2) * 2048 + proj2_dst(wave & 3, lane);
        first_mem(a, wave, lane, Asrc);
        const unsigned long long pol = get_pol(rs, lane, dead);
        asm volatile("" : "+v"(pdst));
        ST(0);
        if (wave == 0) {
            const int rho = lane & 15, g = lane >> 4;
            const Pc pc = mkpc(4, 4, lane);
            const int key = (((rho >> 2) & 1) << 3) | (((rho >> 1) & 1) << 2) | (((rho >> 3) & 1) << 1) | (rho & 1);
            lptr qsrc = L + L_QK + (g >> 1) * 256 + (g & 1) * 32 + tokoff(rho & 7);
            lptr msrc = L + L_QK + 2048 + (g >> 1) * 256 + (g & 1) * 32 + tokoff(2 * (rho >> 2) + (rho & 1));
            lptr ksrc = L + L_KQX + (g * 32 + key) * 16;
            asm volatile("" : "+v"(qsrc), "+v"(msrc));
            const bool ch = (lane & 8) != 0;
            const unsigned vpx = WS_SLOT + SL_PX + (h >> 4) * CH + (h & 15) * 512 + ((lane >> 3) & 1) * 256 + (g >> 1) * 32 + tokoff(lane & 7) + (g & 1) * 8;
            const unsigned vpm = ch ? OOB : WS_SLOT + SL_PM + ((lane >> 2) & 1) * 2048 + (h >> 1) * 128 + tokoff(lane & 3) + (g & 1) * 32 + (h & 1) * 8 + (g >> 1) * 4;
            unsigned long long rt0 = 0;
            for (int t = 0; t < NSEG; ++t) {
                ST_T(t); if (t == 2) { ST_RESET; }
                u32x4 k0[4], k1[4];
                int fl;
                unsigned so = SO(t);
                lptr kp = ksrc + (t % 3) * 16384;
                asm volatile("" : "+s"(so), "+v"(kp));
                auto ldk = [&]() {
                    #pragma unroll
                    for (int s = 0; s < 4; ++s) { k0[s] = lrd(kp + s * 2048); k1[s] = lrd(kp + s * 2048 + 256); } };
                auto scores = [&]() -> bool {
                    u32x4 q[4], km[4];
                    #pragma unroll
                    for (int s = 0; s < 4; ++s) { q[s] = lrd(qsrc + s * 512); SB(); }
                    #pragma unroll
                    for (int s = 0; s < 4; ++s) { km[s] = lrd(msrc + s * 512); SB(); }
                    fl = *(LAS int*)(L + L_FAIL);
                    SB();
                    f32x4 sm = {0, 0, 0, 0}, s0 = sm, s1 = sm;
                    #pragma unroll
                    for (int s = 0; s < 4; ++s) { s0 = mf16(k0[s], q[s], s0); s1 = mf16(k1[s], q[s], s1); }
                    asm volatile("" : "+v"(s0), "+v"(s1), "+v"(km[0]), "+v"(fl));
                    #pragma unroll
                    for (int s = 0; s < 4; ++s) sm = mf16(km[s], q[s], sm);
                    asm volatile("" : "+v"(sm));
                    if (__builtin_expect(__builtin_amdgcn_readfirstlane(fl) == tagc, 0)) return true;
                    const float mx = fmx(fmx(fmx(fmx(s0[0], s0[1]), s0[2]), fmx(fmx(s0[3], s1[0]), s1[1])), fmx(s1[2], s1[3]));
                    float v0 = ch ? s1[0] : s0[0], v1 = ch ? s1[1] : s0[1], v2 = ch ? s1[2] : s0[2], v3 = ch ? s1[3] : s0[3];
                    SB();
                    float ma = fmx(fmx(mx, sm[0]), sm[1]), mb = fmx(fmx(mx, sm[1]), sm[0]), m2, m3;
                    float vm = ch ? sm[1] : sm[0];
                    sw16(ma, mb); max3x2(ma, mb, mb, m2, m3); sw32(m2, m3);
                    const float mc = fmx(m2, m3) * CS;
                    v0 = __builtin_amdgcn_exp2f(v0 * CS - mc); v1 = __builtin_amdgcn_exp2f(v1 * CS - mc); v2 = __builtin_amdgcn_exp2f(v2 * CS - mc); v3 = __builtin_amdgcn_exp2f(v3 * CS - mc); vm = __builtin_amdgcn_exp2f(vm * CS - mc);
                    float sa, sb2, sc, sd;
                    addx2((v0 + v1) + v2, v3 + vm, sa, sb2); sw16(sa, sb2); addx2(sa, sb2, sc, sd); sw32(sc, sd);
                    float sum = sc + sd;
                    const float vmo = ror<8>(vm);
                    sum += ror<8>(sum);
                    const float inv = __builtin_amdgcn_rcpf(sum);
                    u32x2 px; px.x = pk2(v0 * inv, v1 * inv); px.y = pk2(v2 * inv, v3 * inv);
                    st64(px, rs, vpx, so);
                    st32(pk2(vm * inv, vmo * inv), rs, vpm, so);
                    return false; };
                FETCH(1, );
                proj2<2>(W, Asrc, pdst, sidx, t, pc, lane, L, false, dg_, ldk);
                ST(2);
                bar();
                ST(3);
                again = scores();
                if (again) { --t; ++nfail; continue; }
                ST(4); LG(0, rt32()); DGL;
            }
        } else if (wave == 1) {
            const int r32 = lane & 31, hf = lane >> 5;
            const int prow = (r32 & 19) | ((r32 & 4) << 1) | ((r32 & 8) >> 1);
            lptr qsrc = L + L_KQX + 8192 + (hf * 32 + r32) * 16;
            lptr ksrc = L + L_KQX + (hf * 32 + prow) * 16;
            lptr msrc = L + L_QK + 2048 + hf * 32 + tokoff(prow & 7);
            const unsigned vax = WS_AX + (h * 2 + (r32 >> 4)) * 1024 + (hf * 16 + (r32 & 15)) * 16;
            const unsigned vax1 = r32 < 16 ? OOB : vax + 512;
            const unsigned vam = WS_AM + ((h >> 2) * 2 + (r32 >> 4)) * 1024 + ((h & 3) * 16 + (r32 & 15)) * 16;
            const int lim = r32 - 8 * hf;
            const Pc pc = mkpc(5, 5, lane);
            for (int t = 0; t < NSEG; ++t) {
                ST_T(t); if (t == 2) { ST_RESET; }
                FETCH(1, );
                if (tagc < 0) drain(); else asm volatile("s_waitcnt vmcnt(1)" ::: "memory");
                st32(0u, rs, (lane || again || t == 0) ? OOB : WS_PP + h * FS - FSEG, t * FSEG);
                proj2<5>(W, Asrc, pdst, sidx, t, pc, lane, L, false, dg_); bar();
                again = failed(tagc, L); if (again) { --t; continue; } DGL;
                f32x16 sx, sm;
                #pragma unroll
                for (int i = 0; i < 16; ++i) { sx[i] = 0; sm[i] = 0; }
                const int po = (t % 3) * 16384;
                #pragma unroll 2
                for (int s = 0; s < 8; ++s) {
                    u32x4 q = lrd(qsrc + po + s * 1024), k = lrd(ksrc + po + s * 1024), km = lrd(msrc + s * 256);
                    sx = mf32(k, q, sx); sm = mf32(km, q, sm);
                }
                float m = sm[0];
                #pragma unroll
                for (int j = 1; j < 8; ++j) m = fmx(m, sm[j]);
                #pragma unroll
                for (int i = 0; i < 16; ++i) { if (16 * (i >> 3) + (i & 7) > lim) sx[i] = -3.0e38f; m = fmx(m, sx[i]); }
                m = x32max(m);
                const float mc = m * CS; float sum = 0, summ = 0;
                #pragma unroll
                for (int i = 0; i < 16; ++i) { sx[i] = __builtin_amdgcn_exp2f(sx[i] * CS - mc); sum += sx[i]; }
                #pragma unroll
                for (int j = 0; j < 8; ++j) { sm[j] = __builtin_amdgcn_exp2f(sm[j] * CS - mc); summ += sm[j]; }
                sum = x32sum(sum) + summ;
                const float inv = __builtin_amdgcn_rcpf(sum);
                #pragma unroll
                for (int p = 0; p < 2; ++p) { u32x4 o; o.x = pk2(sx[8 * p] * inv, sx[8 * p + 1] * inv); o.y = pk2(sx[8 * p + 2] * inv, sx[8 * p + 3] * inv); o.z = pk2(sx[8 * p + 4] * inv, sx[8 * p + 5] * inv); o.w = pk2(sx[8 * p + 6] * inv, sx[8 * p + 7] * inv);
                    st128(o, rs, p ? vax1 : vax, RI(t) * 65536); }
                if (hf == 0) { u32x4 o; o.x = pk2(sm[0] * inv, sm[1] * inv); o.y = pk2(sm[2] * inv, sm[3] * inv); o.z = pk2(sm[4] * inv, sm[5] * inv); o.w = pk2(sm[6] * inv, sm[7] * inv);
                    st128(o, rs, vam, RI(t) * 16384); }
                ST(4);
            }
            drain();
            if (lane == 0) st32(0u, rs, WS_PP + h * FS, (NSEG - 1) * FSEG);
        } else if (wave < 4) {
            const unsigned vsrc = WS_KQ + h * CH + lane * 16;
            lptr ldst = L + L_KQX + (wave - 2) * 8192;
            int seen = -1, nf = 1; unsigned pv = 1;
            const Pc pc = mkpc(0, 0, lane);
            auto fetch = [&](int t) {
                wait_fl(rs, WS_PQK + h * FS, t, seen, dead, 2);
                const unsigned so = RO(t, wave - 2);
                dma4(rs, ldst + (t % 3) * 16384, vsrc, so); dma4(rs, ldst + (t % 3) * 16384 + 4096, vsrc, so + 4096); };
            fetch(0);
            for (int t = 0; t < NSEG; ++t) {
                ST_T(t); if (t == 2) { ST_RESET; }
                PROJ(4, , nf > t + 1);
                if (fl_ok(pv)) seen = t + 2;
                pv = ld_fl(rs, t + 3 >= NSEG ? OOB : WS_PQK + h * FS, t + 3);
                if (nf == t + 1 && nf < NSEG) { fetch(nf); ++nf; }
                if (nf == t + 2 && nf < NSEG && seen >= nf) { const unsigned so = RO(nf, wave - 2);
                    for (int i = 0; i < 8; ++i) { dma1(rs, ldst + (nf % 3) * 16384 + i * 1024, vsrc, so + i * 1024); dly(a.tp[7]); }
                    ++nf; }
                ST(4);
            }
        } else {
            unsigned tgt = rt32();
            const Pc pc = mkpc(wave - 4, wave + 2, lane);
            const unsigned vcx = WS_SLOT + SL_PX + (h >> 4) * CH + (h & 15) * 512 + lane * 8, vcm = lane < 16 ? WS_SLOT + SL_PM + (lane >> 3) * 2048 + (h >> 1) * 128 + (lane & 7) * 16 + (h & 1) * 8 : OOB;
            for (int t = 0; t < NSEG; ++t) {
                ST_T(t); if (t == 2) { ST_RESET; }
                PROJ(3, gate(tgt), false);
                if (wave == 7) LG(0, tgt);
                if (wave == 6) { st64((u32x2){~0u, ~0u}, rs, vcx, SO(t + 2)); st64((u32x2){~0u, ~0u}, rs, vcm, SO(t + 2)); }
                tgt = (wave == 7 && gated && t > 0) ? read_G(rs, SO(t - 1), lane, dead) + DLT : rt32();
                ST(4);
            }
        }
    } else if (role == R_V) {
        const int h = id;
        u32x8 W[20];
        if (wave < 4) {
            load_w_smf<false>(W, mkrs(a.Wv, D * HE * 2), h * 256, wave, lane, L);
            TL(1);
            if (tid == 0) *(LAS int*)(L + L_FAIL) = -1;
            lptr pdst = L + L_QK + proj2_dst(wave, lane);
            const Pc pc = mkpc(wave, wave, lane);
            lptr Asrc = L + L_A + lane * 16;
            first_mem(a, wave, lane, Asrc);
            const unsigned long long pol = get_pol(rs, lane, dead);
            unsigned tgt = rt32();
            for (int t = 0; t < NSEG; ++t) {
                ST_T(t); if (t == 2) { ST_RESET; }
                PROJ(1, gate(tgt), false);
                tgt = (wave == 3 && gated && t > 0) ? read_G(rs, SO(t - 1), lane, dead) + DLT : rt32();
                ST(4);
            }
        } else {
            load_w_smf<true>(W, mkrs(a.Wo, D * HE * 2), h * E * D * 2, wave, lane, L);
            TL(1);
            const unsigned vu = WS_SLOT + SL_U + (10 * (wave - 4) + 8 + ((lane >> 4) & 1)) * CH + h * 256 + (lane & 15) * 16 + (lane >> 5) * 8;
            const unsigned vw = WS_SLOT + SL_U + (10 * (wave - 4) + (lane >> 4)) * CH + h * 256 + (lane & 15) * 16;
            const Pc pc = mkpc(wave, wave + 2, lane);
            lptr Asrc = L + L_A + lane * 16;
            first_mem(a, wave, lane, Asrc);
            const unsigned long long pol = get_pol(rs, lane, dead);
            ST(0);
            for (int t = 0; t < NSEG; ++t) {
                ST_T(t); if (t == 2) { ST_RESET; }
                f32x4 dd[10];
                int fl;
                unsigned so = SO(t), so2 = so + 4 * CH;
                asm volatile("" : "+s"(so), "+s"(so2));
                auto zero = [&]() {
                    #pragma unroll
                    for (int j = 0; j < 10; ++j) { dd[j] = (f32x4){0, 0, 0, 0}; asm volatile("" : "+v"(dd[j])); } };
                auto ustage = [&]() -> bool {
                    const u32x4 a0 = lrd(L + L_QK + lane * 16); SB();
                    const u32x4 a1 = lrd(L + L_QK + 1024 + lane * 16); SB();
                    fl = *(LAS int*)(L + L_FAIL);
                    SB();
                    unsigned o[10];
                    auto fin = [&](int j) { o[j] = pk2(dd[j][0] + dd[j][2], dd[j][1] + dd[j][3]); };
                    auto s16 = [&](int j) { auto r = __builtin_amdgcn_permlane16_swap(o[j], o[j + 1], false, false); o[j] = r[0]; o[j + 1] = r[1]; };
                    auto s32 = [&](int j) { auto r = __builtin_amdgcn_permlane32_swap(o[j], o[j + 2], false, false); o[j] = r[0]; o[j + 2] = r[1]; };
                    auto m0 = [&](int j) { dd[j] = smf(a0, W[2 * j], dd[j], sidx); };
                    auto m1 = [&](int j) { dd[j] = smf(a1, W[2 * j + 1], dd[j], sidx); };
                    m0(0); m0(1); m0(2); m0(3); SB();
                    m1(0); SB(); const int flu = __builtin_amdgcn_readfirstlane(fl); m1(1); SB(); m1(2); m1(3); asm volatile("" : "+v"(dd[0]), "+v"(dd[1]), "+v"(dd[2]), "+v"(dd[3])); SB();
                    if (__builtin_expect(flu == tagc, 0)) return true;
                    m0(4); fin(0); SB(); m0(5); fin(1); SB(); m0(6); fin(2); SB(); m0(7); s16(0); SB();
                    m1(4); fin(3); SB(); m1(5); SB(); m1(6); s16(2); SB(); m1(7); SB();
                    m0(8); s32(0); s32(1); SB(); m0(9); fin(4); SB();
                    m1(8); st128((u32x4){o[0], o[1], o[2], o[3]}, rs, vw, so); fin(5); SB(); m1(9); fin(6); SB();
                    fin(7); s16(4); fin(8); fin(9); s16(6); s16(8); s32(4); s32(5);
                    st64((u32x2){o[8], o[9]}, rs, vu, so);
                    st128((u32x4){o[4], o[5], o[6], o[7]}, rs, vw, so2);
                    return false; };
                zero();
                if (wave >= 6) { FETCH(2, ); bar(); drain(); bar(); check_own<2>(t, lane, pc, Asrc, L); } else { FETCH(1, ); bar(); drain(); bar(); check_own<1>(t, lane, pc, Asrc, L); }
                bar();
                ST(3);
                again = ustage();
                if (again) { --t; continue; }
                ST(4); LG(0, rt32());
                dly(a.tp[9]);
                const unsigned sc = SO(t + 2);
                st128((u32x4){~0u, ~0u, ~0u, ~0u}, rs, vw, sc); dly(a.tp[13]); st128((u32x4){~0u, ~0u, ~0u, ~0u}, rs, vw, sc + 4 * CH); dly(a.tp[13]);
                st64((u32x2){~0u, ~0u}, rs, vu, sc);
            }
        }
    } else if (role == R_C) {
      const int c = id;
      LAS unsigned* gbox = (LAS unsigned*)(L + 98304);
      constexpr int L_CU = 98560;
      const unsigned long long pol = get_pol(rs, lane, dead);
      const unsigned cmem = WS_SLOT + SL_MEM + (c >> 5) * CH + (c & 31) * 256;
      if (wave >= 4) {
        const unsigned vcf = WS_CF + ((c % 20) * 4 + wave - 4) * CH + lane * 16;
        int seen = -1, nf = 1;
        const unsigned vpv = WS_PVO + (8 * (wave - 4) + (lane & 7)) * FS;
        lptr cdst = L + (wave - 4) * 8192;
        auto ldcf = [&](int t) {
            wait_fl(rs, vpv, t, seen, dead, 2);
            const unsigned so = CO(t, c >= 20);
            dma4(rs, cdst + (t % 3) * 32768, vcf, so); dma4(rs, cdst + (t % 3) * 32768 + 4096, vcf, so + 4096); };
        const unsigned vclr = wave != 7 ? OOB : lane < 32 ? cmem + lane * 8 : lane == 32 ? WS_SLOT + SL_STP + c * 32 : OOB;
        ldcf(0); drain(); bar();
        for (int t = 0;; ++t) {
            if (seen < t + 2 && t + 2 < NSEG - 1) { const unsigned f1 = ld_fl(rs, vpv, nf), f2 = ld_fl(rs, vpv, t + 2); if (fl_ok(f2)) seen = t + 2; else if (fl_ok(f1)) seen = max(seen, nf); }
            if (nf == t + 1 && nf < NSEG - 1 && seen >= nf) { ldcf(nf); ++nf; }
            if (t > 0) st64((u32x2){~0u, ~0u}, rs, vclr, SO(t + 2));
            if (nf == t + 2 && nf < NSEG - 1 && seen >= nf) {
                const unsigned so = CO(nf, c >= 20);
                lptr d = cdst + (nf % 3) * 32768;
                unsigned pv = ld_fl(rs, vpv, t + 3);
                #pragma unroll
                for (int i = 0; i < NB1; ++i) { dma1(rs, d + i * 1024, vcf, so + i * 1024); if (i + 1 < NB1) dly(a.tp[8]); }
                DG(rt32());
                bar();
                #pragma unroll
                for (int i = NB1; i < NB1 + NB2; ++i) { dma1(rs, d + i * 1024, vcf, so + i * 1024); if (i + 1 < NB1 + NB2) dly(4); }
                bar();
                #pragma unroll
                for (int i = NB1 + NB2; i < 8; ++i) dma1(rs, d + i * 1024, vcf, so + i * 1024);
                asm volatile("" : "+v"(pv));
                if (fl_ok(pv)) seen = t + 3;
                asm volatile("s_waitcnt vmcnt(8)" ::: "memory");
                ++nf;
            } else {
                DG(rt32());
                bar();
                bar();
                if (t == NSEG - 2) break;
                if (nf == t + 1) { ldcf(nf); ++nf; }
                drain();
            }
            bar();
            DGL;
        }
      } else if (wave >= 2) {
        const unsigned vu = WS_SLOT + SL_U + c * CH + (wave - 2) * 4096 + lane * 16;
        unsigned G = rt32() - 100000u;
        *gbox = G;
        bar();
        for (int t = 0;; ++t) {
            const unsigned so = SO(t);
            unsigned d2_ = 0;
            DG(rt32());
            gate(G + DU);
            bar();
            dma4(rs, L + L_CU + (wave - 2) * 4096, vu, so);
            drain();
            bar();
            if (t == NSEG - 2) break;
            bar();
            if (wave == 2 && gated) {
                dly(a.tp[11]);
                G = read_G(rs, so, lane, dead);
                *gbox = G; }
            if (wave == 3 && c == 0 && t + 4 >= RS) { int sn = -1; wait_fl(rs, WS_PF2 + (lane < 20 ? lane : 0) * FS, t + 4 - RS, sn, dead, 4); }
        }
      } else {
        const int rh = wave, rho = lane & 15, kg = lane >> 4;
        u32x4 cf[32];
        auto ldcf = [&](int t) {
            #pragma unroll
            for (int i = 0; i < 32; ++i) cf[i] = lrd(L + (t % 3) * 32768 + lane * 16 + i * 1024); };
        bar();
        ldcf(0);
        const unsigned opx = WS_SLOT + SL_PX + (2 * (rho >> 3) + (kg >> 1)) * 512 + (kg & 1) * 256 + rh * 128 + (rho & 7) * 16;
        const unsigned opm = WS_SLOT + SL_PM + rh * 2048 + (4 * (rho >> 3) + kg) * 128 + (rho & 7) * 16;
        const unsigned ou = WS_SLOT + SL_U + c * CH + lane * 16;
        const unsigned vmem = (lane & 1) ? OOB : cmem + rh * 128 + (4 * (kg & 1) + 2 * (rho >> 3) + (lane >> 5)) * 16 + (rho & 7) * 2;
        const unsigned vstp = lane ? OOB : WS_SLOT + SL_STP + c * 32 + rh * 4;
        const bool hi = lane >= 32;
        unsigned nlate = 0;
        for (int t = 0; t < NSEG - 1; ++t) {
            ST_T(t); if (t == 2) { ST_RESET; }
            const unsigned sb = SO(t);
            u32x4 p[10], u[8];
            f32x4 ca, cb;
            unsigned res; float v;
            auto ldu = [&]() {
                #pragma unroll
                for (int i = 0; i < 8; ++i) u[i] = ld_sc1(rs, ou + (i & 3) * 1024, sb + (i >> 2) * 4096); };
            auto ldp = [&]() {
                #pragma unroll
                for (int i = 0; i < 8; ++i) { p[i] = ld_sc1(rs, opx + (i & 1) * 2048, sb + (i >> 2) * CH + ((i >> 1) & 1) * 4096); __builtin_amdgcn_sched_barrier(0); }
                p[8] = ld_sc1(rs, opm, sb); __builtin_amdgcn_sched_barrier(0); p[9] = ld_sc1(rs, opm + 1024, sb); };
            auto comb = [&](bool first) {
                #pragma unroll
                for (int f = 0; f < 8; ++f) {
                    ca = smf(p[f], __builtin_shufflevector(cf[4 * f], cf[4 * f + 1], 0, 1, 2, 3, 4, 5, 6, 7), ca, sidx);
                    cb = smf(p[f], __builtin_shufflevector(cf[4 * f + 2], cf[4 * f + 3], 0, 1, 2, 3, 4, 5, 6, 7), cb, sidx);
                    if (first && f == UBAR) { asm volatile("" : "+v"(ca), "+v"(cb)); bar();
                        #pragma unroll
                        for (int i = 0; i < 8; ++i) u[i] = lrd(L + L_CU + lane * 16 + i * 1024); }
                    __builtin_amdgcn_sched_barrier(0); }
                #pragma unroll
                for (int f = 0; f < 2; ++f) {
                    ca = smf(p[8 + f], __builtin_shufflevector(u[4 * f], u[4 * f + 1], 0, 1, 2, 3, 4, 5, 6, 7), ca, sidx);
                    cb = smf(p[8 + f], __builtin_shufflevector(u[4 * f + 2], u[4 * f + 3], 0, 1, 2, 3, 4, 5, 6, 7), cb, sidx);
                    __builtin_amdgcn_sched_barrier(0); }
                const float t0 = hi ? cb[0] + cb[2] : ca[0] + ca[2], t1 = hi ? cb[1] + cb[3] : ca[1] + ca[3];
                v = fold32(t0, t1);
                res = pk2(v, __int_as_float(__builtin_amdgcn_update_dpp(0, __float_as_int(v), 0xB1, 0xf, 0xf, true)));
                asm volatile("" : "+v"(res)); };
            ca = (f32x4){0, 0, 0, 0}; cb = ca;
            asm volatile("" : "+v"(ca), "+v"(cb));
            bar();
            ST(5);
            ldp();
            const unsigned G = __builtin_amdgcn_readfirstlane(*gbox);
            __builtin_amdgcn_sched_barrier(0);
            comb(true);
            if (__builtin_expect(__builtin_amdgcn_ballot_w64(v != v) != 0ull, 0)) {
                for (unsigned sp = 0;; ++sp) {
                    unsigned m = 0, mu = 0;
                    #pragma unroll
                    for (int i = 0; i < 10; ++i) m = mx4(p[i], m);
                    #pragma unroll
                    for (int i = 0; i < 8; ++i) mu = mx4(u[i], mu);
                    if (all_ok(m > mu ? m : mu) || dead) break;
                    if (sp == 0) nfail += all_ok(mu) ? 1 : 0x10000;
                    if (sp > SPIN_MAX) dead = true;
                    asm volatile("" ::: "memory");
                    if (mu == 0xFFFFFFFFu) ldu();
                    if (m == 0xFFFFFFFFu) ldp(); }
                ca = (f32x4){0, 0, 0, 0}; cb = ca;
                comb(false); }
            ST(0);
            st32(res, rs, vmem, sb);
            ST(3);
            const unsigned now = rt32();
            LG(0, now); LG(1, G);
            unsigned Sn = G + DLT;
            const int lt = (int)(now - Sn);
            if (lt > a.tp[5]) { ++nlate; Sn += max((lt + a.tp[6] + 3) & ~3, a.tp[12]); if (t == 0) Sn &= ~3u; }
            st32(nosent(Sn), rs, vstp, sb);
            if (t == NSEG - 2) break;
            bar();
            ldcf(t + 1);
            ST(4);
            if (t == 0) TL(2);
        }
      }
    } else if (role == R_QK || role == R_VO) {
        const int h = id; const bool vo = role == R_VO, st2 = vo && wave >= 4;
        u32x4 W[40];
        if (st2) load_w_32<true>(W, mkrs(a.Wo, D * HE * 2), h * E * D * 2, wave, lane, L);
        else load_w_32<false>(W, mkrs(vo ? a.Wv : wave < 4 ? a.Wq : a.Wk, D * HE * 2), h * 256, wave, lane, L);
        TL(1);
        const unsigned vh = WS_H1 + (tid >> 4) * (D * 2) + (tid & 15) * 16;
        lptr wdst = L + (tid & 15) * 512 + (((tid >> 4) ^ (tid & 7)) * 16);
        lptr vbuf = L + 81920;
        bool allh = false;
        int seen = -1;
        u32x4 hv[5];
        auto waith = [&](int s) {
            if (!allh) for (unsigned sp = 0;; ++sp) { asm volatile("" ::: "memory");
                const u32x2 f = __builtin_amdgcn_raw_buffer_load_b64(rs, WS_FH1 + lane * 8, 0, 16);
                const unsigned long long bad = __builtin_amdgcn_ballot_w64((f.x | f.y) != 0u);
                allh = bad == 0ull;
                if (!((bad >> s) & 1ull) || dead) break;
                if (sp > SPIN_MAX) { dead = true; break; }
                dly(2); }
            asm volatile("" ::: "memory"); };
        waith(0);
        #pragma unroll
        for (int i = 0; i < 5; ++i) hv[i] = ld_sc1(rs, vh + i * 256, 0);
        waith(1);
        #pragma unroll
        for (int i = 0; i < 5; ++i) { lwr(wdst + i * 8192, hv[i]); hv[i] = ld_sc1(rs, vh + i * 256, 32 * D * 2); }
        bar();
        const unsigned long long pol = get_pol(rs, lane, dead);
        auto stage = [&](int s, int i) { lwr(wdst + ((s + 1) & 1) * 40960 + i * 8192, hv[i]); hv[i] = ld_sc1(rs, vh + i * 256, (s + 2 < NSEG ? s + 2 : NSEG - 1) * (32 * D * 2)); };
        if (!vo) {
            const unsigned vdst = WS_KQ + h * CH + (wave & 3) * 2048 + lane * 16;
            for (int s = 0; s < NSEG; ++s) {
                if (s == 1) { ST_RESET; }
                if (s + 2 < NSEG) waith(s + 2);
                ST(0);
                u32x4 o0, o1;
                const unsigned pr = ld_fl(rs, (wave || s < RC - 1) ? OOB : WS_PP + h * FS, max(s - (RC - 1), 0));
                tile640(W, L + (s & 1) * 40960, lane, o0, o1, [&](int ks) { if (ks % 6 == 2 && ks < 30) stage(s, ks / 6); });
                asm volatile("s_waitcnt vmcnt(5)" ::: "memory");
                const unsigned so = RO(s, wave < 4);
                st128(o0, rs, vdst, so); st128(o1, rs, vdst + 1024, so);
                if (!fl_ok(pr)) wait_ring(rs, WS_PP + h * FS, s - (RC - 1), seen, dead);
                ST(1);
                const bool now = s < 3 || s == NSEG - 1;
                if (now) drain();
                ST(3);
                bar();
                ST(4);
                if (wave == 0) { int l = lane; asm volatile("" : "+v"(l)); st32(0u, rs, (l == 0 ? s > 3 : l == 1 && now) ? l * FSEG : OOB, WS_PQK + h * FS + s * FSEG - FSEG); }
                if (s == 0) TL(2);
            }
        } else if (!st2) {
            for (int s = 0; s <= NSEG; ++s) {
                if (s == 1) { ST_RESET; }
                if (s + 2 < NSEG) waith(s + 2);
                ST(0);
                if (s < NSEG) { u32x4 o0, o1;
                    const unsigned pr = ld_fl(rs, (wave != 1 || s < RC) ? OOB : WS_POUT + (lane < 40 ? lane : 0) * FS, max(s - (RC - 1), 0));
                    tile640(W, L + (s & 1) * 40960, lane, o0, o1, [&](int ks) { if (ks % 6 == 2 && ks < 30) stage(s, ks / 6); });
                    lwr(vbuf + (s & 1) * 8192 + wave * 2048 + lane * 16, o0); lwr(vbuf + (s & 1) * 8192 + wave * 2048 + 1024 + lane * 16, o1);
                    if (!fl_ok(pr)) wait_ring(rs, WS_POUT + (lane < 40 ? lane : 0) * FS, s - (RC - 1), seen, dead); }
                ST(1);
                bar();
                ST(4);
                if (tid == 0 && (s == 1 || s == NSEG)) st32(0u, rs, WS_PVO + h * FS, (s - 1) * FSEG);
                if (s == 1) TL(2);
            }
        } else {
            const int r32 = lane & 31, hf = lane >> 5;
            const int prow = (r32 & 19) | ((r32 & 4) << 1) | ((r32 & 8) >> 1);
            lptr asrc = vbuf + hf * 512 + prow * 16;
            __builtin_amdgcn_s_setprio(1);
            const unsigned vd0 = WS_CF + ((10 * (wave & 1) + (r32 >> 4)) * 4 + (h >> 3)) * CH + (h & 7) * 1024 + (hf * 16 + (r32 & 15)) * 16;
            LAS unsigned* cnt = (LAS unsigned*)(L + 98304);
            if (tid == 256) *cnt = 0;
            for (int s = 0; s <= NSEG; ++s) {
                if (s == 1) { ST_RESET; }
                if (s + 2 < NSEG) waith(s + 2);
                ST(0);
                u32x4 af[8];
                #pragma unroll
                for (int ks = 0; ks < 8; ++ks) af[ks] = lrd(asrc + ((s - 1) & 1) * 8192 + ks * 1024);
                const unsigned so = CO(s > 0 ? s - 1 : 0, wave >= 6), vdst = s > 0 ? vd0 : OOB;
                #pragma unroll
                for (int tt = 0; tt < 5; ++tt) {
                    f32x16 acc;
                    #pragma unroll
                    for (int i = 0; i < 16; ++i) acc[i] = 0;
                    if (s > 0) {
                        #pragma unroll
                        for (int ks = 0; ks < 8; ++ks) acc = mf32(af[ks], W[8 * tt + ks], acc);
                    }
                    stage(s, tt);
                    #pragma unroll
                    for (int p = 0; p < 2; ++p) st128((u32x4){pk2(acc[8 * p], acc[8 * p + 1]), pk2(acc[8 * p + 2], acc[8 * p + 3]), pk2(acc[8 * p + 4], acc[8 * p + 5]), pk2(acc[8 * p + 6], acc[8 * p + 7])}, rs, vdst + p * 512, so + tt * (8 * CH));
                    if (tt == VPT && s > 2) { asm volatile("s_waitcnt vmcnt(%0)" :: "n"(3 * VPT + 3) : "memory");
                        if (lane == 0 && (__hip_atomic_fetch_add(cnt, 1u, __ATOMIC_RELAXED, __HIP_MEMORY_SCOPE_WORKGROUP) & 3u) == 3u) st32(0u, rs, WS_PVO + h * FS, (s - 2) * FSEG); }
                }
                ST(1);
                if (s == 1 || s == NSEG) drain();
                ST(3);
                bar();
                ST(4);
            }
        }
    } else if (role == R_OUT) {
        const int c = id;
        const unsigned lo = (lane >> 5) * 1024 + (lane & 31) * 16;
        const unsigned vcf = WS_CF + ((c % 20) * 4 + (wave >> 1)) * CH + (wave & 1) * 4096 + lo;
        const unsigned vax = WS_AX + wave * 8192 + (lane >> 5) * 2048 + (lane & 31) * 16, vam = WS_AM + wave * 2048 + lane * 16;
        const unsigned vu = WS_SLOT + SL_U + c * CH + wave * 1024 + lane * 16;
        const unsigned long long pol = get_pol(rs, lane, dead);
        lptr part = L + wave * 2560 + (lane & 15) * 80 + (lane >> 4) * 16;
        const int tok = 4 * wave + (lane >> 4), col = lane & 15;
        lptr rsrc = L + tok * 80 + col * 4;
        LAS unsigned* cnt = (LAS unsigned*)(L + 49152);
        const unsigned vx = tok * (D * 2) + c * 32 + col * 2, vo = WS_X1 + c * 1024 + wave * 128 + (((col >> 3) ^ (wave >> 1)) & 1) * 64 + (lane >> 4) * 16 + (col & 7) * 2;
        const unsigned vl = col ? OOB : WS_LS + (tok >> 4) * 5120 + c * 128 + (tok & 15) * 8;
        const unsigned vpp = WS_PP + (4 * wave + (lane & 3)) * FS, vpv = WS_PVO + (4 * wave + (lane & 3)) * FS;
        const rsrc_t rx = mkrs(a.x, S * D * 2);
        const float ga = b2f(*(LAS u16*)(L + L_MOD + 2560 + c * 32 + col * 2));
        int seenv = -1, seenp = -1;
        u32x4 cf[4];
        auto ldcf = [&](int s) {
            wait_fl(rs, vpv, s, seenv, dead, 4);
            const unsigned so = CO(s, c >= 20);
            #pragma unroll
            for (int i = 0; i < 4; ++i) cf[i] = ld_sc1(rs, vcf + (i >> 1) * 2048 + (i & 1) * 512, so); };
        auto done = [&](int s) { if (lane == 0 && (__hip_atomic_fetch_add(cnt, 1u, __ATOMIC_RELAXED, __HIP_MEMORY_SCOPE_WORKGROUP) & 7u) == 7u) st32(0u, rs, WS_POUT + id * FS, s * FSEG); };
        if (tid == 0) *cnt = 0;
        ldcf(0);
        for (int s = 0; s < NSEG; ++s) {
            if (s == 2) { ST_RESET; }
            bool pub = s > 0;
            if (seenp < s) {
                if (pub) { drain(); done(s - 1); pub = false; }
                wait_fl(rs, vpp, s, seenp, dead, s < 63 ? a.tp[17] : 2); }
            ST(0);
            DG(rt32());
            u32x4 ax[6], am0, am1, u;
            #pragma unroll
            for (int j = 0; j < 2; ++j) { ax[3 * j] = ld_sc1(rs, vax, RI(s) * 65536 + j * 4096); ax[3 * j + 1] = ld_sc1(rs, vax + 1024, RI(s) * 65536 + j * 4096); ax[3 * j + 2] = ld_sc1(rs, vax + 1536, RI(s) * 65536 + j * 4096); }
            u = ld_sc1(rs, vu, SO(s));
            am0 = ld_sc1(rs, vam, RI(s) * 16384); am1 = ld_sc1(rs, vam + 1024, RI(s) * 16384);
            SB();
            const unsigned pw = ld_fl(rs, s + 2 < NSEG ? vpv : OOB, s + 2);
            const unsigned xr = __builtin_amdgcn_raw_buffer_load_b16(rx, vx, s * (32 * D * 2), 0);
            SB();
            if (pub) { asm volatile("s_waitcnt vmcnt(11)" ::: "memory"); done(s - 1); }
            ST(1);
            f32x4 d0 = {0, 0, 0, 0}, d1 = d0;
            #pragma unroll
            for (int j = 0; j < 2; ++j) { d0 = mf16(cf[2 * j], ax[3 * j], d0); d1 = mf16(cf[2 * j], ax[3 * j + 1], d1); d1 = mf16(cf[2 * j + 1], ax[3 * j + 2], d1); }
            if (__builtin_expect(!all_ok(mx4(u, 0)), 0))
                for (unsigned sp = 0;; ++sp) { asm volatile("" ::: "memory");
                    u = ld_sc1(rs, vu, SO(s));
                    if (all_ok(mx4(u, 0)) || dead) break;
                    if (sp > SPIN_MAX) { dead = true; break; }
                    dly(2); }
            d0 = mf16(u, am0, d0); d1 = mf16(u, am1, d1);
            lptr pp = part + (s & 1) * 20480;
            *(LAS f32x4*)pp = d0; *(LAS f32x4*)(pp + 1280) = d1;
            const unsigned pv = ld_fl(rs, s + 1 < NSEG ? vpp : OOB, s + 1);
            if (fl_ok(pw)) seenv = s + 2;
            if (s + 1 < NSEG) ldcf(s + 1);
            ST(3);
            bar();
            ST(4);
            float q[8];
            #pragma unroll
            for (int w = 0; w < 8; ++w) q[w] = *(LAS float*)(rsrc + (s & 1) * 20480 + w * 2560);
            const unsigned o = pk2(blo(xr) + rnd(ga * rnd(((q[0] + q[1]) + (q[2] + q[3])) + ((q[4] + q[5]) + (q[6] + q[7])))), 0.f);
            __builtin_amdgcn_raw_buffer_store_b16((unsigned short)o, rs, vo, RI(s) * (32 * D * 2), 16);
            const float of = blo(o);
            st64((u32x2){__float_as_uint(sum16(of)), __float_as_uint(sum16(of * of))}, rs, vl, RI(s) * 10240);
            if (fl_ok(pv)) seenp = s + 1;
            LS2(dg_, rt32());
            ST(5);
            if (s == 0) TL(2);
        }
        drain();
        done(NSEG - 1);
    } else if (role == R_F1) {
        u32x4 W[4][10];
        f32x4 cs[2], bs[2];
        const int kg = lane >> 4, t16 = lane & 15, kh = (wave >> 1) & 1, cw = 2 * (wave & 1) + kh, th = wave >> 2;
        gate(tstart + a.tp[23]);
        #pragma unroll
        for (int mat = 0; mat < 2; ++mat) {
            const rsrc_t rg = mkrs(mat ? a.wu : a.wg, D * F * 2);
            const unsigned vw = (wave * 80 + ((lane >> 1) & 7)) * (F * 2) + id * 128 + (lane >> 4) * 32 + (lane & 1) * 16;
            bar();
            #pragma unroll 1
            for (int i = 0; i < 10; ++i) __builtin_amdgcn_raw_ptr_buffer_load_lds(rg, (LAS void*)(L + (wave * 10 + i) * 1024), 16, vw, i * 8 * F * 2, 0, 0);
            drain(); if (mat) TL(6); else TL(4); bar();
            lptr rp = L + kg * 1024 + cw * 256 + t16 * 8;
            const u32x4 ones = {0x3F803F80u, 0x3F803F80u, 0x3F803F80u, 0x3F803F80u};
            cs[mat] = (f32x4){0, 0, 0, 0}; bs[mat] = cs[mat];
            #pragma unroll 4
            for (int ks = 0; ks < 20; ++ks) { const u32x2 a0 = trd(rp + ks * 4096), a1 = trd(rp + ks * 4096 + 128);
                bs[mat] = mf16((u32x4){a0.x, a0.y, a1.x, a1.y}, lrd(L + L_MOD + 3840 + (32 * ks + 8 * kg) * 2), bs[mat]); }
            bar();
            #pragma unroll 2
            for (int i = 0; i < 10; ++i) { lptr q = L + (wave * 10 + i) * 1024 + lane * 16;
                u32x4 w = lrd(q);
                const float sc = rnd(1.f + b2f(*(LAS u16*)(L + L_MOD + 5120 + (wave * 80 + i * 8 + ((lane >> 1) & 7)) * 2)));
                #pragma unroll
                for (int j = 0; j < 4; ++j) w[j] = pk2(blo(w[j]) * sc, bhi(w[j]) * sc);
                lwr(q, w); }
            bar();
            #pragma unroll 4
            for (int ks = 0; ks < 20; ++ks) { const u32x2 a0 = trd(rp + ks * 4096), a1 = trd(rp + ks * 4096 + 128);
                cs[mat] = mf16((u32x4){a0.x, a0.y, a1.x, a1.y}, ones, cs[mat]); }
            #pragma unroll
            for (int j = 0; j < 2; ++j) { lptr r2 = L + kh * 40960 + kg * 1024 + (cw ^ j) * 256 + t16 * 8;
                #pragma unroll
                for (int i = 0; i < 10; ++i) { const u32x2 a0 = trd(r2 + i * 4096), a1 = trd(r2 + i * 4096 + 128); W[2 * j + mat][i] = (u32x4){a0.x, a0.y, a1.x, a1.y}; } }
            if (!mat) TL(5); }
        TL(2);
        lptr bb = L + kh * 20480 + (kg >> 1) * 1024 + (th * 4 + (t16 >> 2)) * 128 + ((kg ^ (t16 >> 3)) & 1) * 64 + (t16 & 3) * 16;
        lptr stat = L + 81920 + wave * 1280 + lane * 8;
        lptr xw = L + 102400 + wave * 2048 + lane * 16, xr = L + 102400 + (wave ^ 2) * 2048 + lane * 16;
        lptr sw = L + 118784 + wave * 128 + t16 * 8, sr = L + 118784 + th * 512 + t16 * 8;
        volatile LAS int* rdy = (volatile LAS int*)(L + 119808);
        LAS unsigned* cnt = (LAS unsigned*)(L + 119812);
        const int ab = th * 54 + 2 * id + (cw >> 1);
        const unsigned vact = WS_ACT + (ab >> 3) * CH + (ab & 7) * 1024 + ((2 * (cw & 1) + (kg >> 1)) * 16 + t16) * 16 + (kg & 1) * 8;
        const unsigned long long pol = get_pol(rs, lane, dead);
        int seen = -1;
        const unsigned vpo = wave ? OOB : WS_POUT + (lane < 40 ? lane : 0) * FS;
        const unsigned vx1 = WS_X1 + wave * 5120 + lane * 16, vls = WS_LS + tid * 16;
        auto ldp = [&](int s, int i) {
            if (i < 5) dma1(rs, L + (s & 1) * 40960 + wave * 5120 + i * 1024, vx1, RI(s) * (32 * D * 2) + i * 1024);
            else if (i == 5) dma1(rs, L + 81920 + (s & 1) * 10240 + wave * 1024, vls, RI(s) * 10240);
            else if (wave < 2) dma1(rs, L + 81920 + (s & 1) * 10240 + 8192 + wave * 1024, vls, RI(s) * 10240 + 8192); };
        auto ldh = [&](int s) {
            #pragma unroll
            for (int i = 0; i < 7; ++i) ldp(s, i); };
        auto ready = [&](int s) { return __builtin_amdgcn_readfirstlane(*rdy) >= s; };
        wait_fl(rs, vpo, 0, seen, dead, 24);
        if (tid == 0) { *rdy = 0; *cnt = 0; }
        bar();
        ldh(0);
        drain();
        bool pend = false;
        auto done = [&](int s) { if (lane == 0) { unsigned old; asm volatile("ds_add_rtn_u32 %0, %1, %2\n\ts_waitcnt lgkmcnt(0)" : "=v"(old) : "v"(cnt), "v"(1u) : "memory");
            st32(0u, rs, (old & 7u) == 7u ? WS_PF1 + id * FS : OOB, s * FSEG); } };
        for (int s = 0; s < NSEG; ++s) {
            if (s == 2) { ST_RESET; }
            unsigned pv, pw;
            if (wave == 0) asm volatile("buffer_load_dword %0, %2, %4, %5 offen sc1\n\tbuffer_load_dword %1, %3, %4, %6 offen sc1" : "=&v"(pv), "=&v"(pw)
                : "v"((s + 1 < NSEG && seen <= s) ? vpo : OOB), "v"((s + 2 < NSEG && pend) ? vpo : OOB), "s"(rs), "s"((s + 1) * FSEG), "s"((s + 2) * FSEG) : "memory");
            ST(0);
            bar();
            ST(1);
            DG(rt32());
            const int bo = (s & 1) * 40960;
            f32x2 t2, t3, t4;
            u32x4 f[4];
            const int r0 = *rdy;
            #pragma unroll
            for (int m = 0; m < 4; ++m) f[m] = lrd(bb + bo + m * 2048);
            t2 = *(LAS f32x2*)(stat + (s & 1) * 10240); t3 = *(LAS f32x2*)(stat + (s & 1) * 10240 + 512); t4 = *(LAS f32x2*)(stat + (s & 1) * 10240 + 1024);
            SB();
            const bool early = __builtin_amdgcn_readfirstlane(r0) > s && s + 1 < NSEG;
            f32x4 d[4];
            #pragma unroll
            for (int j = 0; j < 4; ++j) d[j] = (f32x4){0, 0, 0, 0};
            #pragma unroll
            for (int i = 0; i < 10; ++i) {
                #pragma unroll
                for (int j = 3; j >= 0; --j) d[j] = mf16(W[j][i], f[i & 3], d[j]);
                asm volatile("" : "+v"(d[0]), "+v"(d[1]), "+v"(d[2]), "+v"(d[3]));
                if (i + 4 < 10) f[i & 3] = lrd(bb + bo + (i + 4) * 2048);
                if (i == 5 && pend) { if (early) asm volatile("s_waitcnt vmcnt(5)" ::: "memory"); else drain(); done(s - 1); }
                if (i < 7 && early) ldp(s + 1, i);
                if (i == 4) { t2 += t3; if (lane < 32) t2 += t4; }
                if (i == 5) { t2[0] = x16sum(t2[0]); t2[1] = x16sum(t2[1]); }
                if (i == 6) { t2[0] = x32sum(t2[0]); t2[1] = x32sum(t2[1]); *(LAS f32x2*)sw = t2; }
                SB(); }
            *(LAS f32x4*)(xw + 1024) = d[3]; *(LAS f32x4*)xw = d[2];
            if (wave == 0) {
                unsigned long long mv, mw;
                if (!pend) drain();
                asm volatile("v_cmp_ne_u32 %0, 0, %2\n\tv_cmp_ne_u32 %1, 0, %3" : "=&s"(mv), "=&s"(mw) : "v"(pv), "v"(pw) : "memory");
                if (pend && s + 2 < NSEG && mw == 0ull) seen = s + 2; else if (mv == 0ull) seen = max(seen, s + 1);
                *rdy = seen; }
            bar();
            const int rv = *rdy;
            t2 = *(LAS f32x2*)sr; t3 = *(LAS f32x2*)(sr + 128); t4 = *(LAS f32x2*)(sr + 256);
            const f32x2 t5 = *(LAS f32x2*)(sr + 384);
            d[2] = *(LAS f32x4*)xr; d[3] = *(LAS f32x4*)(xr + 1024);
            SB();
            bool got = s + 1 >= NSEG || early;
            if (!got && __builtin_amdgcn_readfirstlane(rv) > s) { ldh(s + 1); got = true; }
            ST(2);
            t2 = ((t2 + t3) + (t4 + t5)) * (1.f / D);
            const float rstd = __builtin_amdgcn_rcpf(rnd(__builtin_amdgcn_sqrtf(rnd(rnd(__builtin_fmaxf(t2[1] - t2[0] * t2[0], 0.f)) + 1e-5f)))), mean = rnd(t2[0]);
            const f32x4 g = (d[0] + d[2] - cs[0] * mean) * rstd + bs[0], u = (d[1] + d[3] - cs[1] * mean) * rstd + bs[1];
            float act[4];
            #pragma unroll
            for (int i = 0; i < 4; ++i) act[i] = g[i] * u[i] * __builtin_amdgcn_rcpf(1.f + __expf(-g[i]));
            LS2(dg_, (rt32() & ~1u) | (got ? 1u : 0u));
            st64((u32x2){pk2(act[0], act[1]), pk2(act[2], act[3])}, rs, vact, FO(s));
            ST(4);
            pend = got && s + 1 < NSEG;
            if (pend) asm volatile("s_waitcnt vmcnt(1)" ::: "memory");
            else {
                const bool ok = fl_ok(ld_fl(rs, got ? OOB : vpo, s + 1));
                drain();
                if (wave == 0 && ok) { seen = s + 1; *rdy = s + 1; }
                done(s);
                if (!got) {
                    if (wave == 0) { wait_fl(rs, vpo, s + 1, seen, dead, s < 61 ? a.tp[16] : 2); *rdy = s + 1; }
                    while (!ready(s + 1)) dly(1);
                    ldh(s + 1); drain(); } }
            ST(5);
            if (s == 0) TL(3);
        }
    } else if (role == R_F2) {
        const int cg = id >> 1, mt = id & 1;
        const int ks0 = wave < 6 ? 7 * wave : 42 + 6 * (wave - 6);
        u32x4 W[28];
        gate(tstart + a.tp[23]);
        {   const rsrc_t rw = mkrs(a.wd, F * D * 2);
            const unsigned vw = (32 * ks0 + 8 * (lane >> 4) + ((lane >> 2) & 3)) * (D * 2) + cg * 128 + (lane & 3) * 16;
            u32x4 v[28];
            #pragma unroll
            for (int i = 0; i < 28; ++i) v[i] = ld_pl(rw, vw, (i >= 24 && wave >= 6) ? 0 : (32 * (i >> 2) + 4 * (i & 1)) * (D * 2) + ((i >> 1) & 1) * 64);
            __builtin_amdgcn_sched_barrier(0);
            lptr wp = stg + lane * 16, rp = stg + (lane >> 2) * 64 + (lane & 3) * 8;
            #pragma unroll
            for (int i = 0; i < 28; ++i) { const int pi = i & 3;
                if (i >= 24 && wave >= 6) v[i] = (u32x4){0, 0, 0, 0};
                lwr(wp + pi * 1024, v[i]);
                const u32x2 r0 = trd(rp + pi * 1024), r1 = trd(rp + pi * 1024 + 32);
                const int i0 = (i & ~3) + (pi & 2);
                W[i0][2 * (pi & 1)] = r0.x; W[i0][2 * (pi & 1) + 1] = r0.y; W[i0 + 1][2 * (pi & 1)] = r1.x; W[i0 + 1][2 * (pi & 1) + 1] = r1.y; } }
        TL(2);
        bar();
        const int ab = mt * 54 + ks0;
        const unsigned vact = WS_ACT + lane * 16;
        const unsigned long long pol = get_pol(rs, lane, dead);
        const int pf = ((ks0 >> 1) + (lane & 3)) < 27 ? (ks0 >> 1) + (lane & 3) : 26;
        lptr part = L + wave * 4352 + (lane & 15) * 272 + (lane >> 4) * 16;
        lptr rsrc = L + (tid >> 5) * 272 + (tid & 31) * 8;
        const unsigned vx = (mt * 16 + (tid >> 5)) * (D * 2) + cg * 128 + (tid & 31) * 4;
        const unsigned vx1 = WS_X1 + (cg * 4 + ((tid & 31) >> 3)) * 1024 + (mt * 4 + (tid >> 7)) * 128 + (((tid >> 2) ^ (tid >> 8)) & 1) * 64 + ((tid >> 5) & 3) * 16 + (tid & 3) * 4;
        const unsigned gb = *(LAS unsigned*)(L + L_MOD + 6400 + cg * 128 + (tid & 31) * 4);
        const rsrc_t ro = mkrs(a.out, S * D * 2);
        int seen = -1;
        for (int s = 0; s < NSEG; ++s) {
            wait_fl(rs, WS_PF1 + pf * FS, s, seen, dead, s < 60 ? a.tp[18] : 2);
            DG(rt32());
            u32x4 af[7];
            #pragma unroll
            for (int i = 0; i < 7; ++i) { const int b = ab + ((i == 6 && wave >= 6) ? 0 : i); af[i] = ld_sc1(rs, vact, FO(s) + (b >> 3) * CH + (b & 7) * 1024); }
            const unsigned xr = __builtin_amdgcn_raw_buffer_load_b32(rs, vx1, RI(s) * (32 * D * 2), 16);
            f32x4 d[4];
            #pragma unroll
            for (int j = 0; j < 4; ++j) d[j] = (f32x4){0, 0, 0, 0};
            #pragma unroll
            for (int i = 0; i < 7; ++i)
                #pragma unroll
                for (int j = 0; j < 4; ++j) d[j] = mf16(W[4 * i + j], af[i], d[j]);
            #pragma unroll
            for (int j = 0; j < 4; ++j) *(LAS f32x4*)(part + (s & 1) * 34816 + j * 64) = d[j];
            const unsigned pv = ld_fl(rs, s + 1 < NSEG ? WS_PF1 + pf * FS : OOB, s + 1);
            bar();
            if (tid == 0 && s > 0) st32(0u, rs, WS_PF2 + id * FS, (s - 1) * FSEG);
            f32x2 r = {0, 0};
            #pragma unroll
            for (int w = 0; w < 8; ++w) r += *(LAS f32x2*)(rsrc + (s & 1) * 34816 + w * 4352);
            __builtin_amdgcn_raw_buffer_store_b32(pk2(blo(xr) + rnd(blo(gb) * rnd(r[0])), bhi(xr) + rnd(bhi(gb) * rnd(r[1]))), ro, vx, s * (32 * D * 2), 0);
            if (fl_ok(pv)) seen = s + 1;
            LS2(dg_, rt32());
            if (s == 0) TL(3);
        }
    }
    TL(7);
    ST_OUT;
}

#define WOFF 0
extern "C" void kernel_launch(void* const* d_in, const int* in_sizes, int n_in, void* d_out, int out_size, void* d_ws, size_t ws_size, hipStream_t stream) {
    unsigned char* wsb = (unsigned char*)(((size_t)d_ws + WOFF + 0x1FFFFF) & ~(size_t)0x1FFFFF);
    Args a{(const u16*)d_in[0], (const u16*)d_in[1], (const u16*)d_in[2], (const u16*)d_in[3], (const u16*)d_in[4], (const u16*)d_in[5],
           (const u16*)d_in[8], (const u16*)d_in[9], (const u16*)d_in[10], (const u16*)d_in[11], (const u16*)d_in[12], (u16*)d_out, wsb, {196, 0, 116, 0, 0, -4, 72, 3, 1, 12, 0, 6, 112, 4, 0, 0, 8, 4, 4, 0, 63, 0, 0, 1400}};
    hipMemsetAsync(wsb, 0xFF, WS_CLR_END, stream);
    hipFuncSetAttribute((const void*)mega, hipFuncAttributeMaxDynamicSharedMemorySize, L_TOT);
    mega<<<256, 512, L_TOT, stream>>>(a);
}
```

```cpp
#include <hip/hip_runtime.h>
#include <hip/hip_bf16.h>

typedef unsigned short u16;
typedef __bf16 bf16x8 __attribute__((ext_vector_type(8)));
typedef __bf16 bf16x16 __attribute__((ext_vector_type(16)));
typedef __bf16 bf16x2 __attribute__((ext_vector_type(2)));
typedef short s16x4 __attribute__((ext_vector_type(4)));
typedef float f32x2 __attribute__((ext_vector_type(2)));
typedef float f32x4 __attribute__((ext_vector_type(4)));
typedef float f32x16 __attribute__((ext_vector_type(16)));
typedef unsigned u32x2 __attribute__((ext_vector_type(2)));
typedef unsigned u32x4 __attribute__((ext_vector_type(4)));
typedef unsigned u32x8 __attribute__((ext_vector_type(8)));
typedef __amdgpu_buffer_rsrc_t rsrc_t;
#define LAS __attribute__((address_space(3)))
typedef LAS unsigned char* lptr;

constexpr int S = 2048, D = 640, H = 32, E = 128, HE = 4096, NSEG = 64, F = 1728;
constexpr unsigned WS_EP = 0;
constexpr unsigned WS_FH1 = 1024;
#define FS 32
constexpr unsigned WS_FLG = 65536, FSEG = 192 * FS;
constexpr unsigned WS_PVO = WS_FLG, WS_PQK = WS_FLG + 32 * FS, WS_PP = WS_FLG + 64 * FS, WS_POUT = WS_FLG + 96 * FS, WS_PF2 = WS_FLG + 136 * FS, WS_PF1 = WS_FLG + 160 * FS;
#define RC 16
#define RS 16
constexpr int NPG = 2 * RC > RS ? 2 * RC : RS;
constexpr unsigned WS_POL = 2048;
constexpr unsigned WS_MOD = 4096;
constexpr unsigned WS_SLOT = 2u << 20, SLOT = 2u << 20, CH = 16384;
constexpr unsigned SL_PX = 0, SL_PM = 2 * CH, SL_STP = 2 * CH + 4096, SL_MEM = 3 * CH, SL_U = 5 * CH, SL_END = 45 * CH, SL_CAL = 126 * CH;
#define CFN 0
constexpr unsigned WS_CF = WS_SLOT + (CFN ? 45 * CH : 0), WS_KQ = WS_SLOT + 80 * CH, WS_ACT = WS_SLOT + 112 * CH;
constexpr unsigned WS_H1  = WS_SLOT + NPG * SLOT;
constexpr unsigned WS_AX  = WS_H1 + S * D * 2;
constexpr unsigned WS_AM  = WS_AX + RS * 32 * 2048;
constexpr unsigned WS_X1  = WS_AM + RS * 8 * 2048;
constexpr unsigned WS_LS  = WS_X1 + RS * 32 * D * 2;
constexpr unsigned WS_DBG = WS_LS + RS * 10240;
constexpr unsigned WS_END = WS_DBG + (2 << 20);

__device__ __forceinline__ float b2f(u16 b) { return __uint_as_float((unsigned)b << 16); }
__device__ __forceinline__ float blo(unsigned x) { return __uint_as_float(x << 16); }
__device__ __forceinline__ float bhi(unsigned x) { return __uint_as_float(x & 0xffff0000u); }
__device__ __forceinline__ unsigned pk2(float lo, float hi) { bf16x2 v; v[0] = (__bf16)lo; v[1] = (__bf16)hi; return __builtin_bit_cast(unsigned, v); }
__device__ __forceinline__ float rnd(float f) { return blo(pk2(f, 0.f)); }
__device__ __forceinline__ unsigned nosent(unsigned x) { return x < 0xFFFFFFFEu ? x : 0xFFFFFFFEu; }

struct Args { const u16 *x, *mem0, *Wq, *Wk, *Wv, *Wo, *wg, *wu, *wd, *c, *wm; u16* out; unsigned char* ws; int tp[24]; };
__device__ __forceinline__ void dly(int n) { for (int i = 0; i < n; ++i) __builtin_amdgcn_s_sleep(1); }
__device__ __forceinline__ unsigned long long rtime() { unsigned long long t; asm volatile("s_memrealtime %0\n\ts_waitcnt lgkmcnt(0)" : "=s"(t) :: "memory"); return t; }
constexpr unsigned SPIN_MAX = 200000u, OOB = 0x7FFFF000u;
#define TL(i)
#define ST(i)
#define ST_INIT
#define ST_RESET
#define ST_OUT
#define ST_T(t)
#define LG(k, val)
__device__ __forceinline__ rsrc_t mkrs(const void* p, unsigned bytes) { return __builtin_amdgcn_make_buffer_rsrc((void*)p, 0, (int)bytes, 0x00020000); }
__device__ __forceinline__ u32x4 ld_sc1(rsrc_t r, unsigned voff, unsigned soff) { return __builtin_amdgcn_raw_buffer_load_b128(r, voff, soff, 16); }
__device__ __forceinline__ u32x4 ld_pl(rsrc_t r, unsigned voff, unsigned soff) { return __builtin_amdgcn_raw_buffer_load_b128(r, voff, soff, 0); }
__device__ __forceinline__ void st128(u32x4 v, rsrc_t r, unsigned voff, unsigned soff) { __builtin_amdgcn_raw_buffer_store_b128(v, r, voff, soff, 16); asm volatile("s_nop 1" :: "v"(v)); }
__device__ __forceinline__ void st64(u32x2 v, rsrc_t r, unsigned voff, unsigned soff) { __builtin_amdgcn_raw_buffer_store_b64(v, r, voff, soff, 16); }
__device__ __forceinline__ void st32(unsigned v, rsrc_t r, unsigned voff, unsigned soff) { __builtin_amdgcn_raw_buffer_store_b32(v, r, voff, soff, 16); }
__device__ __forceinline__ unsigned umax3(unsigned a, unsigned b, unsigned c) { unsigned m = a > b ? a : b; return m > c ? m : c; }
__device__ __forceinline__ unsigned mx4(u32x4 v, unsigned m) { return umax3(umax3(v.x, v.y, m), v.z, v.w); }
__device__ __forceinline__ bool all_ok(unsigned m) { return __builtin_amdgcn_ballot_w64(m == 0xFFFFFFFFu) == 0ull; }
__device__ __forceinline__ f32x4 smf(u32x4 a, u32x8 b, f32x4 c, int idx) { return __builtin_amdgcn_smfmac_f32_16x16x64_bf16(__builtin_bit_cast(bf16x8, a), __builtin_bit_cast(bf16x16, b), c, idx, 0, 0); }
__device__ __forceinline__ f32x4 mf16(u32x4 a, u32x4 b, f32x4 c) { return __builtin_amdgcn_mfma_f32_16x16x32_bf16(__builtin_bit_cast(bf16x8, a), __builtin_bit_cast(bf16x8, b), c, 0, 0, 0); }
__device__ __forceinline__ f32x16 mf32(u32x4 a, u32x4 b, f32x16 c) { return __builtin_amdgcn_mfma_f32_32x32x16_bf16(__builtin_bit_cast(bf16x8, a), __builtin_bit_cast(bf16x8, b), c, 0, 0, 0); }
__device__ __forceinline__ float fmx(float a, float b) { return __builtin_elementwise_maximum(a, b); }
__device__ __forceinline__ float fold32(float a, float b) { auto r = __builtin_amdgcn_permlane32_swap(__float_as_uint(a), __float_as_uint(b), false, false); return __uint_as_float(r[0]) + __uint_as_float(r[1]); }
__device__ __forceinline__ float x32max(float a) { auto r = __builtin_amdgcn_permlane32_swap(__float_as_uint(a), __float_as_uint(a), false, false); return fmx(__uint_as_float(r[0]), __uint_as_float(r[1])); }
__device__ __forceinline__ float x32sum(float a) { return fold32(a, a); }
__device__ __forceinline__ float x16max(float a) { auto r = __builtin_amdgcn_permlane16_swap(__float_as_uint(a), __float_as_uint(a), false, false); return fmx(__uint_as_float(r[0]), __uint_as_float(r[1])); }
__device__ __forceinline__ float x16sum(float a) { auto r = __builtin_amdgcn_permlane16_swap(__float_as_uint(a), __float_as_uint(a), false, false); return __uint_as_float(r[0]) + __uint_as_float(r[1]); }
template <int N> __device__ __forceinline__ float ror(float a) { return __int_as_float(__builtin_amdgcn_update_dpp(0, __float_as_int(a), 0x120 + N, 0xf, 0xf, true)); }
template <int C> __device__ __forceinline__ float qp(float a) { return __int_as_float(__builtin_amdgcn_update_dpp(0, __float_as_int(a), C, 0xf, 0xf, true)); }
__device__ __forceinline__ float sum16(float a) { a += ror<8>(a); a += ror<4>(a); a += ror<2>(a); a += ror<1>(a); return a; }
__device__ __forceinline__ void bar() { asm volatile("s_waitcnt lgkmcnt(0)\n\ts_barrier" ::: "memory"); __builtin_amdgcn_s_waitcnt(0xC07F); }
__device__ __forceinline__ void drain() { asm volatile("s_waitcnt vmcnt(0)" ::: "memory"); }
__device__ __forceinline__ u32x2 trd(lptr p) { return __builtin_bit_cast(u32x2, __builtin_amdgcn_ds_read_tr16_b64_v4i16((LAS s16x4*)p)); }
__device__ __forceinline__ u32x4 lrd(lptr p) { return *(LAS u32x4*)p; }
__device__ __forceinline__ void lwr(lptr p, u32x4 v) { *(LAS u32x4*)p = v; }
__device__ __forceinline__ float dot2(unsigned a, unsigned b, float c) { return __builtin_amdgcn_fdot2_f32_bf16(__builtin_bit_cast(bf16x2, a), __builtin_bit_cast(bf16x2, b), c, false); }

__device__ __forceinline__ unsigned ld_fl(rsrc_t rs, unsigned voff, int seg) { asm volatile("" ::: "memory"); return __builtin_amdgcn_raw_buffer_load_b32(rs, voff, seg * FSEG, 16); }
__device__ __forceinline__ bool fl_ok(unsigned v) { const bool r = __builtin_amdgcn_ballot_w64(v != 0u) == 0ull; asm volatile("" ::: "memory"); return r; }
__device__ __forceinline__ void wait_fl(rsrc_t rs, unsigned voff, int seg, int& seen, bool& dead, int slp) {
    if (seen >= seg) return;
    for (unsigned sp = 0;; ++sp) {
        if (fl_ok(ld_fl(rs, voff, seg)) || dead) break;
        if (sp > SPIN_MAX) { dead = true; break; }
        dly(slp);
    }
    seen = seg;
}
__device__ __forceinline__ void wait_ring(rsrc_t rs, unsigned voff, int seg, int& seen, bool& dead) {
    if (seen >= seg) return;
    wait_fl(rs, voff, seg, seen, dead, 8);
}

constexpr int L_A = 0;
constexpr int L_QK = 10240;
constexpr int L_KQX = 14336;
constexpr int L_STG = 49152;
constexpr int L_MOD = 98304;
constexpr int L_TOT = 120320;

#define FHR 5
#define WAUX 2
template <int KIND> __device__ __forceinline__ unsigned wsoff(int r, int j) {
    const int i = 5 * r + j;
    return KIND == 0 ? (64 * (i >> 2) + 32 * ((i & 3) >> 1) + 2 * (i & 1)) * (HE * 2) : KIND == 1 ? (64 * (r >> 2) + 32 * ((r & 3) >> 1) + (r & 1)) * (D * 2) + j * 256
         : KIND == 2 ? 16 * i * (HE * 2) : (32 * (r >> 1) + 8 * (r & 1)) * (D * 2) + j * 256; }
template <int KIND> __device__ __forceinline__ void wissue(u32x4 (&v)[40], rsrc_t rw, unsigned base, int wq, int lane) {
    const int q = lane >> 4, jj = 4 * (wq & 1) + q;
    const int row = KIND == 0 ? 16 * (wq >> 1) + 8 * (q >> 1) + 4 * (wq & 1) + (q & 1) : KIND == 1 ? 4 * (q >> 1) + 8 * (wq >> 1) + 2 * (wq & 1) + 16 * (q & 1)
                  : KIND == 2 ? 4 * wq + q : 4 * (wq >> 1) + (jj >> 1) + 16 * (jj & 1);
    const unsigned voff = row * ((KIND & 1) ? D * 2 : HE * 2) + base + (lane & 15) * 16;
    #pragma unroll
    for (int i = 0; i < 40; ++i) v[i] = __builtin_amdgcn_raw_buffer_load_b128(rw, voff, wsoff<KIND>(i / 5, i % 5), WAUX);
    __builtin_amdgcn_sched_barrier(0);
}
template <bool WO>
__device__ __forceinline__ void load_w_smf(u32x8 (&W)[20], rsrc_t rw, unsigned base, int wave, int lane, lptr L) {
    const int wq = wave & 3;
    u32x4 v[40];
    wissue<WO ? 1 : 0>(v, rw, base, wq, lane);
    lptr wp = L + (wave >> 2) * 20480 + ((lane >> 2) & 3) * 1024 + (4 * wq + (lane >> 4)) * 64 + (lane & 3) * 16;
    lptr rp = L + (wave >> 2) * 20480 + (WO ? 5 * wq : wq) * 1024 + (lane >> 2) * 64 + (lane & 3) * 8;
    #pragma unroll
    for (int r = 0; r < 8; ++r) {
        #pragma unroll
        for (int j = 0; j < 5; ++j) lwr(wp + (r & 1) * 40960 + j * 4096, v[5 * r + j]);
        bar();
        #pragma unroll
        for (int j = 0; j < 5; ++j) { const int i = 5 * r + j, pi = WO ? r & 3 : i & 3;
            lptr p = rp + (r & 1) * 40960 + j * (WO ? 1024 : 4096);
            const u32x2 r0 = trd(p), r1 = trd(p + 32);
            const int i0 = WO ? 4 * j + (r >> 2) : 2 * (i >> 2), i1 = WO ? i0 + 2 : i0 + 1;
            W[i0][2 * pi] = r0.x; W[i0][2 * pi + 1] = r0.y; W[i1][2 * pi] = r1.x; W[i1][2 * pi + 1] = r1.y; }
    }
    bar();
}
template <bool WO>
__device__ __forceinline__ void load_w_32(u32x4 (&W)[40], rsrc_t rw, unsigned base, int wave, int lane, lptr L, rsrc_t rs, u32x4& fh) {
    const int wq = wave & 3, G = lane >> 4;
    u32x4 v[40];
    wissue<WO ? 3 : 2>(v, rw, base, wq, lane);
    lptr wp = L + (wave >> 2) * 20480 + ((lane >> 2) & 3) * 1024 + (4 * wq + (lane >> 4)) * 64 + (lane & 3) * 16;
    lptr rp = L + (wave >> 2) * 20480 + (WO ? 5 * wq : wq) * 1024 + (8 * (G >> 1) + ((lane >> 2) & 3)) * 64 + (G & 1) * 32 + (lane & 3) * 8;
    #pragma unroll
    for (int r = 0; r < 8; ++r) {
        #pragma unroll
        for (int j = 0; j < 5; ++j) lwr(wp + (r & 1) * 40960 + j * 4096, v[5 * r + j]);
        bar();
        #pragma unroll
        for (int j = 0; j < 5; ++j) { lptr p = rp + (r & 1) * 40960 + j * (WO ? 1024 : 4096);
            const u32x2 r0 = trd(p), r1 = trd(p + 256);
            W[WO ? 8 * j + r : 5 * r + j] = (u32x4){r0.x, r0.y, r1.x, r1.y}; }
        if (r == FHR) { fh = ld_sc1(rs, WS_FH1 + lane * 16, 0); __builtin_amdgcn_sched_barrier(0); }
    }
    bar();
}

struct Pc { unsigned g1, g2; int p1, p2; };
__device__ __forceinline__ Pc mkpc(int p1, int p2, int lane) { Pc r; r.p1 = p1; r.p2 = p2; r.g1 = WS_SLOT + SL_MEM + (p1 >> 3) * CH + (p1 & 7) * 1024 + lane * 16; r.g2 = WS_SLOT + SL_MEM + (p2 >> 3) * CH + (p2 & 7) * 1024 + lane * 16; return r; }
template <int NP>
__device__ __forceinline__ void gather_mem(const Args& a, rsrc_t rs, unsigned so, bool fresh, const Pc& pc, lptr A, bool& dead) {
    if (NP == 0) return;
    u32x4 v0 = {~0u, ~0u, ~0u, ~0u}, v1 = {0, 0, 0, 0};
    if (!fresh) { v0 = lrd(A + pc.p1 * 1024); if (NP == 2) v1 = lrd(A + pc.p2 * 1024); }
    for (unsigned sp = 0;; ++sp) {
        unsigned m = mx4(v0, 0); if (NP == 2) m = mx4(v1, m);
        if (all_ok(m) || dead) break;
        if (sp > SPIN_MAX) { dead = true; break; }
        asm volatile("" ::: "memory");
        if (m == 0xFFFFFFFFu) { v0 = ld_sc1(rs, pc.g1, so); if (NP == 2) v1 = ld_sc1(rs, pc.g2, so); }
    }
    lwr(A + pc.p1 * 1024, v0);
    if (NP == 2) lwr(A + pc.p2 * 1024, v1);
}
__device__ __forceinline__ void first_mem(const Args& a, int wave, int l, lptr A) {
    const u16* s = a.mem0 + (2 * ((l >> 2) & 3) + (l & 1)) * D + 16 * (l >> 4) + 8 * ((l >> 1) & 1);
    lwr(A + wave * 1024, *(const u32x4*)(s + 64 * wave));
    if (wave < 2) lwr(A + (wave + 8) * 1024, *(const u32x4*)(s + 64 * (wave + 8)));
}
__device__ __forceinline__ u32x4 ld_pol(rsrc_t rs, int lane) { asm volatile("" ::: "memory"); return ld_sc1(rs, WS_POL + (lane & (NPG / 2 - 1)) * 16, 0); }
__device__ __forceinline__ unsigned long long get_pol(rsrc_t rs, int lane, bool& dead, u32x2 tag, u32x4 v) {
    for (unsigned sp = 0;; ++sp) {
        if (__builtin_amdgcn_ballot_w64(v.z != tag.x || v.w != tag.y) == 0ull || dead) break;
        if (sp > SPIN_MAX) { dead = true; break; }
        if (sp) dly(8);
        v = ld_pol(rs, lane); }
    asm volatile("" ::: "memory");
    return __builtin_amdgcn_ballot_w64((lane & (NPG / 2) ? v.y : v.x) == 1u);
}
#define PG(p) (((unsigned)(p) << 21) + (((unsigned)(pol >> (p)) & 1u) << 13))
#define SO(t) PG((t) & (RS - 1))
#define FO(t) (SO(t) ^ 8192u)
#define RO(s, odd) (PG(2 * ((unsigned)(s) % RC) + (odd)) ^ 8192u)
#define RI(s) ((s) & (RS - 1))
#define CO(s, odd) (RO(s, odd) ^ (CFN ? 8192u : 0u))
__device__ __forceinline__ unsigned rt32() { return (unsigned)__builtin_amdgcn_s_memrealtime(); }
__device__ __forceinline__ void gate(unsigned target) {
    asm volatile("1:\n\ts_memrealtime vcc\n\ts_waitcnt lgkmcnt(0)\n\ts_sub_i32 vcc_lo, %0, vcc_lo\n\ts_cmp_gt_i32 vcc_lo, 0\n\ts_cbranch_scc1 1b" :: "s"(target) : "vcc", "scc", "memory"); }
template <int N> __device__ __forceinline__ int rori(int a) { return __builtin_amdgcn_update_dpp(0, a, 0x120 + N, 0xf, 0xf, true); }
__device__ __forceinline__ unsigned read_G(rsrc_t rs, unsigned so, int lane, bool& dead) {
    u32x2 v;
    for (unsigned sp = 0;; ++sp) { asm volatile("" ::: "memory");
        v = __builtin_amdgcn_raw_buffer_load_b64(rs, WS_SLOT + SL_STP + (lane < 40 ? lane : 0) * 32, so, 16);
        if (all_ok(v.x > v.y ? v.x : v.y) || dead) break;
        if (sp > SPIN_MAX) { dead = true; break; }
        dly(2); }
    const unsigned ref = rt32();
    int d = max((int)(v.x - ref), (int)(v.y - ref));
    d = max(d, rori<8>(d)); d = max(d, rori<4>(d)); d = max(d, rori<2>(d)); d = max(d, rori<1>(d));
    { auto r = __builtin_amdgcn_permlane16_swap(d, d, false, false); d = max((int)r[0], (int)r[1]); }
    { auto r = __builtin_amdgcn_permlane32_swap(d, d, false, false); d = max((int)r[0], (int)r[1]); }
    d = min(max(d, -100000), 600);
    return ref + (unsigned)__builtin_amdgcn_readfirstlane(d);
}
constexpr int L_FAIL = 63488;
constexpr int L_DUM = 63744;
#define LS2(x0, x1)
#define DG(x)
__device__ __forceinline__ void dma1(rsrc_t rs, lptr d, unsigned vo, unsigned so) { __builtin_amdgcn_raw_ptr_buffer_load_lds(rs, (LAS void*)d, 16, vo, so, 0, 16); }
__device__ __forceinline__ void dma4(rsrc_t rs, lptr d, unsigned vo, unsigned so) {
    __builtin_amdgcn_raw_ptr_buffer_load_lds(rs, (LAS void*)d, 16, vo, so, 0, 16); __builtin_amdgcn_raw_ptr_buffer_load_lds(rs, (LAS void*)d, 16, vo, so, 1024, 16);
    __builtin_amdgcn_raw_ptr_buffer_load_lds(rs, (LAS void*)d, 16, vo, so, 2048, 16); __builtin_amdgcn_raw_ptr_buffer_load_lds(rs, (LAS void*)d, 16, vo, so, 3072, 16); }
template <int NP>
__device__ __forceinline__ void dma_mem(rsrc_t rs, unsigned so, const Pc& pc, lptr L) {
    if (NP > 0) __builtin_amdgcn_raw_ptr_buffer_load_lds(rs, (LAS void*)(L + L_A + pc.p1 * 1024), 16, pc.g1, so, 0, 16);
    if (NP > 1) __builtin_amdgcn_raw_ptr_buffer_load_lds(rs, (LAS void*)(L + L_A + pc.p2 * 1024), 16, pc.g2, so, 0, 16);
}
#define FETCH(NP, GATE) do { const unsigned so_ = SO(t > 0 ? t - 1 : 0); \
    if (__builtin_expect(!again && gated && t > 1, 1)) { tagc = t; GATE; ST(0); bar(); ST(5); dma_mem<NP>(rs, so_, pc, L); } \
    else { if (t > 0) gather_mem<NP>(a, rs, so_, !again, pc, Asrc, dead); tagc = -5; } } while (0)
__device__ __forceinline__ void flag(unsigned m, int t, int lane, lptr L) { *(LAS int*)(L + (m == 0xFFFFFFFFu ? L_FAIL : L_DUM + lane * 4)) = t; }
template <int NP>
__device__ __forceinline__ void check_own(int t, int lane, const Pc& pc, lptr A, lptr L) {
    unsigned m = mx4(lrd(A + pc.p1 * 1024), 0);
    if (NP == 2) m = mx4(lrd(A + pc.p2 * 1024), m);
    flag(m, t, lane, L);
}
__device__ __forceinline__ bool failed(int t, lptr L) { return __builtin_expect(__builtin_amdgcn_readfirstlane(*(LAS int*)(L + L_FAIL)) == t, 0); }
#define SB() __builtin_amdgcn_sched_barrier(0)
#define VPT 1
#define QPT 15
#define UBAR 3
#define NCAL 4
#define NB1 8
#define NB2 0
#define PH1 2
template <bool B> struct Bc { static constexpr bool value = B; };
struct Nop { __device__ void operator()() const {} };
template <int KIND, class FN = Nop>
__device__ __forceinline__ void proj2(const u32x8 (&W)[20], lptr Asrc, lptr dstp, int idx, int t, const Pc& pc, int lane, lptr L, bool young, unsigned& dg_, FN mid = Nop()) {
    f32x4 e = {0, 0, 0, 0}, o = {0, 0, 0, 0};
    u32x4 af[10], own, own2;
    asm volatile("" : "+v"(e), "+v"(o));
    if (KIND == 1) { drain(); DG(rt32()); }
    if (KIND == 3) { asm volatile("s_waitcnt vmcnt(1)" ::: "memory"); DG(rt32()); }
    if (KIND == 4) { if (young) asm volatile("s_waitcnt vmcnt(8)" ::: "memory"); else drain(); DG(rt32()); }
    bar();
    #pragma unroll
    for (int ks = 0; ks < 4; ++ks) af[ks] = lrd(Asrc + ks * 1024);
    SB();
    #pragma unroll
    for (int ks = 0; ks < PH1; ++ks) { e = smf(af[ks], W[2 * ks], e, idx); o = smf(af[ks], W[2 * ks + 1], o, idx); }
    asm volatile("" : "+v"(e), "+v"(o));
    if (KIND == 2 || KIND == 3) drain();
    if (KIND == 5) asm volatile("s_waitcnt vmcnt(1)" ::: "memory");
    if (KIND == 2 || KIND == 5) DG(rt32());
    bar();
    if (KIND != 4) own = lrd(Asrc + pc.p1 * 1024);
    if (KIND == 3) own2 = lrd(Asrc + pc.p2 * 1024);
    SB();
    #pragma unroll
    for (int ks = 4; ks < 10; ++ks) af[ks] = lrd(Asrc + ks * 1024);
    SB();
    #pragma unroll
    for (int ks = PH1; ks < 4; ++ks) { e = smf(af[ks], W[2 * ks], e, idx); o = smf(af[ks], W[2 * ks + 1], o, idx); }
    SB();
    if (KIND != 4) { asm volatile("" : "+v"(own)); unsigned m = mx4(own, 0); if (KIND == 3) m = mx4(own2, m); flag(m, t, lane, L); }
    #pragma unroll
    for (int ks = 4; ks < 10; ++ks) { e = smf(af[ks], W[2 * ks], e, idx); o = smf(af[ks], W[2 * ks + 1], o, idx); if (ks == 5) { asm volatile("" : "+v"(e), "+v"(o)); SB(); mid(); SB(); } }
    *(LAS unsigned*)dstp = pk2(e[0] + e[2], o[0] + o[2]); *(LAS unsigned*)(dstp + 16) = pk2(e[1] + e[3], o[1] + o[3]);
}
#define PROJ(KIND, GATE, YOUNG) FETCH((KIND == 3 ? 2 : KIND == 4 ? 0 : 1), GATE); proj2<KIND>(W, Asrc, pdst, sidx, t, pc, lane, L, YOUNG, dg_); ST(2); bar(); ST(3); \
        again = failed(tagc, L); if (again) { --t; continue; } DGL;
__device__ __forceinline__ void max3x2(float a, float b, float c, float& r0, float& r1) { asm("v_maximum3_f32 %0, %2, %3, %4\n\tv_maximum3_f32 %1, %2, %3, %4\n\ts_nop 1" : "=&v"(r0), "=&v"(r1) : "v"(a), "v"(b), "v"(c)); }
__device__ __forceinline__ void addx2(float a, float b, float& r0, float& r1) { asm("v_add_f32 %0, %2, %3\n\tv_add_f32 %1, %2, %3\n\ts_nop 1" : "=&v"(r0), "=&v"(r1) : "v"(a), "v"(b)); }
__device__ __forceinline__ void sw16(float& a, float& b) { auto r = __builtin_amdgcn_permlane16_swap(__float_as_uint(a), __float_as_uint(b), false, false); a = __uint_as_float(r[0]); b = __uint_as_float(r[1]); }
__device__ __forceinline__ void sw32(float& a, float& b) { auto r = __builtin_amdgcn_permlane32_swap(__float_as_uint(a), __float_as_uint(b), false, false); a = __uint_as_float(r[0]); b = __uint_as_float(r[1]); }
__device__ __forceinline__ int proj2_dst(int wq, int lane) { const int c = lane & 15; return (2 * wq + (c >> 3)) * 256 + (4 * (lane >> 4) + 2 * ((c >> 2) & 1)) * 16 + (c & 3) * 4; }
__device__ __forceinline__ int tokoff(int tok) { return (4 * (tok >> 1) + (tok & 1)) * 16; }

template <class FN> __device__ __forceinline__ void tile640(const u32x4 (&W)[40], lptr img, int lane, u32x4& o0, u32x4& o1, FN mid) {
    const int tok = lane & 31, kg = lane >> 5;
    f32x16 acc;
    #pragma unroll
    for (int i = 0; i < 16; ++i) acc[i] = 0;
    lptr b[4];
    #pragma unroll
    for (int m = 0; m < 4; ++m) b[m] = img + kg * 512 + m * 1024 + ((tok ^ (2 * m + kg)) * 16);
    u32x4 f[4];
    #pragma unroll
    for (int m = 0; m < 4; ++m) f[m] = lrd(b[m]);
    __builtin_amdgcn_sched_barrier(0);
    #pragma unroll
    for (int ks = 0; ks < 40; ++ks) { acc = mf32(W[ks], f[ks & 3], acc); if (ks + 4 < 40) f[ks & 3] = lrd(b[ks & 3] + ((ks + 4) >> 2) * 4096); mid(ks); __builtin_amdgcn_sched_barrier(0); }
    o0 = (u32x4){pk2(acc[0], acc[8]), pk2(acc[1], acc[9]), pk2(acc[2], acc[10]), pk2(acc[3], acc[11])};
    o1 = (u32x4){pk2(acc[4], acc[12]), pk2(acc[5], acc[13]), pk2(acc[6], acc[14]), pk2(acc[7], acc[15])};
}

constexpr float CS = 0.08838834764831845f * 1.4426950408889634f;
enum { R_P, R_V, R_C, R_OUT, R_QK, R_VO, R_F1, R_F2, R_SP };
#define FSW 1
#define HM 1
#define RMAP 2
__host__ __device__ inline void rolemap(int b, int& role, int& id) {
    const int grp = b & 7, idx8 = b >> 3;
    if (RMAP == 2) {
        if (grp < 4) {
            if (idx8 < 8) { role = R_P; id = HM ? grp * 8 + idx8 : idx8 * 4 + grp; } else if (idx8 < 16) { role = R_V; id = HM ? grp * 8 + idx8 - 8 : (idx8 - 8) * 4 + grp; } else if (idx8 < 26) { role = R_C; id = grp * 10 + idx8 - 16; }
            else if (FSW) { const int k = (idx8 - 26) * 4 + grp; if (k < 23) { role = R_F1; id = k; } else { role = R_SP; id = 0; } }
            else if (idx8 < 31) { role = R_F2; id = (idx8 - 26) * 4 + grp; } else if (grp < 3) { role = R_F1; id = 24 + grp; } else { role = R_SP; id = 0; }
        } else if (idx8 < 10) { role = R_OUT; id = (grp - 4) * 10 + idx8; }
        else { const int wi = (idx8 - 10) * 4 + grp - 4;
            if (wi < 32) { role = R_VO; id = HM ? (wi & 3) * 8 + (wi >> 2) : wi; } else if (wi < 64) { role = R_QK; id = HM ? (wi & 3) * 8 + ((wi - 32) >> 2) : wi - 32; } else if (!FSW) { role = R_F1; id = wi - 64; }
            else if (wi < 84) { role = R_F2; id = wi - 64; } else { role = R_F1; id = wi - 61; } }
        return;
    }
    if (RMAP == 3) {
        if (idx8 < 16) { role = idx8 < 4 ? R_P : idx8 < 8 ? R_QK : idx8 < 12 ? R_V : R_VO; id = (idx8 & 3) * 8 + grp; }
        else if (idx8 < 21) { role = R_C; id = grp * 5 + idx8 - 16; }
        else if (idx8 < 25) { role = R_OUT; id = grp * 4 + idx8 - 21; }
        else if (idx8 < 27 && grp < 4) { role = R_OUT; id = 32 + grp * 2 + idx8 - 25; }
        else { const int k = idx8 < 27 ? (idx8 - 25) * 4 + grp - 4 : 8 + (idx8 - 27) * 8 + grp;
            if (k < 27) { role = R_F1; id = k; } else if (k < 47) { role = R_F2; id = k - 27; } else { role = R_SP; id = 0; } }
        return;
    }
    if (grp == 0) { role = R_P; id = idx8; return; }
    if (grp == 1) { role = R_V; id = idx8; return; }
    if (RMAP == 0) {
        if (grp == 7) { if (idx8 < 27) { role = R_F1; id = idx8; } else { role = R_F2; id = idx8 - 27; } }
        else if (idx8 < 8) { role = R_C; id = (grp - 2) * 8 + idx8; }
        else if (idx8 < 16) { role = R_OUT; id = (grp - 2) * 8 + idx8 - 8; }
        else { const int wi = (idx8 - 16) * 5 + grp - 2;
            if (wi < 32) { role = R_QK; id = wi; } else if (wi < 64) { role = R_VO; id = wi - 32; } else if (wi < 79) { role = R_F2; id = wi - 59; } else { role = R_SP; id = 0; } }
    } else {
        if (grp < 4) {
            if (idx8 < 20) { role = R_C; id = (grp - 2) * 20 + idx8; }
            else if (idx8 < 30) { role = R_F2; id = (idx8 - 20) * 2 + grp - 2; }
            else { const int k = (idx8 - 30) * 2 + grp - 2; if (k < 3) { role = R_QK; id = 29 + k; } else { role = R_SP; id = 0; } }
        } else if (idx8 < 10) { role = R_OUT; id = (grp - 4) * 10 + idx8; }
        else { const int wi = (idx8 - 10) * 4 + grp - 4;
            if (wi < 32) { role = R_VO; id = wi; } else if (wi < 61) { role = R_QK; id = wi - 32; } else { role = R_F1; id = wi - 61; } }
    }
}
__global__ void __launch_bounds__(512, 2) mega(Args a) {
    extern __shared__ __attribute__((aligned(16))) unsigned char smem[];
    lptr L = (lptr)smem;
    const int tid = threadIdx.x, lane = tid & 63, wave = __builtin_amdgcn_readfirstlane(tid >> 6);
    int role, id; rolemap(blockIdx.x, role, id);
    const int hid = role == R_C ? id : role == R_OUT ? 40 + id : role == R_F1 ? 80 + id : role == R_F2 ? 107 + id : role == R_SP ? 127 : -1;
    const rsrc_t rs = mkrs(a.ws, WS_END);
    const int sidx = (lane & 2) ? 0xEEEE : 0x4444;
    bool dead = false;
    lptr stg = L + L_STG + wave * 4096;
    ST_INIT
    TL(0);
    const unsigned tstart = rt32();
    unsigned long long ep;
    asm volatile("s_load_dwordx2 %0, %1, 0x0\n\ts_waitcnt lgkmcnt(0)" : "=s"(ep) : "s"(a.ws + WS_EP) : "memory");
    ++ep;
    const u32x2 tag = {(unsigned)ep, (unsigned)(ep >> 32)};
    if (hid >= 0) {
        const int row = hid * 16 + wave * 2 + (lane >> 5), l32 = lane & 31;
        if (hid < 120) {
            #pragma unroll
            for (int i = 0; i < 2; ++i) { const unsigned u = (i * 120 + hid) * 8192 + tid * 16;
                st128((u32x4){~0u, ~0u, ~0u, ~0u}, rs, u < SL_END ? u : u < 2 * SL_END ? u - SL_END + SLOT : u < 2 * SL_END + 3 * CH ? u - 2 * SL_END + 2 * SLOT + 2 * CH : OOB, WS_SLOT); } }
        if (hid < 96 && tid < 128) st32(~0u, rs, WS_FLG + (hid * 128 + tid) * FS, 0);
        u32x2 xv[5];
        #pragma unroll
        for (int i = 0; i < 5; ++i) xv[i] = *(const u32x2*)(a.x + row * D + (l32 + 32 * i) * 4);
        if (hid < 120) {
            u32x4 wv[5]; float sv[5];
            #pragma unroll
            for (int i = 0; i < 5; ++i) { const int k = (tid >> 2) + 128 * i; wv[i] = *(const u32x4*)(a.wm + k * 3840 + hid * 32 + (tid & 3) * 8); sv[i] = b2f(a.c[k]); }
            float acc[8] = {0, 0, 0, 0, 0, 0, 0, 0};
            #pragma unroll
            for (int i = 0; i < 5; ++i) { const float s = rnd(sv[i] / (1.f + __expf(-sv[i])));
                #pragma unroll
                for (int j = 0; j < 4; ++j) { acc[2 * j] += s * blo(wv[i][j]); acc[2 * j + 1] += s * bhi(wv[i][j]); } }
            #pragma unroll
            for (int j = 0; j < 8; ++j) { float v = acc[j]; v += ror<8>(v); v += ror<4>(v); v = x16sum(v); acc[j] = x32sum(v); }
            if (lane < 4) { *(LAS f32x4*)(L + wave * 128 + lane * 32) = (f32x4){acc[0], acc[1], acc[2], acc[3]}; *(LAS f32x4*)(L + wave * 128 + lane * 32 + 16) = (f32x4){acc[4], acc[5], acc[6], acc[7]}; }
            drain(); bar();
            if (tid < 8) { f32x4 s = {0, 0, 0, 0};
                #pragma unroll
                for (int w = 0; w < 8; ++w) s += *(LAS f32x4*)(L + w * 128 + tid * 16);
                st128((u32x4){pk2(s[0], s[1]), pk2(s[2], s[3]), tag.x, tag.y}, rs, WS_MOD + hid * 128 + tid * 16, 0); }
        }
        float xf[20], s = 0;
        #pragma unroll
        for (int i = 0; i < 5; ++i) { xf[4 * i] = blo(xv[i].x); xf[4 * i + 1] = bhi(xv[i].x); xf[4 * i + 2] = blo(xv[i].y); xf[4 * i + 3] = bhi(xv[i].y); }
        #pragma unroll
        for (int i = 0; i < 20; ++i) s += xf[i];
        const float mean = x16sum(sum16(s)) * (1.f / D);
        float vs = 0;
        #pragma unroll
        for (int i = 0; i < 20; ++i) { const float d = xf[i] - mean; vs += d * d; }
        const float var = x16sum(sum16(vs)) * (1.f / D);
        const float mb = rnd(mean), rd = 1.f / rnd(__builtin_sqrtf(rnd(rnd(var) + 1e-5f)));
        #pragma unroll
        for (int i = 0; i < 20; ++i) xf[i] = rnd(rnd(xf[i] - mb) * rd);
        {   u32x4 mv, mw; const int j = tid < 480 ? tid : 479;
            for (unsigned sp = 0;; ++sp) { asm volatile("" ::: "memory"); mv = ld_sc1(rs, WS_MOD + j * 32, 0); mw = ld_sc1(rs, WS_MOD + j * 32 + 16, 0);
                if (__builtin_amdgcn_ballot_w64(mv.z != tag.x || mv.w != tag.y || mw.z != tag.x || mw.w != tag.y) == 0ull) break;
                if (sp > SPIN_MAX) { dead = true; break; } dly(2); }
            lwr(L + L_MOD + j * 16, (u32x4){mv.x, mv.y, mw.x, mw.y}); }
        bar();
        #pragma unroll
        for (int i = 0; i < 5; ++i) { const u32x2 sa = *(LAS u32x2*)(L + L_MOD + (l32 + 32 * i) * 8), ca = *(LAS u32x2*)(L + L_MOD + 1280 + (l32 + 32 * i) * 8);
            u32x2 o;
            o.x = pk2(rnd(xf[4 * i] * rnd(1.f + blo(ca.x))) + blo(sa.x), rnd(xf[4 * i + 1] * rnd(1.f + bhi(ca.x))) + bhi(sa.x));
            o.y = pk2(rnd(xf[4 * i + 2] * rnd(1.f + blo(ca.y))) + blo(sa.y), rnd(xf[4 * i + 3] * rnd(1.f + bhi(ca.y))) + bhi(sa.y));
            st64(o, rs, WS_H1 + row * (D * 2) + (l32 + 32 * i) * 8, 0); }
        drain(); bar();
        if (tid == 0) st64(tag, rs, WS_FH1 + hid * 8, 0);
        TL(1);
        if (hid < NPG / 2) {
            const unsigned vo = lane < 32 ? WS_SLOT + SL_CAL + (wave & 1) * 4096 + ((wave >> 1) & 1) * CH + lane * 4 : OOB;
            unsigned m[2] = {~0u, ~0u};
            for (int r = 0; r < NCAL; ++r)
                #pragma unroll
                for (int q = 0; q < 2; ++q) { unsigned long long t0, t1;
                    asm volatile("s_waitcnt vmcnt(0)\n\ts_memtime %0\n\ts_waitcnt lgkmcnt(0)" : "=s"(t0) :: "memory");
                    st32(0u, rs, vo, (hid + (wave >> 2) * (NPG / 2)) * SLOT + q * 8192);
                    asm volatile("s_waitcnt vmcnt(0)\n\ts_memtime %0\n\ts_waitcnt lgkmcnt(0)" : "=s"(t1) :: "memory");
                    m[q] = min(m[q], (unsigned)(t1 - t0)); }
            *(LAS u32x2*)(L + wave * 8) = (u32x2){m[0], m[1]};
            bar();
            if (wave == 0) { const u32x2 mm = *(LAS u32x2*)(L + (lane & 7) * 8);
                const unsigned bm = (unsigned)__builtin_amdgcn_ballot_w64(mm.y < mm.x);
                if (lane == 0) st128((u32x4){__builtin_popcount(bm & 7u) > 1 ? 1u : 0u, __builtin_popcount(bm & 0x70u) > 1 ? 1u : 0u, tag.x, tag.y}, rs, WS_POL + hid * 16, 0); }
            bar();
            TL(4);
        }
    }
    const bool gated = a.tp[0] > 0;
    const unsigned DLT = a.tp[0], DU = a.tp[2];
    int tagc = 0; bool again = false; unsigned nfail = 0, dg_ = 0;
#define DGL
    if (role == R_P) {
        const int h = id;
        u32x8 W[20];
        load_w_smf<false>(W, mkrs(wave < 4 ? a.Wq : a.Wk, D * HE * 2), h * 256, wave, lane, L);
        TL(1);
        if (tid == 0) *(LAS int*)(L + L_FAIL) = -1;
        lptr Asrc = L + L_A + lane * 16;
        lptr pdst = L + L_QK + (wave >> 2) * 2048 + proj2_dst(wave & 3, lane);
        first_mem(a, wave, lane, Asrc);
        const unsigned long long pol = get_pol(rs, lane, dead, tag, ld_pol(rs, lane));
        asm volatile("" : "+v"(pdst));
        ST(0);
        if (wave == 0) {
            const int rho = lane & 15, g = lane >> 4;
            const Pc pc = mkpc(4, 4, lane);
            const int key = (((rho >> 2) & 1) << 3) | (((rho >> 1) & 1) << 2) | (((rho >> 3) & 1) << 1) | (rho & 1);
            lptr qsrc = L + L_QK + (g >> 1) * 256 + (g & 1) * 32 + tokoff(rho & 7);
            lptr msrc = L + L_QK + 2048 + (g >> 1) * 256 + (g & 1) * 32 + tokoff(2 * (rho >> 2) + (rho & 1));
            lptr ksrc = L + L_KQX + (g * 32 + key) * 16;
            asm volatile("" : "+v"(qsrc), "+v"(msrc));
            const bool ch = (lane & 8) != 0;
            const unsigned vpx = WS_SLOT + SL_PX + (h >> 4) * CH + (h & 15) * 512 + ((lane >> 3) & 1) * 256 + (g >> 1) * 32 + tokoff(lane & 7) + (g & 1) * 8;
            const unsigned vpm = ch ? OOB : WS_SLOT + SL_PM + ((lane >> 2) & 1) * 2048 + (h >> 1) * 128 + tokoff(lane & 3) + (g & 1) * 32 + (h & 1) * 8 + (g >> 1) * 4;
            unsigned long long rt0 = 0;
            for (int t = 0; t < NSEG; ++t) {
                ST_T(t); if (t == 2) { ST_RESET; }
                u32x4 k0[4], k1[4];
                int fl;
                unsigned so = SO(t);
                lptr kp = ksrc + (t & 1) * 16384;
                asm volatile("" : "+s"(so), "+v"(kp));
                auto ldk = [&]() {
                    #pragma unroll
                    for (int s = 0; s < 4; ++s) { k0[s] = lrd(kp + s * 2048); k1[s] = lrd(kp + s * 2048 + 256); } };
                auto scores = [&]() -> bool {
                    u32x4 q[4], km[4];
                    #pragma unroll
                    for (int s = 0; s < 4; ++s) { q[s] = lrd(qsrc + s * 512); SB(); }
                    #pragma unroll
                    for (int s = 0; s < 4; ++s) { km[s] = lrd(msrc + s * 512); SB(); }
                    fl = *(LAS int*)(L + L_FAIL);
                    SB();
                    f32x4 sm = {0, 0, 0, 0}, s0 = sm, s1 = sm;
                    #pragma unroll
                    for (int s = 0; s < 4; ++s) { s0 = mf16(k0[s], q[s], s0); s1 = mf16(k1[s], q[s], s1); }
                    asm volatile("" : "+v"(s0), "+v"(s1), "+v"(km[0]), "+v"(fl));
                    #pragma unroll
                    for (int s = 0; s < 4; ++s) sm = mf16(km[s], q[s], sm);
                    asm volatile("" : "+v"(sm));
                    if (__builtin_expect(__builtin_amdgcn_readfirstlane(fl) == tagc, 0)) return true;
                    const float mx = fmx(fmx(fmx(fmx(s0[0], s0[1]), s0[2]), fmx(fmx(s0[3], s1[0]), s1[1])), fmx(s1[2], s1[3]));
                    float v0 = ch ? s1[0] : s0[0], v1 = ch ? s1[1] : s0[1], v2 = ch ? s1[2] : s0[2], v3 = ch ? s1[3] : s0[3];
                    SB();
                    float ma = fmx(fmx(mx, sm[0]), sm[1]), mb = fmx(fmx(mx, sm[1]), sm[0]), m2, m3;
                    float vm = ch ? sm[1] : sm[0];
                    sw16(ma, mb); max3x2(ma, mb, mb, m2, m3); sw32(m2, m3);
                    const float mc = fmx(m2, m3) * CS;
                    v0 = __builtin_amdgcn_exp2f(v0 * CS - mc); v1 = __builtin_amdgcn_exp2f(v1 * CS - mc); v2 = __builtin_amdgcn_exp2f(v2 * CS - mc); v3 = __builtin_amdgcn_exp2f(v3 * CS - mc); vm = __builtin_amdgcn_exp2f(vm * CS - mc);
                    float sa, sb2, sc, sd;
                    addx2((v0 + v1) + v2, v3 + vm, sa, sb2); sw16(sa, sb2); addx2(sa, sb2, sc, sd); sw32(sc, sd);
                    float sum = sc + sd;
                    const float vmo = ror<8>(vm);
                    sum += ror<8>(sum);
                    const float inv = __builtin_amdgcn_rcpf(sum);
                    u32x2 px; px.x = pk2(v0 * inv, v1 * inv); px.y = pk2(v2 * inv, v3 * inv);
                    st64(px, rs, vpx, so);
                    st32(pk2(vm * inv, vmo * inv), rs, vpm, so);
                    return false; };
                FETCH(1, );
                proj2<2>(W, Asrc, pdst, sidx, t, pc, lane, L, false, dg_, ldk);
                ST(2);
                bar();
                ST(3);
                again = scores();
                if (again) { --t; ++nfail; continue; }
                ST(4); LG(0, rt32()); DGL;
            }
        } else if (wave == 1) {
            const int r32 = lane & 31, hf = lane >> 5;
            const int prow = (r32 & 19) | ((r32 & 4) << 1) | ((r32 & 8) >> 1);
            lptr qsrc = L + L_KQX + 8192 + (hf * 32 + r32) * 16;
            lptr ksrc = L + L_KQX + (hf * 32 + prow) * 16;
            lptr msrc = L + L_QK + 2048 + hf * 32 + tokoff(prow & 7);
            const unsigned vax = WS_AX + (h * 2 + (r32 >> 4)) * 1024 + (hf * 16 + (r32 & 15)) * 16;
            const unsigned vax1 = r32 < 16 ? OOB : vax + 512;
            const unsigned vam = WS_AM + ((h >> 2) * 2 + (r32 >> 4)) * 1024 + ((h & 3) * 16 + (r32 & 15)) * 16;
            const int lim = r32 - 8 * hf;
            const Pc pc = mkpc(5, 5, lane);
            for (int t = 0; t < NSEG; ++t) {
                ST_T(t); if (t == 2) { ST_RESET; }
                FETCH(1, );
                if (tagc < 0) drain(); else asm volatile("s_waitcnt vmcnt(1)" ::: "memory");
                st32(0u, rs, (lane || again || t == 0) ? OOB : WS_PP + h * FS - FSEG, t * FSEG);
                proj2<5>(W, Asrc, pdst, sidx, t, pc, lane, L, false, dg_); bar();
                again = failed(tagc, L); if (again) { --t; continue; } DGL;
                f32x16 sx, sm;
                #pragma unroll
                for (int i = 0; i < 16; ++i) { sx[i] = 0; sm[i] = 0; }
                const int po = (t & 1) * 16384;
                #pragma unroll 2
                for (int s = 0; s < 8; ++s) {
                    u32x4 q = lrd(qsrc + po + s * 1024), k = lrd(ksrc + po + s * 1024), km = lrd(msrc + s * 256);
                    sx = mf32(k, q, sx); sm = mf32(km, q, sm);
                }
                float m = sm[0];
                #pragma unroll
                for (int j = 1; j < 8; ++j) m = fmx(m, sm[j]);
                #pragma unroll
                for (int i = 0; i < 16; ++i) { if (16 * (i >> 3) + (i & 7) > lim) sx[i] = -3.0e38f; m = fmx(m, sx[i]); }
                m = x32max(m);
                const float mc = m * CS; float sum = 0, summ = 0;
                #pragma unroll
                for (int i = 0; i < 16; ++i) { sx[i] = __builtin_amdgcn_exp2f(sx[i] * CS - mc); sum += sx[i]; }
                #pragma unroll
                for (int j = 0; j < 8; ++j) { sm[j] = __builtin_amdgcn_exp2f(sm[j] * CS - mc); summ += sm[j]; }
                sum = x32sum(sum) + summ;
                const float inv = __builtin_amdgcn_rcpf(sum);
                #pragma unroll
                for (int p = 0; p < 2; ++p) { u32x4 o; o.x = pk2(sx[8 * p] * inv, sx[8 * p + 1] * inv); o.y = pk2(sx[8 * p + 2] * inv, sx[8 * p + 3] * inv); o.z = pk2(sx[8 * p + 4] * inv, sx[8 * p + 5] * inv); o.w = pk2(sx[8 * p + 6] * inv, sx[8 * p + 7] * inv);
                    st128(o, rs, p ? vax1 : vax, RI(t) * 65536); }
                if (hf == 0) { u32x4 o; o.x = pk2(sm[0] * inv, sm[1] * inv); o.y = pk2(sm[2] * inv, sm[3] * inv); o.z = pk2(sm[4] * inv, sm[5] * inv); o.w = pk2(sm[6] * inv, sm[7] * inv);
                    st128(o, rs, vam, RI(t) * 16384); }
                ST(4);
            }
            drain();
            if (lane == 0) st32(0u, rs, WS_PP + h * FS, (NSEG - 1) * FSEG);
        } else if (wave < 4) {
            const unsigned vsrc = WS_KQ + h * CH + lane * 16;
            lptr ldst = L + L_KQX + (wave - 2) * 8192;
            int seen = -1; unsigned pv = 1;
            const Pc pc = mkpc(0, 0, lane);
            auto fetch = [&](int t) {
                const int pace = seen >= t ? a.tp[7] : 0;
                wait_fl(rs, WS_PQK + h * FS, t, seen, dead, 2);
                pv = ld_fl(rs, t + 1 >= NSEG ? OOB : WS_PQK + h * FS, t + 1);
                const unsigned so = RO(t, wave - 2);
                for (int i = 0; i < 8; ++i) { dma1(rs, ldst + (t & 1) * 16384 + i * 1024, vsrc, so + i * 1024); dly(pace); } };
            fetch(0);
            for (int t = 0; t < NSEG; ++t) {
                ST_T(t); if (t == 2) { ST_RESET; }
                PROJ(4, , false);
                if (fl_ok(pv)) seen = t + 1;
                if (t + 1 < NSEG) fetch(t + 1);
                ST(4);
            }
        } else {
            unsigned tgt = rt32();
            const Pc pc = mkpc(wave - 4, wave + 2, lane);
            const unsigned vcx = WS_SLOT + SL_PX + (h >> 4) * CH + (h & 15) * 512 + lane * 8, vcm = lane < 16 ? WS_SLOT + SL_PM + (lane >> 3) * 2048 + (h >> 1) * 128 + (lane & 7) * 16 + (h & 1) * 8 : OOB;
            for (int t = 0; t < NSEG; ++t) {
                ST_T(t); if (t == 2) { ST_RESET; }
                PROJ(3, gate(tgt), false);
                if (wave == 7) LG(0, tgt);
                if (wave == 6) { st64((u32x2){~0u, ~0u}, rs, vcx, SO(t + 2)); st64((u32x2){~0u, ~0u}, rs, vcm, SO(t + 2)); }
                tgt = (wave == 7 && gated && t > 0) ? read_G(rs, SO(t - 1), lane, dead) + DLT : rt32();
                ST(4);
            }
        }
    } else if (role == R_V) {
        const int h = id;
        u32x8 W[20];
        if (wave < 4) {
            load_w_smf<false>(W, mkrs(a.Wv, D * HE * 2), h * 256, wave, lane, L);
            TL(1);
            if (tid == 0) *(LAS int*)(L + L_FAIL) = -1;
            lptr pdst = L + L_QK + proj2_dst(wave, lane);
            const Pc pc = mkpc(wave, wave, lane);
            lptr Asrc = L + L_A + lane * 16;
            first_mem(a, wave, lane, Asrc);
            const unsigned long long pol = get_pol(rs, lane, dead, tag, ld_pol(rs, lane));
            unsigned tgt = rt32();
            for (int t = 0; t < NSEG; ++t) {
                ST_T(t); if (t == 2) { ST_RESET; }
                PROJ(1, gate(tgt), false);
                tgt = (wave == 3 && gated && t > 0) ? read_G(rs, SO(t - 1), lane, dead) + DLT : rt32();
                ST(4);
            }
        } else {
            load_w_smf<true>(W, mkrs(a.Wo, D * HE * 2), h * E * D * 2, wave, lane, L);
            TL(1);
            const unsigned vu = WS_SLOT + SL_U + (10 * (wave - 4) + 8 + ((lane >> 4) & 1)) * CH + h * 256 + (lane & 15) * 16 + (lane >> 5) * 8;
            const unsigned vw = WS_SLOT + SL_U + (10 * (wave - 4) + (lane >> 4)) * CH + h * 256 + (lane & 15) * 16;
            const Pc pc = mkpc(wave, wave + 2, lane);
            lptr Asrc = L + L_A + lane * 16;
            first_mem(a, wave, lane, Asrc);
            const unsigned long long pol = get_pol(rs, lane, dead, tag, ld_pol(rs, lane));
            ST(0);
            for (int t = 0; t < NSEG; ++t) {
                ST_T(t); if (t == 2) { ST_RESET; }
                f32x4 dd[10];
                int fl;
                unsigned so = SO(t), so2 = so + 4 * CH;
                asm volatile("" : "+s"(so), "+s"(so2));
                auto zero = [&]() {
                    #pragma unroll
                    for (int j = 0; j < 10; ++j) { dd[j] = (f32x4){0, 0, 0, 0}; asm volatile("" : "+v"(dd[j])); } };
                auto ustage = [&]() -> bool {
                    const u32x4 a0 = lrd(L + L_QK + lane * 16); SB();
                    const u32x4 a1 = lrd(L + L_QK + 1024 + lane * 16); SB();
                    fl = *(LAS int*)(L + L_FAIL);
                    SB();
                    unsigned o[10];
                    auto fin = [&](int j) { o[j] = pk2(dd[j][0] + dd[j][2], dd[j][1] + dd[j][3]); };
                    auto s16 = [&](int j) { auto r = __builtin_amdgcn_permlane16_swap(o[j], o[j + 1], false, false); o[j] = r[0]; o[j + 1] = r[1]; };
                    auto s32 = [&](int j) { auto r = __builtin_amdgcn_permlane32_swap(o[j], o[j + 2], false, false); o[j] = r[0]; o[j + 2] = r[1]; };
                    auto m0 = [&](int j) { dd[j] = smf(a0, W[2 * j], dd[j], sidx); };
                    auto m1 = [&](int j) { dd[j] = smf(a1, W[2 * j + 1], dd[j], sidx); };
                    m0(0); m0(1); m0(2); m0(3); SB();
                    m1(0); SB(); const int flu = __builtin_amdgcn_readfirstlane(fl); m1(1); SB(); m1(2); m1(3); asm volatile("" : "+v"(dd[0]), "+v"(dd[1]), "+v"(dd[2]), "+v"(dd[3])); SB();
                    if (__builtin_expect(flu == tagc, 0)) return true;
                    m0(4); fin(0); SB(); m0(5); fin(1); SB(); m0(6); fin(2); SB(); m0(7); s16(0); SB();
                    m1(4); fin(3); SB(); m1(5); SB(); m1(6); s16(2); SB(); m1(7); SB();
                    m0(8); s32(0); s32(1); SB(); m0(9); fin(4); SB();
                    m1(8); st128((u32x4){o[0], o[1], o[2], o[3]}, rs, vw, so); fin(5); SB(); m1(9); fin(6); SB();
                    fin(7); s16(4); fin(8); fin(9); s16(6); s16(8); s32(4); s32(5);
                    st64((u32x2){o[8], o[9]}, rs, vu, so);
                    st128((u32x4){o[4], o[5], o[6], o[7]}, rs, vw, so2);
                    return false; };
                zero();
                if (wave >= 6) { FETCH(2, ); bar(); drain(); bar(); check_own<2>(t, lane, pc, Asrc, L); } else { FETCH(1, ); bar(); drain(); bar(); check_own<1>(t, lane, pc, Asrc, L); }
                bar();
                ST(3);
                again = ustage();
                if (again) { --t; continue; }
                ST(4); LG(0, rt32());
                dly(a.tp[9]);
                const unsigned sc = SO(t + 2);
                st128((u32x4){~0u, ~0u, ~0u, ~0u}, rs, vw, sc); dly(a.tp[13]); st128((u32x4){~0u, ~0u, ~0u, ~0u}, rs, vw, sc + 4 * CH); dly(a.tp[13]);
                st64((u32x2){~0u, ~0u}, rs, vu, sc);
            }
        }
    } else if (role == R_C) {
      const int c = id;
      LAS unsigned* gbox = (LAS unsigned*)(L + 98304);
      constexpr int L_CU = 98560;
      const unsigned long long pol = get_pol(rs, lane, dead, tag, ld_pol(rs, lane));
      const unsigned cmem = WS_SLOT + SL_MEM + (c >> 5) * CH + (c & 31) * 256;
      if (wave >= 4) {
        const unsigned vcf = WS_CF + ((c % 20) * 4 + wave - 4) * CH + lane * 16;
        int seen = -1;
        const unsigned vpv = WS_PVO + (8 * (wave - 4) + (lane & 7)) * FS;
        lptr cdst = L + (wave - 4) * 8192;
        unsigned pv;
        auto ldcf = [&](int t) {
            const bool known = seen >= t;
            wait_fl(rs, vpv, t, seen, dead, 2);
            pv = ld_fl(rs, vpv, t + 1);
            const unsigned so = CO(t, c >= 20);
            for (int i = 0; i < 8; ++i) { dma1(rs, cdst + (t & 1) * 32768 + i * 1024, vcf, so + i * 1024); if (known && i < 7) dly(a.tp[8]); } };
        const unsigned vclr = wave != 7 ? OOB : lane < 32 ? cmem + lane * 8 : lane == 32 ? WS_SLOT + SL_STP + c * 32 : OOB;
        ldcf(0); drain(); bar();
        for (int t = 0;; ++t) {
            if (fl_ok(pv)) seen = t + 1;
            if (t > 0) st64((u32x2){~0u, ~0u}, rs, vclr, SO(t + 2));
            const bool early = seen > t;
            if (t < NSEG - 2 && early) ldcf(t + 1);
            DG(rt32());
            bar();
            bar();
            if (t == NSEG - 2) break;
            if (!early) ldcf(t + 1);
            drain();
            if (!fl_ok(pv)) pv = ld_fl(rs, vpv, t + 2);
            bar();
            DGL;
        }
      } else if (wave >= 2) {
        const unsigned vu = WS_SLOT + SL_U + c * CH + (wave - 2) * 4096 + lane * 16;
        unsigned G = rt32() - 100000u;
        *gbox = G;
        bar();
        for (int t = 0;; ++t) {
            const unsigned so = SO(t);
            unsigned d2_ = 0;
            DG(rt32());
            gate(G + DU);
            bar();
            dma4(rs, L + L_CU + (wave - 2) * 4096, vu, so);
            drain();
            bar();
            if (t == NSEG - 2) break;
            bar();
            if (wave == 2 && gated) {
                dly(a.tp[11]);
                G = read_G(rs, so, lane, dead);
                *gbox = G; }
            if (wave == 3 && c == 0 && t + 4 >= RS) { int sn = -1; wait_fl(rs, WS_PF2 + (lane < 20 ? lane : 0) * FS, t + 4 - RS, sn, dead, 4); }
        }
      } else {
        const int rh = wave, rho = lane & 15, kg = lane >> 4;
        u32x4 cf[32];
        auto ldcf = [&](int t) {
            #pragma unroll
            for (int i = 0; i < 32; ++i) cf[i] = lrd(L + (t & 1) * 32768 + lane * 16 + i * 1024); };
        bar();
        ldcf(0);
        const unsigned opx = WS_SLOT + SL_PX + (2 * (rho >> 3) + (kg >> 1)) * 512 + (kg & 1) * 256 + rh * 128 + (rho & 7) * 16;
        const unsigned opm = WS_SLOT + SL_PM + rh * 2048 + (4 * (rho >> 3) + kg) * 128 + (rho & 7) * 16;
        const unsigned ou = WS_SLOT + SL_U + c * CH + lane * 16;
        const unsigned vmem = (lane & 1) ? OOB : cmem + rh * 128 + (4 * (kg & 1) + 2 * (rho >> 3) + (lane >> 5)) * 16 + (rho & 7) * 2;
        const unsigned vstp = lane ? OOB : WS_SLOT + SL_STP + c * 32 + rh * 4;
        const bool hi = lane >= 32;
        unsigned nlate = 0;
        for (int t = 0; t < NSEG - 1; ++t) {
            ST_T(t); if (t == 2) { ST_RESET; }
            const unsigned sb = SO(t);
            u32x4 p[10], u[8];
            f32x4 ca, cb;
            unsigned res; float v;
            auto ldu = [&]() {
                #pragma unroll
                for (int i = 0; i < 8; ++i) u[i] = ld_sc1(rs, ou + (i & 3) * 1024, sb + (i >> 2) * 4096); };
            auto ldp = [&]() {
                #pragma unroll
                for (int i = 0; i < 8; ++i) { p[i] = ld_sc1(rs, opx + (i & 1) * 2048, sb + (i >> 2) * CH + ((i >> 1) & 1) * 4096); __builtin_amdgcn_sched_barrier(0); }
                p[8] = ld_sc1(rs, opm, sb); __builtin_amdgcn_sched_barrier(0); p[9] = ld_sc1(rs, opm + 1024, sb); };
            auto comb = [&](bool first) {
                #pragma unroll
                for (int f = 0; f < 8; ++f) {
                    ca = smf(p[f], __builtin_shufflevector(cf[4 * f], cf[4 * f + 1], 0, 1, 2, 3, 4, 5, 6, 7), ca, sidx);
                    cb = smf(p[f], __builtin_shufflevector(cf[4 * f + 2], cf[4 * f + 3], 0, 1, 2, 3, 4, 5, 6, 7), cb, sidx);
                    if (first && f == UBAR) { asm volatile("" : "+v"(ca), "+v"(cb)); bar();
                        #pragma unroll
                        for (int i = 0; i < 8; ++i) u[i] = lrd(L + L_CU + lane * 16 + i * 1024); }
                    __builtin_amdgcn_sched_barrier(0); }
                #pragma unroll
                for (int f = 0; f < 2; ++f) {
                    ca = smf(p[8 + f], __builtin_shufflevector(u[4 * f], u[4 * f + 1], 0, 1, 2, 3, 4, 5, 6, 7), ca, sidx);
                    cb = smf(p[8 + f], __builtin_shufflevector(u[4 * f + 2], u[4 * f + 3], 0, 1, 2, 3, 4, 5, 6, 7), cb, sidx);
                    __builtin_amdgcn_sched_barrier(0); }
                const float t0 = hi ? cb[0] + cb[2] : ca[0] + ca[2], t1 = hi ? cb[1] + cb[3] : ca[1] + ca[3];
                v = fold32(t0, t1);
                res = pk2(v, __int_as_float(__builtin_amdgcn_update_dpp(0, __float_as_int(v), 0xB1, 0xf, 0xf, true)));
                asm volatile("" : "+v"(res)); };
            ca = (f32x4){0, 0, 0, 0}; cb = ca;
            asm volatile("" : "+v"(ca), "+v"(cb));
            bar();
            ST(5);
            ldp();
            const unsigned G = __builtin_amdgcn_readfirstlane(*gbox);
            __builtin_amdgcn_sched_barrier(0);
            comb(true);
            if (__builtin_expect(__builtin_amdgcn_ballot_w64(v != v) != 0ull, 0)) {
                for (unsigned sp = 0;; ++sp) {
                    unsigned m = 0, mu = 0;
                    #pragma unroll
                    for (int i = 0; i < 10; ++i) m = mx4(p[i], m);
                    #pragma unroll
                    for (int i = 0; i < 8; ++i) mu = mx4(u[i], mu);
                    if (all_ok(m > mu ? m : mu) || dead) break;
                    if (sp == 0) nfail += all_ok(mu) ? 1 : 0x10000;
                    if (sp > SPIN_MAX) dead = true;
                    asm volatile("" ::: "memory");
                    if (mu == 0xFFFFFFFFu) ldu();
                    if (m == 0xFFFFFFFFu) ldp(); }
                ca = (f32x4){0, 0, 0, 0}; cb = ca;
                comb(false); }
            ST(0);
            st32(res, rs, vmem, sb);
            ST(3);
            const unsigned now = rt32();
            LG(0, now); LG(1, G);
            unsigned Sn = G + DLT;
            const int lt = (int)(now - Sn);
            if (lt > a.tp[5]) { ++nlate; Sn += max((lt + a.tp[6] + 3) & ~3, a.tp[12]); if (t == 0) Sn &= ~3u; }
            st32(nosent(Sn), rs, vstp, sb);
            if (t == NSEG - 2) break;
            bar();
            ldcf(t + 1);
            ST(4);
            if (t == 0) TL(2);
        }
      }
    } else if (role == R_QK || role == R_VO) {
        const int h = id; const bool vo = role == R_VO, st2 = vo && wave >= 4;
        u32x4 W[40], fh;
        if (st2) load_w_32<true>(W, mkrs(a.Wo, D * HE * 2), h * E * D * 2, wave, lane, L, rs, fh);
        else load_w_32<false>(W, mkrs(vo ? a.Wv : wave < 4 ? a.Wq : a.Wk, D * HE * 2), h * 256, wave, lane, L, rs, fh);
        TL(1);
        const unsigned vh = (tid >> 4) * (D * 2) + (tid & 15) * 16;
        lptr wdst = L + (tid & 15) * 512 + (((tid >> 4) ^ (tid & 7)) * 16);
        lptr vbuf = L + 81920;
        bool allh = __builtin_amdgcn_ballot_w64(fh.x != tag.x || fh.y != tag.y || fh.z != tag.x || fh.w != tag.y) == 0ull;
        int seen = -1;
        u32x4 hv[5];
        auto waith = [&](int s) {
            if (!allh) for (unsigned sp = 0;; ++sp) { asm volatile("" ::: "memory");
                const u32x4 f = ld_sc1(rs, WS_FH1 + lane * 16, 0);
                const unsigned long long bad = __builtin_amdgcn_ballot_w64(f.x != tag.x || f.y != tag.y || f.z != tag.x || f.w != tag.y);
                allh = bad == 0ull;
                if (!((bad >> s) & 1ull) || dead) break;
                if (sp > SPIN_MAX) { dead = true; break; }
                dly(2); }
            asm volatile("" ::: "memory"); };
        LAS unsigned* cnt = (LAS unsigned*)(L + 98304);
        unsigned long long pol;
        auto start = [&]() {
            u32x4 h0[5];
            waith(0); waith(1);
            fh = ld_pol(rs, lane);
            #pragma unroll
            for (int i = 0; i < 5; ++i) h0[i] = ld_sc1(rs, vh + i * 256, WS_H1);
            #pragma unroll
            for (int i = 0; i < 5; ++i) hv[i] = ld_sc1(rs, vh + i * 256, WS_H1 + 32 * D * 2);
            SB();
            #pragma unroll
            for (int i = 0; i < 5; ++i) lwr(wdst + i * 8192, h0[i]);
            if (tid == 0) *cnt = 0;
            pol = get_pol(rs, lane, dead, tag, fh);
            bar(); };
        auto stage = [&](int s, int i) { lwr(wdst + ((s + 1) & 1) * 40960 + i * 8192, hv[i]); hv[i] = ld_sc1(rs, vh + i * 256, WS_H1 + (s + 2 < NSEG ? s + 2 : NSEG - 1) * (32 * D * 2)); };
        if (!vo) {
            const unsigned vdst = WS_KQ + h * CH + (wave & 3) * 2048 + lane * 16;
            start();
            for (int s = 0; s < NSEG; ++s) {
                if (s == 1) { ST_RESET; }
                if (s + 2 < NSEG) waith(s + 2);
                ST(0);
                u32x4 o0, o1;
                const unsigned pr = ld_fl(rs, (wave || s < RC - 1) ? OOB : WS_PP + h * FS, s - (RC - 1));
                unsigned old;
                tile640(W, L + (s & 1) * 40960, lane, o0, o1, [&](int ks) { if (ks % 6 == 2 && ks < 30) stage(s, ks / 6);
                    if (ks == QPT) { asm volatile("s_waitcnt vmcnt(%0)" :: "n"(QPT / 6 + 2) : "memory");
                        old = __hip_atomic_fetch_add(cnt + lane, 1u, __ATOMIC_RELAXED, __HIP_MEMORY_SCOPE_WORKGROUP); }
                    if (ks == QPT + 3) st32(0u, rs, ((old & 7u) | lane * 8u) == 7u ? 0 : OOB, WS_PQK + h * FS + s * FSEG - FSEG); });
                const unsigned so = RO(s, wave < 4);
                st128(o0, rs, vdst, so); st128(o1, rs, vdst + 1024, so);
                if (!fl_ok(pr)) wait_ring(rs, WS_PP + h * FS, s - (RC - 1), seen, dead);
                ST(1);
                const bool now = s == 0 || s == NSEG - 1;
                if (now) drain();
                ST(3);
                bar();
                ST(4);
                if (now && tid == 0) st32(0u, rs, 0, WS_PQK + h * FS + s * FSEG);
                if (s == 0) TL(2);
            }
        } else if (!st2) {
            start();
            for (int s = 0; s <= NSEG; ++s) {
                if (s == 1) { ST_RESET; }
                if (s + 2 < NSEG) waith(s + 2);
                ST(0);
                if (s < NSEG) { u32x4 o0, o1;
                    const unsigned pr = ld_fl(rs, (wave != 1 || s < RC) ? OOB : WS_POUT + (lane < 40 ? lane : 0) * FS, s - (RC - 1));
                    tile640(W, L + (s & 1) * 40960, lane, o0, o1, [&](int ks) { if (ks % 6 == 2 && ks < 30) stage(s, ks / 6); });
                    lwr(vbuf + (s & 1) * 8192 + wave * 2048 + lane * 16, o0); lwr(vbuf + (s & 1) * 8192 + wave * 2048 + 1024 + lane * 16, o1);
                    if (!fl_ok(pr)) wait_ring(rs, WS_POUT + (lane < 40 ? lane : 0) * FS, s - (RC - 1), seen, dead); }
                ST(1);
                bar();
                ST(4);
                if (tid == 0 && (s == 1 || s == NSEG)) st32(0u, rs, WS_PVO + h * FS, (s - 1) * FSEG);
                if (s == 1) TL(2);
            }
        } else {
            const int r32 = lane & 31, hf = lane >> 5;
            const int prow = (r32 & 19) | ((r32 & 4) << 1) | ((r32 & 8) >> 1);
            lptr asrc = vbuf + hf * 512 + prow * 16;
            __builtin_amdgcn_s_setprio(1);
            const unsigned vd0 = WS_CF + ((10 * (wave & 1) + (r32 >> 4)) * 4 + (h >> 3)) * CH + (h & 7) * 1024 + (hf * 16 + (r32 & 15)) * 16;
            start();
            for (int s = 0; s <= NSEG; ++s) {
                if (s == 1) { ST_RESET; }
                if (s + 2 < NSEG) waith(s + 2);
                ST(0);
                u32x4 af[8];
                #pragma unroll
                for (int ks = 0; ks < 8; ++ks) af[ks] = lrd(asrc + ((s - 1) & 1) * 8192 + ks * 1024);
                const unsigned so = CO(s > 0 ? s - 1 : 0, wave >= 6), vdst = s > 0 ? vd0 : OOB;
                #pragma unroll
                for (int tt = 0; tt < 5; ++tt) {
                    f32x16 acc;
                    #pragma unroll
                    for (int i = 0; i < 16; ++i) acc[i] = 0;
                    if (s > 0) {
                        #pragma unroll
                        for (int ks = 0; ks < 8; ++ks) acc = mf32(af[ks], W[8 * tt + ks], acc);
                    }
                    stage(s, tt);
                    #pragma unroll
                    for (int p = 0; p < 2; ++p) st128((u32x4){pk2(acc[8 * p], acc[8 * p + 1]), pk2(acc[8 * p + 2], acc[8 * p + 3]), pk2(acc[8 * p + 4], acc[8 * p + 5]), pk2(acc[8 * p + 6], acc[8 * p + 7])}, rs, vdst + p * 512, so + tt * (8 * CH));
                    if (tt == VPT && s > 2) { asm volatile("s_waitcnt vmcnt(%0)" :: "n"(3 * VPT + 3) : "memory");
                        if (lane == 0 && (__hip_atomic_fetch_add(cnt, 1u, __ATOMIC_RELAXED, __HIP_MEMORY_SCOPE_WORKGROUP) & 3u) == 3u) st32(0u, rs, WS_PVO + h * FS, (s - 2) * FSEG); }
                }
                ST(1);
                if (s == 1 || s == NSEG) drain();
                ST(3);
                bar();
                ST(4);
            }
        }
    } else if (role == R_OUT) {
        const int c = id;
        const unsigned lo = (lane >> 5) * 1024 + (lane & 31) * 16;
        const unsigned vcf = WS_CF + ((c % 20) * 4 + (wave >> 1)) * CH + (wave & 1) * 4096 + lo;
        const unsigned vax = WS_AX + wave * 8192 + (lane >> 5) * 2048 + (lane & 31) * 16, vam = WS_AM + wave * 2048 + lane * 16;
        const unsigned vu = WS_SLOT + SL_U + c * CH + wave * 1024 + lane * 16;
        const unsigned long long pol = get_pol(rs, lane, dead, tag, ld_pol(rs, lane));
        lptr part = L + wave * 2560 + (lane & 15) * 80 + (lane >> 4) * 16;
        const int tok = 4 * wave + (lane >> 4), col = lane & 15;
        lptr rsrc = L + tok * 80 + col * 4;
        LAS unsigned* cnt = (LAS unsigned*)(L + 49152);
        const unsigned vx = tok * (D * 2) + c * 32 + col * 2, vo = WS_X1 + c * 1024 + wave * 128 + (((col >> 3) ^ (wave >> 1)) & 1) * 64 + (lane >> 4) * 16 + (col & 7) * 2;
        const unsigned vl = col ? OOB : WS_LS + (tok >> 4) * 5120 + c * 128 + (tok & 15) * 8;
        const unsigned vpp = WS_PP + (4 * wave + (lane & 3)) * FS, vpv = WS_PVO + (4 * wave + (lane & 3)) * FS;
        const rsrc_t rx = mkrs(a.x, S * D * 2);
        const float ga = b2f(*(LAS u16*)(L + L_MOD + 2560 + c * 32 + col * 2));
        int seenv = -1, seenp = -1;
        u32x4 cf[4];
        auto ldcf = [&](int s) {
            wait_fl(rs, vpv, s, seenv, dead, 4);
            const unsigned so = CO(s, c >= 20);
            #pragma unroll
            for (int i = 0; i < 4; ++i) cf[i] = ld_sc1(rs, vcf + (i >> 1) * 2048 + (i & 1) * 512, so); };
        auto done = [&](int s) { if (lane == 0 && (__hip_atomic_fetch_add(cnt, 1u, __ATOMIC_RELAXED, __HIP_MEMORY_SCOPE_WORKGROUP) & 7u) == 7u) st32(0u, rs, WS_POUT + id * FS, s * FSEG); };
        if (tid == 0) *cnt = 0;
        ldcf(0);
        for (int s = 0; s < NSEG; ++s) {
            if (s == 2) { ST_RESET; }
            bool pub = s > 0;
            if (seenp < s) {
                if (pub) { drain(); done(s - 1); pub = false; }
                wait_fl(rs, vpp, s, seenp, dead, s < 63 ? a.tp[17] : 2); }
            ST(0);
            DG(rt32());
            u32x4 ax[6], am0, am1, u;
            #pragma unroll
            for (int j = 0; j < 2; ++j) { ax[3 * j] = ld_sc1(rs, vax, RI(s) * 65536 + j * 4096); ax[3 * j + 1] = ld_sc1(rs, vax + 1024, RI(s) * 65536 + j * 4096); ax[3 * j + 2] = ld_sc1(rs, vax + 1536, RI(s) * 65536 + j * 4096); }
            u = ld_sc1(rs, vu, SO(s));
            am0 = ld_sc1(rs, vam, RI(s) * 16384); am1 = ld_sc1(rs, vam + 1024, RI(s) * 16384);
            SB();
            const unsigned pw = ld_fl(rs, s + 2 < NSEG ? vpv : OOB, s + 2);
            const unsigned xr = __builtin_amdgcn_raw_buffer_load_b16(rx, vx, s * (32 * D * 2), 0);
            SB();
            if (pub) { asm volatile("s_waitcnt vmcnt(11)" ::: "memory"); done(s - 1); }
            ST(1);
            f32x4 d0 = {0, 0, 0, 0}, d1 = d0;
            #pragma unroll
            for (int j = 0; j < 2; ++j) { d0 = mf16(cf[2 * j], ax[3 * j], d0); d1 = mf16(cf[2 * j], ax[3 * j + 1], d1); d1 = mf16(cf[2 * j + 1], ax[3 * j + 2], d1); }
            if (__builtin_expect(!all_ok(mx4(u, 0)), 0))
                for (unsigned sp = 0;; ++sp) { asm volatile("" ::: "memory");
                    u = ld_sc1(rs, vu, SO(s));
                    if (all_ok(mx4(u, 0)) || dead) break;
                    if (sp > SPIN_MAX) { dead = true; break; }
                    dly(2); }
            d0 = mf16(u, am0, d0); d1 = mf16(u, am1, d1);
            lptr pp = part + (s & 1) * 20480;
            *(LAS f32x4*)pp = d0; *(LAS f32x4*)(pp + 1280) = d1;
            const unsigned pv = ld_fl(rs, s + 1 < NSEG ? vpp : OOB, s + 1);
            if (fl_ok(pw)) seenv = s + 2;
            if (s + 1 < NSEG) ldcf(s + 1);
            ST(3);
            bar();
            ST(4);
            float q[8];
            #pragma unroll
            for (int w = 0; w < 8; ++w) q[w] = *(LAS float*)(rsrc + (s & 1) * 20480 + w * 2560);
            const unsigned o = pk2(blo(xr) + rnd(ga * rnd(((q[0] + q[1]) + (q[2] + q[3])) + ((q[4] + q[5]) + (q[6] + q[7])))), 0.f);
            __builtin_amdgcn_raw_buffer_store_b16((unsigned short)o, rs, vo, RI(s) * (32 * D * 2), 16);
            const float of = blo(o);
            st64((u32x2){__float_as_uint(sum16(of)), __float_as_uint(sum16(of * of))}, rs, vl, RI(s) * 10240);
            if (fl_ok(pv)) seenp = s + 1;
            LS2(dg_, rt32());
            ST(5);
            if (s == 0) TL(2);
        }
        drain();
        done(NSEG - 1);
    } else if (role == R_F1) {
        u32x4 W[4][10];
        f32x4 cs[2], bs[2];
        const int kg = lane >> 4, t16 = lane & 15, kh = (wave >> 1) & 1, cw = 2 * (wave & 1) + kh, th = wave >> 2;
        gate(tstart + a.tp[23]);
        #pragma unroll
        for (int mat = 0; mat < 2; ++mat) {
            const rsrc_t rg = mkrs(mat ? a.wu : a.wg, D * F * 2);
            const unsigned vw = (wave * 80 + ((lane >> 1) & 7)) * (F * 2) + id * 128 + (lane >> 4) * 32 + (lane & 1) * 16;
            bar();
            #pragma unroll 1
            for (int i = 0; i < 10; ++i) __builtin_amdgcn_raw_ptr_buffer_load_lds(rg, (LAS void*)(L + (wave * 10 + i) * 1024), 16, vw, i * 8 * F * 2, 0, 0);
            drain(); if (mat) TL(6); else TL(4); bar();
            lptr rp = L + kg * 1024 + cw * 256 + t16 * 8;
            const u32x4 ones = {0x3F803F80u, 0x3F803F80u, 0x3F803F80u, 0x3F803F80u};
            cs[mat] = (f32x4){0, 0, 0, 0}; bs[mat] = cs[mat];
            #pragma unroll 4
            for (int ks = 0; ks < 20; ++ks) { const u32x2 a0 = trd(rp + ks * 4096), a1 = trd(rp + ks * 4096 + 128);
                bs[mat] = mf16((u32x4){a0.x, a0.y, a1.x, a1.y}, lrd(L + L_MOD + 3840 + (32 * ks + 8 * kg) * 2), bs[mat]); }
            bar();
            #pragma unroll 2
            for (int i = 0; i < 10; ++i) { lptr q = L + (wave * 10 + i) * 1024 + lane * 16;
                u32x4 w = lrd(q);
                const float sc = rnd(1.f + b2f(*(LAS u16*)(L + L_MOD + 5120 + (wave * 80 + i * 8 + ((lane >> 1) & 7)) * 2)));
                #pragma unroll
                for (int j = 0; j < 4; ++j) w[j] = pk2(blo(w[j]) * sc, bhi(w[j]) * sc);
                lwr(q, w); }
            bar();
            #pragma unroll 4
            for (int ks = 0; ks < 20; ++ks) { const u32x2 a0 = trd(rp + ks * 4096), a1 = trd(rp + ks * 4096 + 128);
                cs[mat] = mf16((u32x4){a0.x, a0.y, a1.x, a1.y}, ones, cs[mat]); }
            #pragma unroll
            for (int j = 0; j < 2; ++j) { lptr r2 = L + kh * 40960 + kg * 1024 + (cw ^ j) * 256 + t16 * 8;
                #pragma unroll
                for (int i = 0; i < 10; ++i) { const u32x2 a0 = trd(r2 + i * 4096), a1 = trd(r2 + i * 4096 + 128); W[2 * j + mat][i] = (u32x4){a0.x, a0.y, a1.x, a1.y}; } }
            if (!mat) TL(5); }
        TL(2);
        lptr bb = L + kh * 20480 + (kg >> 1) * 1024 + (th * 4 + (t16 >> 2)) * 128 + ((kg ^ (t16 >> 3)) & 1) * 64 + (t16 & 3) * 16;
        lptr stat = L + 81920 + wave * 1280 + lane * 8;
        lptr xw = L + 102400 + wave * 2048 + lane * 16, xr = L + 102400 + (wave ^ 2) * 2048 + lane * 16;
        lptr sw = L + 118784 + wave * 128 + t16 * 8, sr = L + 118784 + th * 512 + t16 * 8;
        volatile LAS int* rdy = (volatile LAS int*)(L + 119808);
        LAS unsigned* cnt = (LAS unsigned*)(L + 119812);
        const int ab = th * 54 + 2 * id + (cw >> 1);
        const unsigned vact = WS_ACT + (ab >> 3) * CH + (ab & 7) * 1024 + ((2 * (cw & 1) + (kg >> 1)) * 16 + t16) * 16 + (kg & 1) * 8;
        const unsigned long long pol = get_pol(rs, lane, dead, tag, ld_pol(rs, lane));
        int seen = -1;
        const unsigned vpo = wave ? OOB : WS_POUT + (lane < 40 ? lane : 0) * FS;
        const unsigned vx1 = WS_X1 + wave * 5120 + lane * 16, vls = WS_LS + tid * 16;
        auto ldp = [&](int s, int i) {
            if (i < 5) dma1(rs, L + (s & 1) * 40960 + wave * 5120 + i * 1024, vx1, RI(s) * (32 * D * 2) + i * 1024);
            else if (i == 5) dma1(rs, L + 81920 + (s & 1) * 10240 + wave * 1024, vls, RI(s) * 10240);
            else if (wave < 2) dma1(rs, L + 81920 + (s & 1) * 10240 + 8192 + wave * 1024, vls, RI(s) * 10240 + 8192); };
        auto ldh = [&](int s) {
            #pragma unroll
            for (int i = 0; i < 7; ++i) ldp(s, i); };
        auto ready = [&](int s) { return __builtin_amdgcn_readfirstlane(*rdy) >= s; };
        wait_fl(rs, vpo, 0, seen, dead, 24);
        if (tid == 0) { *rdy = 0; *cnt = 0; }
        bar();
        ldh(0);
        drain();
        bool pend = false;
        auto done = [&](int s) { if (lane == 0) { unsigned old; asm volatile("ds_add_rtn_u32 %0, %1, %2\n\ts_waitcnt lgkmcnt(0)" : "=v"(old) : "v"(cnt), "v"(1u) : "memory");
            st32(0u, rs, (old & 7u) == 7u ? WS_PF1 + id * FS : OOB, s * FSEG); } };
        for (int s = 0; s < NSEG; ++s) {
            if (s == 2) { ST_RESET; }
            unsigned pv, pw;
            if (wave == 0) asm volatile("buffer_load_dword %0, %2, %4, %5 offen sc1\n\tbuffer_load_dword %1, %3, %4, %6 offen sc1" : "=&v"(pv), "=&v"(pw)
                : "v"((s + 1 < NSEG && seen <= s) ? vpo : OOB), "v"((s + 2 < NSEG && pend) ? vpo : OOB), "s"(rs), "s"((s + 1) * FSEG), "s"((s + 2) * FSEG) : "memory");
            ST(0);
            bar();
            ST(1);
            DG(rt32());
            const int bo = (s & 1) * 40960;
            f32x2 t2, t3, t4;
            u32x4 f[4];
            const int r0 = *rdy;
            #pragma unroll
            for (int m = 0; m < 4; ++m) f[m] = lrd(bb + bo + m * 2048);
            t2 = *(LAS f32x2*)(stat + (s & 1) * 10240); t3 = *(LAS f32x2*)(stat + (s & 1) * 10240 + 512); t4 = *(LAS f32x2*)(stat + (s & 1) * 10240 + 1024);
            SB();
            const bool early = __builtin_amdgcn_readfirstlane(r0) > s && s + 1 < NSEG;
            f32x4 d[4];
            #pragma unroll
            for (int j = 0; j < 4; ++j) d[j] = (f32x4){0, 0, 0, 0};
            #pragma unroll
            for (int i = 0; i < 10; ++i) {
                #pragma unroll
                for (int j = 3; j >= 0; --j) d[j] = mf16(W[j][i], f[i & 3], d[j]);
                asm volatile("" : "+v"(d[0]), "+v"(d[1]), "+v"(d[2]), "+v"(d[3]));
                if (i + 4 < 10) f[i & 3] = lrd(bb + bo + (i + 4) * 2048);
                if (i == 5 && pend) { if (early) asm volatile("s_waitcnt vmcnt(5)" ::: "memory"); else drain(); done(s - 1); }
                if (i < 7 && early) ldp(s + 1, i);
                if (i == 4) { t2 += t3; if (lane < 32) t2 += t4; }
                if (i == 5) { t2[0] = x16sum(t2[0]); t2[1] = x16sum(t2[1]); }
                if (i == 6) { t2[0] = x32sum(t2[0]); t2[1] = x32sum(t2[1]); *(LAS f32x2*)sw = t2; }
                SB(); }
            *(LAS f32x4*)(xw + 1024) = d[3]; *(LAS f32x4*)xw = d[2];
            if (wave == 0) {
                unsigned long long mv, mw;
                if (!pend) drain();
                asm volatile("v_cmp_ne_u32 %0, 0, %2\n\tv_cmp_ne_u32 %1, 0, %3" : "=&s"(mv), "=&s"(mw) : "v"(pv), "v"(pw) : "memory");
                if (pend && s + 2 < NSEG && mw == 0ull) seen = s + 2; else if (mv == 0ull) seen = max(seen, s + 1);
                *rdy = seen; }
            bar();
            const int rv = *rdy;
            t2 = *(LAS f32x2*)sr; t3 = *(LAS f32x2*)(sr + 128); t4 = *(LAS f32x2*)(sr + 256);
            const f32x2 t5 = *(LAS f32x2*)(sr + 384);
            d[2] = *(LAS f32x4*)xr; d[3] = *(LAS f32x4*)(xr + 1024);
            SB();
            bool got = s + 1 >= NSEG || early;
            if (!got && __builtin_amdgcn_readfirstlane(rv) > s) { ldh(s + 1); got = true; }
            ST(2);
            t2 = ((t2 + t3) + (t4 + t5)) * (1.f / D);
            const float rstd = __builtin_amdgcn_rcpf(rnd(__builtin_amdgcn_sqrtf(rnd(rnd(__builtin_fmaxf(t2[1] - t2[0] * t2[0], 0.f)) + 1e-5f)))), mean = rnd(t2[0]);
            const f32x4 g = (d[0] + d[2] - cs[0] * mean) * rstd + bs[0], u = (d[1] + d[3] - cs[1] * mean) * rstd + bs[1];
            float act[4];
            #pragma unroll
            for (int i = 0; i < 4; ++i) act[i] = g[i] * u[i] * __builtin_amdgcn_rcpf(1.f + __expf(-g[i]));
            LS2(dg_, (rt32() & ~1u) | (got ? 1u : 0u));
            st64((u32x2){pk2(act[0], act[1]), pk2(act[2], act[3])}, rs, vact, FO(s));
            ST(4);
            pend = got && s + 1 < NSEG;
            if (pend) asm volatile("s_waitcnt vmcnt(1)" ::: "memory");
            else {
                const bool ok = fl_ok(ld_fl(rs, got ? OOB : vpo, s + 1));
                drain();
                if (wave == 0 && ok) { seen = s + 1; *rdy = s + 1; }
                done(s);
                if (!got) {
                    if (wave == 0) { wait_fl(rs, vpo, s + 1, seen, dead, s < 61 ? a.tp[16] : 2); *rdy = s + 1; }
                    while (!ready(s + 1)) dly(1);
                    ldh(s + 1); drain(); } }
            ST(5);
            if (s == 0) TL(3);
        }
    } else if (role == R_F2) {
        const int cg = id >> 1, mt = id & 1;
        const int ks0 = wave < 6 ? 7 * wave : 42 + 6 * (wave - 6);
        u32x4 W[28];
        gate(tstart + a.tp[23]);
        {   const rsrc_t rw = mkrs(a.wd, F * D * 2);
            const unsigned vw = (32 * ks0 + 8 * (lane >> 4) + ((lane >> 2) & 3)) * (D * 2) + cg * 128 + (lane & 3) * 16;
            u32x4 v[28];
            #pragma unroll
            for (int i = 0; i < 28; ++i) v[i] = ld_pl(rw, vw, (i >= 24 && wave >= 6) ? 0 : (32 * (i >> 2) + 4 * (i & 1)) * (D * 2) + ((i >> 1) & 1) * 64);
            __builtin_amdgcn_sched_barrier(0);
            lptr wp = stg + lane * 16, rp = stg + (lane >> 2) * 64 + (lane & 3) * 8;
            #pragma unroll
            for (int i = 0; i < 28; ++i) { const int pi = i & 3;
                if (i >= 24 && wave >= 6) v[i] = (u32x4){0, 0, 0, 0};
                lwr(wp + pi * 1024, v[i]);
                const u32x2 r0 = trd(rp + pi * 1024), r1 = trd(rp + pi * 1024 + 32);
                const int i0 = (i & ~3) + (pi & 2);
                W[i0][2 * (pi & 1)] = r0.x; W[i0][2 * (pi & 1) + 1] = r0.y; W[i0 + 1][2 * (pi & 1)] = r1.x; W[i0 + 1][2 * (pi & 1) + 1] = r1.y; } }
        TL(2);
        bar();
        const int ab = mt * 54 + ks0;
        const unsigned vact = WS_ACT + lane * 16;
        const unsigned long long pol = get_pol(rs, lane, dead, tag, ld_pol(rs, lane));
        const int pf = ((ks0 >> 1) + (lane & 3)) < 27 ? (ks0 >> 1) + (lane & 3) : 26;
        lptr part = L + wave * 4352 + (lane & 15) * 272 + (lane >> 4) * 16;
        lptr rsrc = L + (tid >> 5) * 272 + (tid & 31) * 8;
        const unsigned vx = (mt * 16 + (tid >> 5)) * (D * 2) + cg * 128 + (tid & 31) * 4;
        const unsigned vx1 = WS_X1 + (cg * 4 + ((tid & 31) >> 3)) * 1024 + (mt * 4 + (tid >> 7)) * 128 + (((tid >> 2) ^ (tid >> 8)) & 1) * 64 + ((tid >> 5) & 3) * 16 + (tid & 3) * 4;
        const unsigned gb = *(LAS unsigned*)(L + L_MOD + 6400 + cg * 128 + (tid & 31) * 4);
        const rsrc_t ro = mkrs(a.out, S * D * 2);
        int seen = -1;
        for (int s = 0; s < NSEG; ++s) {
            wait_fl(rs, WS_PF1 + pf * FS, s, seen, dead, s < 60 ? a.tp[18] : 2);
            DG(rt32());
            u32x4 af[7];
            #pragma unroll
            for (int i = 0; i < 7; ++i) { const int b = ab + ((i == 6 && wave >= 6) ? 0 : i); af[i] = ld_sc1(rs, vact, FO(s) + (b >> 3) * CH + (b & 7) * 1024); }
            const unsigned xr = __builtin_amdgcn_raw_buffer_load_b32(rs, vx1, RI(s) * (32 * D * 2), 16);
            f32x4 d[4];
            #pragma unroll
            for (int j = 0; j < 4; ++j) d[j] = (f32x4){0, 0, 0, 0};
            #pragma unroll
            for (int i = 0; i < 7; ++i)
                #pragma unroll
                for (int j = 0; j < 4; ++j) d[j] = mf16(W[4 * i + j], af[i], d[j]);
            #pragma unroll
            for (int j = 0; j < 4; ++j) *(LAS f32x4*)(part + (s & 1) * 34816 + j * 64) = d[j];
            const unsigned pv = ld_fl(rs, s + 1 < NSEG ? WS_PF1 + pf * FS : OOB, s + 1);
            bar();
            if (tid == 0 && s > 0) st32(0u, rs, WS_PF2 + id * FS, (s - 1) * FSEG);
            f32x2 r = {0, 0};
            #pragma unroll
            for (int w = 0; w < 8; ++w) r += *(LAS f32x2*)(rsrc + (s & 1) * 34816 + w * 4352);
            __builtin_amdgcn_raw_buffer_store_b32(pk2(blo(xr) + rnd(blo(gb) * rnd(r[0])), bhi(xr) + rnd(bhi(gb) * rnd(r[1]))), ro, vx, s * (32 * D * 2), 0);
            if (fl_ok(pv)) seen = s + 1;
            LS2(dg_, rt32());
            if (s == 0) TL(3);
        }
        if (id == 0 && tid == 0) st64(tag, rs, WS_EP, 0);
    }
    TL(7);
    ST_OUT;
}

#define WOFF 0
extern "C" void kernel_launch(void* const* d_in, const int* in_sizes, int n_in, void* d_out, int out_size, void* d_ws, size_t ws_size, hipStream_t stream) {
    unsigned char* wsb = (unsigned char*)(((size_t)d_ws + WOFF + 0x1FFFFF) & ~(size_t)0x1FFFFF);
    Args a{(const u16*)d_in[0], (const u16*)d_in[1], (const u16*)d_in[2], (const u16*)d_in[3], (const u16*)d_in[4], (const u16*)d_in[5],
           (const u16*)d_in[8], (const u16*)d_in[9], (const u16*)d_in[10], (const u16*)d_in[11], (const u16*)d_in[12], (u16*)d_out, wsb, {188, 0, 112, 0, 0, -4, 72, 3, 1, 12, 0, 6, 112, 4, 0, 0, 8, 4, 4, 0, 63, 0, 0, 1400}};
    hipFuncSetAttribute((const void*)mega, hipFuncAttributeMaxDynamicSharedMemorySize, L_TOT);
    mega<<<256, 512, L_TOT, stream>>>(a);
}
```

```cpp
#include <hip/hip_runtime.h>
#include <hip/hip_bf16.h>

typedef unsigned short u16;
typedef __bf16 bf16x8 __attribute__((ext_vector_type(8)));
typedef __bf16 bf16x16 __attribute__((ext_vector_type(16)));
typedef __bf16 bf16x2 __attribute__((ext_vector_type(2)));
typedef short s16x4 __attribute__((ext_vector_type(4)));
typedef float f32x2 __attribute__((ext_vector_type(2)));
typedef float f32x4 __attribute__((ext_vector_type(4)));
typedef float f32x16 __attribute__((ext_vector_type(16)));
typedef unsigned u32x2 __attribute__((ext_vector_type(2)));
typedef unsigned u32x4 __attribute__((ext_vector_type(4)));
typedef unsigned u32x8 __attribute__((ext_vector_type(8)));
typedef __amdgpu_buffer_rsrc_t rsrc_t;
#define LAS __attribute__((address_space(3)))
typedef LAS unsigned char* lptr;

constexpr int S = 2048, D = 640, H = 32, E = 128, HE = 4096, NSEG = 64, F = 1728;
constexpr unsigned WS_EP = 0;
constexpr unsigned WS_FH1 = 1024;
#define FS 32
constexpr unsigned WS_FLG = 262144, FSEG = 192 * FS;
constexpr unsigned WS_PVO = WS_FLG, WS_PQK = WS_FLG + 32 * FS, WS_PP = WS_FLG + 64 * FS, WS_POUT = WS_FLG + 96 * FS, WS_PF2 = WS_FLG + 136 * FS, WS_PF1 = WS_FLG + 160 * FS;
#define RC 16
#define RS 16
constexpr int NPG = 2 * RC > RS ? 2 * RC : RS;
constexpr unsigned WS_POL = 2048;
constexpr unsigned WS_MOD = 4096;
constexpr unsigned WS_SLOT = 2u << 20, SLOT = 2u << 20, CH = 16384;
constexpr unsigned SL_PX = 0, SL_PM = 2 * CH, SL_STP = 2 * CH + 4096, SL_MEM = 3 * CH, SL_U = 5 * CH, SL_END = 45 * CH, SL_CAL = 126 * CH;
#define CFN 0
constexpr unsigned WS_CF = WS_SLOT + (CFN ? 45 * CH : 0), WS_KQ = WS_SLOT + 80 * CH, WS_ACT = WS_SLOT + 112 * CH;
constexpr unsigned WS_H1  = WS_SLOT + NPG * SLOT;
constexpr unsigned WS_AX  = WS_H1 + S * D * 2;
constexpr unsigned WS_AM  = WS_AX + RS * 32 * 2048;
constexpr unsigned WS_X1  = WS_AM + RS * 8 * 2048;
constexpr unsigned WS_LS  = WS_X1 + RS * 32 * D * 2;
constexpr unsigned WS_DBG = WS_LS + RS * 10240;
constexpr unsigned WS_END = WS_DBG + (2 << 20);

__device__ __forceinline__ float b2f(u16 b) { return __uint_as_float((unsigned)b << 16); }
__device__ __forceinline__ float blo(unsigned x) { return __uint_as_float(x << 16); }
__device__ __forceinline__ float bhi(unsigned x) { return __uint_as_float(x & 0xffff0000u); }
__device__ __forceinline__ unsigned pk2(float lo, float hi) { bf16x2 v; v[0] = (__bf16)lo; v[1] = (__bf16)hi; return __builtin_bit_cast(unsigned, v); }
__device__ __forceinline__ float rnd(float f) { return blo(pk2(f, 0.f)); }
__device__ __forceinline__ unsigned nosent(unsigned x) { return x < 0xFFFFFFFEu ? x : 0xFFFFFFFEu; }

struct Args { const u16 *x, *mem0, *Wq, *Wk, *Wv, *Wo, *wg, *wu, *wd, *c, *wm; u16* out; unsigned char* ws; int tp[24]; };
__device__ __forceinline__ void dly(int n) { for (int i = 0; i < n; ++i) __builtin_amdgcn_s_sleep(1); }
__device__ __forceinline__ unsigned long long rtime() { unsigned long long t; asm volatile("s_memrealtime %0\n\ts_waitcnt lgkmcnt(0)" : "=s"(t) :: "memory"); return t; }
constexpr unsigned SPIN_MAX = 200000u, OOB = 0x7FFFF000u;
#define TL(i)
#define ST(i)
#define ST_INIT
#define ST_RESET
#define ST_OUT
#define ST_T(t)
#define LG(k, val)
__device__ __forceinline__ rsrc_t mkrs(const void* p, unsigned bytes) { return __builtin_amdgcn_make_buffer_rsrc((void*)p, 0, (int)bytes, 0x00020000); }
__device__ __forceinline__ u32x4 ld_sc1(rsrc_t r, unsigned voff, unsigned soff) { return __builtin_amdgcn_raw_buffer_load_b128(r, voff, soff, 16); }
__device__ __forceinline__ u32x4 ld_pl(rsrc_t r, unsigned voff, unsigned soff) { return __builtin_amdgcn_raw_buffer_load_b128(r, voff, soff, 0); }
__device__ __forceinline__ void st128(u32x4 v, rsrc_t r, unsigned voff, unsigned soff) { __builtin_amdgcn_raw_buffer_store_b128(v, r, voff, soff, 16); asm volatile("s_nop 1" :: "v"(v)); }
__device__ __forceinline__ void st64(u32x2 v, rsrc_t r, unsigned voff, unsigned soff) { __builtin_amdgcn_raw_buffer_store_b64(v, r, voff, soff, 16); }
__device__ __forceinline__ void st32(unsigned v, rsrc_t r, unsigned voff, unsigned soff) { __builtin_amdgcn_raw_buffer_store_b32(v, r, voff, soff, 16); }
__device__ __forceinline__ unsigned umax3(unsigned a, unsigned b, unsigned c) { unsigned m = a > b ? a : b; return m > c ? m : c; }
__device__ __forceinline__ unsigned mx4(u32x4 v, unsigned m) { return umax3(umax3(v.x, v.y, m), v.z, v.w); }
__device__ __forceinline__ bool all_ok(unsigned m) { return __builtin_amdgcn_ballot_w64(m == 0xFFFFFFFFu) == 0ull; }
__device__ __forceinline__ f32x4 smf(u32x4 a, u32x8 b, f32x4 c, int idx) { return __builtin_amdgcn_smfmac_f32_16x16x64_bf16(__builtin_bit_cast(bf16x8, a), __builtin_bit_cast(bf16x16, b), c, idx, 0, 0); }
__device__ __forceinline__ f32x4 mf16(u32x4 a, u32x4 b, f32x4 c) { return __builtin_amdgcn_mfma_f32_16x16x32_bf16(__builtin_bit_cast(bf16x8, a), __builtin_bit_cast(bf16x8, b), c, 0, 0, 0); }
__device__ __forceinline__ f32x16 mf32(u32x4 a, u32x4 b, f32x16 c) { return __builtin_amdgcn_mfma_f32_32x32x16_bf16(__builtin_bit_cast(bf16x8, a), __builtin_bit_cast(bf16x8, b), c, 0, 0, 0); }
__device__ __forceinline__ float fmx(float a, float b) { return __builtin_elementwise_maximum(a, b); }
__device__ __forceinline__ float fold32(float a, float b) { auto r = __builtin_amdgcn_permlane32_swap(__float_as_uint(a), __float_as_uint(b), false, false); return __uint_as_float(r[0]) + __uint_as_float(r[1]); }
__device__ __forceinline__ float x32max(float a) { auto r = __builtin_amdgcn_permlane32_swap(__float_as_uint(a), __float_as_uint(a), false, false); return fmx(__uint_as_float(r[0]), __uint_as_float(r[1])); }
__device__ __forceinline__ float x32sum(float a) { return fold32(a, a); }
__device__ __forceinline__ float x16max(float a) { auto r = __builtin_amdgcn_permlane16_swap(__float_as_uint(a), __float_as_uint(a), false, false); return fmx(__uint_as_float(r[0]), __uint_as_float(r[1])); }
__device__ __forceinline__ float x16sum(float a) { auto r = __builtin_amdgcn_permlane16_swap(__float_as_uint(a), __float_as_uint(a), false, false); return __uint_as_float(r[0]) + __uint_as_float(r[1]); }
template <int N> __device__ __forceinline__ float ror(float a) { return __int_as_float(__builtin_amdgcn_update_dpp(0, __float_as_int(a), 0x120 + N, 0xf, 0xf, true)); }
template <int C> __device__ __forceinline__ float qp(float a) { return __int_as_float(__builtin_amdgcn_update_dpp(0, __float_as_int(a), C, 0xf, 0xf, true)); }
__device__ __forceinline__ float sum16(float a) { a += ror<8>(a); a += ror<4>(a); a += ror<2>(a); a += ror<1>(a); return a; }
__device__ __forceinline__ void bar() { asm volatile("s_waitcnt lgkmcnt(0)\n\ts_barrier" ::: "memory"); __builtin_amdgcn_s_waitcnt(0xC07F); }
__device__ __forceinline__ void drain() { asm volatile("s_waitcnt vmcnt(0)" ::: "memory"); }
__device__ __forceinline__ u32x2 trd(lptr p) { return __builtin_bit_cast(u32x2, __builtin_amdgcn_ds_read_tr16_b64_v4i16((LAS s16x4*)p)); }
__device__ __forceinline__ u32x4 lrd(lptr p) { return *(LAS u32x4*)p; }
__device__ __forceinline__ void lwr(lptr p, u32x4 v) { *(LAS u32x4*)p = v; }
__device__ __forceinline__ float dot2(unsigned a, unsigned b, float c) { return __builtin_amdgcn_fdot2_f32_bf16(__builtin_bit_cast(bf16x2, a), __builtin_bit_cast(bf16x2, b), c, false); }

__device__ __forceinline__ unsigned ld_fl(rsrc_t rs, unsigned voff, int seg) { asm volatile("" ::: "memory"); return __builtin_amdgcn_raw_buffer_load_b32(rs, voff, seg * FSEG, 16); }
__device__ __forceinline__ bool fl_ok(unsigned v) { const bool r = __builtin_amdgcn_ballot_w64(v != 0u) == 0ull; asm volatile("" ::: "memory"); return r; }
__device__ __forceinline__ void wait_fl(rsrc_t rs, unsigned voff, int seg, int& seen, bool& dead, int slp) {
    if (seen >= seg) return;
    for (unsigned sp = 0;; ++sp) {
        if (fl_ok(ld_fl(rs, voff, seg)) || dead) break;
        if (sp > SPIN_MAX) { dead = true; break; }
        dly(slp);
    }
    seen = seg;
}
__device__ __forceinline__ void wait_ring(rsrc_t rs, unsigned voff, int seg, int& seen, bool& dead) {
    if (seen >= seg) return;
    wait_fl(rs, voff, seg, seen, dead, 8);
}

constexpr int L_A = 0;
constexpr int L_QK = 10240;
constexpr int L_KQX = 14336;
constexpr int L_STG = 49152;
constexpr int L_MOD = 98304;
constexpr int L_TOT = 123392;

#define FHR 4
#define WD 3
#define WAUX 2
template <int KIND> __device__ __forceinline__ unsigned wsoff(int r, int j) {
    const int i = 5 * r + j;
    return KIND == 0 ? (64 * (i >> 2) + 32 * ((i & 3) >> 1) + 2 * (i & 1)) * (HE * 2) : KIND == 1 ? (64 * (r >> 2) + 32 * ((r & 3) >> 1) + (r & 1)) * (D * 2) + j * 256
         : KIND == 2 ? 16 * i * (HE * 2) : (32 * (r >> 1) + 8 * (r & 1)) * (D * 2) + j * 256; }
template <int KIND> __device__ __forceinline__ unsigned wvoff(unsigned base, int wq, int lane) {
    const int q = lane >> 4, jj = 4 * (wq & 1) + q;
    const int row = KIND == 0 ? 16 * (wq >> 1) + 8 * (q >> 1) + 4 * (wq & 1) + (q & 1) : KIND == 1 ? 4 * (q >> 1) + 8 * (wq >> 1) + 2 * (wq & 1) + 16 * (q & 1)
                  : KIND == 2 ? 4 * wq + q : 4 * (wq >> 1) + (jj >> 1) + 16 * (jj & 1);
    return row * ((KIND & 1) ? D * 2 : HE * 2) + base + (lane & 15) * 16;
}
template <int KIND> __device__ __forceinline__ void wissue(u32x4 (&v)[40], rsrc_t rw, unsigned voff, int r0, int r1) {
    __builtin_amdgcn_sched_barrier(0);
    #pragma unroll
    for (int i = 0; i < 40; ++i) if (i >= 5 * r0 && i < 5 * r1) v[i] = __builtin_amdgcn_raw_buffer_load_b128(rw, voff, wsoff<KIND>(i / 5, i % 5), WAUX);
    __builtin_amdgcn_sched_barrier(0);
}
__device__ __forceinline__ u32x2 ld_pol(rsrc_t rs, int lane) { asm volatile("" ::: "memory"); return __builtin_amdgcn_raw_buffer_load_b64(rs, WS_POL + (lane & (NPG - 1)) * 8, 0, 16); }
template <bool WO>
__device__ __forceinline__ void load_w_smf(u32x8 (&W)[20], rsrc_t rw, unsigned base, int wave, int lane, lptr L, rsrc_t rs, u32x2& pv) {
    const int wq = wave & 3;
    u32x4 v[40];
    const unsigned voff = wvoff<WO ? 1 : 0>(base, wq, lane);
    wissue<WO ? 1 : 0>(v, rw, voff, 0, WD);
    lptr wp = L + (wave >> 2) * 20480 + ((lane >> 2) & 3) * 1024 + (4 * wq + (lane >> 4)) * 64 + (lane & 3) * 16;
    lptr rp = L + (wave >> 2) * 20480 + (WO ? 5 * wq : wq) * 1024 + (lane >> 2) * 64 + (lane & 3) * 8;
    #pragma unroll
    for (int r = 0; r < 8; ++r) {
        #pragma unroll
        for (int j = 0; j < 5; ++j) lwr(wp + (r & 1) * 40960 + j * 4096, v[5 * r + j]);
        bar();
        #pragma unroll
        for (int j = 0; j < 5; ++j) { const int i = 5 * r + j, pi = WO ? r & 3 : i & 3;
            lptr p = rp + (r & 1) * 40960 + j * (WO ? 1024 : 4096);
            const u32x2 r0 = trd(p), r1 = trd(p + 32);
            const int i0 = WO ? 4 * j + (r >> 2) : 2 * (i >> 2), i1 = WO ? i0 + 2 : i0 + 1;
            W[i0][2 * pi] = r0.x; W[i0][2 * pi + 1] = r0.y; W[i1][2 * pi] = r1.x; W[i1][2 * pi + 1] = r1.y; }
        wissue<WO ? 1 : 0>(v, rw, voff, r + WD, r + WD + 1);
        if (r == 6) { pv = ld_pol(rs, lane); __builtin_amdgcn_sched_barrier(0); }
    }
    bar();
}
template <bool WO, class FN>
__device__ __forceinline__ void load_w_32(u32x4 (&W)[40], rsrc_t rw, unsigned base, int wave, int lane, lptr L, FN hook) {
    const int wq = wave & 3, G = lane >> 4;
    u32x4 v[40];
    const unsigned voff = wvoff<WO ? 3 : 2>(base, wq, lane);
    wissue<WO ? 3 : 2>(v, rw, voff, 0, WD);
    lptr wp = L + (wave >> 2) * 20480 + ((lane >> 2) & 3) * 1024 + (4 * wq + (lane >> 4)) * 64 + (lane & 3) * 16;
    lptr rp = L + (wave >> 2) * 20480 + (WO ? 5 * wq : wq) * 1024 + (8 * (G >> 1) + ((lane >> 2) & 3)) * 64 + (G & 1) * 32 + (lane & 3) * 8;
    #pragma unroll
    for (int r = 0; r < 8; ++r) {
        #pragma unroll
        for (int j = 0; j < 5; ++j) lwr(wp + (r & 1) * 40960 + j * 4096, v[5 * r + j]);
        bar();
        #pragma unroll
        for (int j = 0; j < 5; ++j) { lptr p = rp + (r & 1) * 40960 + j * (WO ? 1024 : 4096);
            const u32x2 r0 = trd(p), r1 = trd(p + 256);
            W[WO ? 8 * j + r : 5 * r + j] = (u32x4){r0.x, r0.y, r1.x, r1.y}; }
        __builtin_amdgcn_sched_barrier(0); hook(r);
        wissue<WO ? 3 : 2>(v, rw, voff, r + WD, r + WD + 1);
    }
    bar();
}

struct Pc { unsigned g1, g2; int p1, p2; };
__device__ __forceinline__ Pc mkpc(int p1, int p2, int lane) { Pc r; r.p1 = p1; r.p2 = p2; r.g1 = WS_SLOT + SL_MEM + (p1 >> 3) * CH + (p1 & 7) * 1024 + lane * 16; r.g2 = WS_SLOT + SL_MEM + (p2 >> 3) * CH + (p2 & 7) * 1024 + lane * 16; return r; }
template <int NP>
__device__ __forceinline__ void gather_mem(const Args& a, rsrc_t rs, unsigned so, bool fresh, const Pc& pc, lptr A, bool& dead) {
    if (NP == 0) return;
    u32x4 v0 = {~0u, ~0u, ~0u, ~0u}, v1 = {0, 0, 0, 0};
    if (!fresh) { v0 = lrd(A + pc.p1 * 1024); if (NP == 2) v1 = lrd(A + pc.p2 * 1024); }
    for (unsigned sp = 0;; ++sp) {
        unsigned m = mx4(v0, 0); if (NP == 2) m = mx4(v1, m);
        if (all_ok(m) || dead) break;
        if (sp > SPIN_MAX) { dead = true; break; }
        asm volatile("" ::: "memory");
        if (m == 0xFFFFFFFFu) { v0 = ld_sc1(rs, pc.g1, so); if (NP == 2) v1 = ld_sc1(rs, pc.g2, so); }
    }
    lwr(A + pc.p1 * 1024, v0);
    if (NP == 2) lwr(A + pc.p2 * 1024, v1);
}
struct Fm { u32x4 a, b; };
__device__ __forceinline__ Fm ld_mem0(const Args& a, int wave, int l) {
    const u16* s = a.mem0 + (2 * ((l >> 2) & 3) + (l & 1)) * D + 16 * (l >> 4) + 8 * ((l >> 1) & 1);
    Fm r; r.a = *(const u32x4*)(s + 64 * wave); r.b = *(const u32x4*)(s + 64 * ((wave & 1) + 8));
    return r;
}
__device__ __forceinline__ void first_mem(const Fm& m, int wave, lptr A) { lwr(A + wave * 1024, m.a); lwr(A + ((wave & 1) + 8) * 1024, m.b); }
__device__ __forceinline__ unsigned long long get_pol(rsrc_t rs, int lane, bool& dead, u32x2 tag, u32x2 v) {
    for (unsigned sp = 0;; ++sp) {
        if (__builtin_amdgcn_ballot_w64(v.x != tag.x || ((v.y ^ tag.y) << 1) != 0u) == 0ull || dead) break;
        if (sp > SPIN_MAX) { dead = true; break; }
        if (sp) dly(8);
        v = ld_pol(rs, lane); }
    asm volatile("" ::: "memory");
    return __builtin_amdgcn_ballot_w64(v.y != tag.y);
}
#define PG(p) (((unsigned)(p) << 21) + (((unsigned)(pol >> (p)) & 1u) << 13))
#define SO(t) PG((t) & (RS - 1))
#define FO(t) (SO(t) ^ 8192u)
#define RO(s, odd) (PG(2 * ((unsigned)(s) % RC) + (odd)) ^ 8192u)
#define RI(s) ((s) & (RS - 1))
#define CO(s, odd) (RO(s, odd) ^ (CFN ? 8192u : 0u))
__device__ __forceinline__ unsigned rt32() { return (unsigned)__builtin_amdgcn_s_memrealtime(); }
__device__ __forceinline__ void gate(unsigned target) {
    asm volatile("1:\n\ts_memrealtime vcc\n\ts_waitcnt lgkmcnt(0)\n\ts_sub_i32 vcc_lo, %0, vcc_lo\n\ts_cmp_gt_i32 vcc_lo, 0\n\ts_cbranch_scc1 1b" :: "s"(target) : "vcc", "scc", "memory"); }
template <int N> __device__ __forceinline__ int rori(int a) { return __builtin_amdgcn_update_dpp(0, a, 0x120 + N, 0xf, 0xf, true); }
__device__ __forceinline__ unsigned read_G(rsrc_t rs, unsigned so, int lane, bool& dead) {
    u32x2 v;
    for (unsigned sp = 0;; ++sp) { asm volatile("" ::: "memory");
        v = __builtin_amdgcn_raw_buffer_load_b64(rs, WS_SLOT + SL_STP + (lane < 40 ? lane : 0) * 32, so, 16);
        if (all_ok(v.x > v.y ? v.x : v.y) || dead) break;
        if (sp > SPIN_MAX) { dead = true; break; }
        dly(2); }
    const unsigned ref = rt32();
    int d = max((int)(v.x - ref), (int)(v.y - ref));
    d = max(d, rori<8>(d)); d = max(d, rori<4>(d)); d = max(d, rori<2>(d)); d = max(d, rori<1>(d));
    { auto r = __builtin_amdgcn_permlane16_swap(d, d, false, false); d = max((int)r[0], (int)r[1]); }
    { auto r = __builtin_amdgcn_permlane32_swap(d, d, false, false); d = max((int)r[0], (int)r[1]); }
    d = min(max(d, -100000), 600);
    return ref + (unsigned)__builtin_amdgcn_readfirstlane(d);
}
constexpr int L_FAIL = 63488;
constexpr int L_DUM = 63744;
#define LS2(x0, x1)
#define DG(x)
__device__ __forceinline__ void dma1(rsrc_t rs, lptr d, unsigned vo, unsigned so) { __builtin_amdgcn_raw_ptr_buffer_load_lds(rs, (LAS void*)d, 16, vo, so, 0, 16); }
__device__ __forceinline__ void dma4(rsrc_t rs, lptr d, unsigned vo, unsigned so) {
    __builtin_amdgcn_raw_ptr_buffer_load_lds(rs, (LAS void*)d, 16, vo, so, 0, 16); __builtin_amdgcn_raw_ptr_buffer_load_lds(rs, (LAS void*)d, 16, vo, so, 1024, 16);
    __builtin_amdgcn_raw_ptr_buffer_load_lds(rs, (LAS void*)d, 16, vo, so, 2048, 16); __builtin_amdgcn_raw_ptr_buffer_load_lds(rs, (LAS void*)d, 16, vo, so, 3072, 16); }
template <int NP>
__device__ __forceinline__ void dma_mem(rsrc_t rs, unsigned so, const Pc& pc, lptr L) {
    if (NP > 0) __builtin_amdgcn_raw_ptr_buffer_load_lds(rs, (LAS void*)(L + L_A + pc.p1 * 1024), 16, pc.g1, so, 0, 16);
    if (NP > 1) __builtin_amdgcn_raw_ptr_buffer_load_lds(rs, (LAS void*)(L + L_A + pc.p2 * 1024), 16, pc.g2, so, 0, 16);
}
#define FETCH(NP, GATE) do { const unsigned so_ = SO(t > 0 ? t - 1 : 0); \
    if (__builtin_expect(!again && gated && t > 1, 1)) { tagc = t; GATE; ST(0); bar(); ST(5); dma_mem<NP>(rs, so_, pc, L); } \
    else { if (t > 0) gather_mem<NP>(a, rs, so_, !again, pc, Asrc, dead); tagc = -5; } } while (0)
__device__ __forceinline__ void flag(unsigned m, int t, int lane, lptr L) { *(LAS int*)(L + (m == 0xFFFFFFFFu ? L_FAIL : L_DUM + lane * 4)) = t; }
template <int NP>
__device__ __forceinline__ void check_own(int t, int lane, const Pc& pc, lptr A, lptr L) {
    unsigned m = mx4(lrd(A + pc.p1 * 1024), 0);
    if (NP == 2) m = mx4(lrd(A + pc.p2 * 1024), m);
    flag(m, t, lane, L);
}
__device__ __forceinline__ bool failed(int t, lptr L) { return __builtin_expect(__builtin_amdgcn_readfirstlane(*(LAS int*)(L + L_FAIL)) == t, 0); }
#define SB() __builtin_amdgcn_sched_barrier(0)
#define VPT 1
#define QPT 2
#define VE 1
#define VD0 10
#define HAUX 16
#define FQ 6
#define FV 10
#define UBAR 3
#define NCAL 3
#define NB1 8
#define NB2 0
#define PH1 2
template <int B> struct Ic { static constexpr int value = B; };
__device__ __forceinline__ unsigned lds_inc(LAS unsigned* p) { unsigned old; asm volatile("ds_add_rtn_u32 %0, %1, %2" : "=v"(old) : "v"(p), "v"(1u) : "memory"); return old; }
struct Nop { __device__ void operator()() const {} };
template <int KIND, class FN = Nop>
__device__ __forceinline__ void proj2(const u32x8 (&W)[20], lptr Asrc, lptr dstp, int idx, int t, const Pc& pc, int lane, lptr L, bool young, unsigned& dg_, FN mid = Nop()) {
    f32x4 e = {0, 0, 0, 0}, o = {0, 0, 0, 0};
    u32x4 af[10], own, own2;
    asm volatile("" : "+v"(e), "+v"(o));
    if (KIND == 1) { drain(); DG(rt32()); }
    if (KIND == 3) { asm volatile("s_waitcnt vmcnt(1)" ::: "memory"); DG(rt32()); }
    if (KIND == 4) { if (young) asm volatile("s_waitcnt vmcnt(8)" ::: "memory"); else drain(); DG(rt32()); }
    bar();
    #pragma unroll
    for (int ks = 0; ks < 4; ++ks) af[ks] = lrd(Asrc + ks * 1024);
    SB();
    #pragma unroll
    for (int ks = 0; ks < PH1; ++ks) { e = smf(af[ks], W[2 * ks], e, idx); o = smf(af[ks], W[2 * ks + 1], o, idx); }
    asm volatile("" : "+v"(e), "+v"(o));
    if (KIND == 2 || KIND == 3) drain();
    if (KIND == 5) asm volatile("s_waitcnt vmcnt(1)" ::: "memory");
    if (KIND == 2 || KIND == 5) DG(rt32());
    bar();
    if (KIND != 4) own = lrd(Asrc + pc.p1 * 1024);
    if (KIND == 3) own2 = lrd(Asrc + pc.p2 * 1024);
    SB();
    #pragma unroll
    for (int ks = 4; ks < 10; ++ks) af[ks] = lrd(Asrc + ks * 1024);
    SB();
    #pragma unroll
    for (int ks = PH1; ks < 4; ++ks) { e = smf(af[ks], W[2 * ks], e, idx); o = smf(af[ks], W[2 * ks + 1], o, idx); }
    SB();
    if (KIND != 4) { asm volatile("" : "+v"(own)); unsigned m = mx4(own, 0); if (KIND == 3) m = mx4(own2, m); flag(m, t, lane, L); }
    #pragma unroll
    for (int ks = 4; ks < 10; ++ks) { e = smf(af[ks], W[2 * ks], e, idx); o = smf(af[ks], W[2 * ks + 1], o, idx); if (ks == 5) { asm volatile("" : "+v"(e), "+v"(o)); SB(); mid(); SB(); } }
    *(LAS unsigned*)dstp = pk2(e[0] + e[2], o[0] + o[2]); *(LAS unsigned*)(dstp + 16) = pk2(e[1] + e[3], o[1] + o[3]);
}
#define PROJ(KIND, GATE, YOUNG) FETCH((KIND == 3 ? 2 : KIND == 4 ? 0 : 1), GATE); proj2<KIND>(W, Asrc, pdst, sidx, t, pc, lane, L, YOUNG, dg_); ST(2); bar(); ST(3); \
        again = failed(tagc, L); if (again) { --t; continue; } DGL;
__device__ __forceinline__ void max3x2(float a, float b, float c, float& r0, float& r1) { asm("v_maximum3_f32 %0, %2, %3, %4\n\tv_maximum3_f32 %1, %2, %3, %4\n\ts_nop 1" : "=&v"(r0), "=&v"(r1) : "v"(a), "v"(b), "v"(c)); }
__device__ __forceinline__ void addx2(float a, float b, float& r0, float& r1) { asm("v_add_f32 %0, %2, %3\n\tv_add_f32 %1, %2, %3\n\ts_nop 1" : "=&v"(r0), "=&v"(r1) : "v"(a), "v"(b)); }
__device__ __forceinline__ void sw16(float& a, float& b) { auto r = __builtin_amdgcn_permlane16_swap(__float_as_uint(a), __float_as_uint(b), false, false); a = __uint_as_float(r[0]); b = __uint_as_float(r[1]); }
__device__ __forceinline__ void sw32(float& a, float& b) { auto r = __builtin_amdgcn_permlane32_swap(__float_as_uint(a), __float_as_uint(b), false, false); a = __uint_as_float(r[0]); b = __uint_as_float(r[1]); }
__device__ __forceinline__ int proj2_dst(int wq, int lane) { const int c = lane & 15; return (2 * wq + (c >> 3)) * 256 + (4 * (lane >> 4) + 2 * ((c >> 2) & 1)) * 16 + (c & 3) * 4; }
__device__ __forceinline__ int tokoff(int tok) { return (4 * (tok >> 1) + (tok & 1)) * 16; }

__device__ __forceinline__ u32x4 pk8(const f32x16& a, int p) { return (u32x4){pk2(a[4 * p], a[4 * p + 8]), pk2(a[4 * p + 1], a[4 * p + 9]), pk2(a[4 * p + 2], a[4 * p + 10]), pk2(a[4 * p + 3], a[4 * p + 11])}; }

constexpr float CS = 0.08838834764831845f * 1.4426950408889634f;
enum { R_P, R_V, R_C, R_OUT, R_QK, R_VO, R_F1, R_F2, R_SP };
#define FSW 1
#define HM 1
#define RMAP 2
__host__ __device__ inline void rolemap(int b, int& role, int& id) {
    const int grp = b & 7, idx8 = b >> 3;
    if (RMAP == 2) {
        if (grp < 4) {
            if (idx8 < 8) { role = R_P; id = HM ? grp * 8 + idx8 : idx8 * 4 + grp; } else if (idx8 < 16) { role = R_V; id = HM ? grp * 8 + idx8 - 8 : (idx8 - 8) * 4 + grp; } else if (idx8 < 26) { role = R_C; id = grp * 10 + idx8 - 16; }
            else if (FSW) { const int k = (idx8 - 26) * 4 + grp; if (k < 23) { role = R_F1; id = k; } else { role = R_SP; id = 0; } }
            else if (idx8 < 31) { role = R_F2; id = (idx8 - 26) * 4 + grp; } else if (grp < 3) { role = R_F1; id = 24 + grp; } else { role = R_SP; id = 0; }
        } else if (idx8 < 10) { role = R_OUT; id = (grp - 4) * 10 + idx8; }
        else { const int wi = (idx8 - 10) * 4 + grp - 4;
            if (wi < 32) { role = R_VO; id = HM ? (wi & 3) * 8 + (wi >> 2) : wi; } else if (wi < 64) { role = R_QK; id = HM ? (wi & 3) * 8 + ((wi - 32) >> 2) : wi - 32; } else if (!FSW) { role = R_F1; id = wi - 64; }
            else if (wi < 84) { role = R_F2; id = wi - 64; } else { role = R_F1; id = wi - 61; } }
        return;
    }
    if (RMAP == 3) {
        if (idx8 < 16) { role = idx8 < 4 ? R_P : idx8 < 8 ? R_QK : idx8 < 12 ? R_V : R_VO; id = (idx8 & 3) * 8 + grp; }
        else if (idx8 < 21) { role = R_C; id = grp * 5 + idx8 - 16; }
        else if (idx8 < 25) { role = R_OUT; id = grp * 4 + idx8 - 21; }
        else if (idx8 < 27 && grp < 4) { role = R_OUT; id = 32 + grp * 2 + idx8 - 25; }
        else { const int k = idx8 < 27 ? (idx8 - 25) * 4 + grp - 4 : 8 + (idx8 - 27) * 8 + grp;
            if (k < 27) { role = R_F1; id = k; } else if (k < 47) { role = R_F2; id = k - 27; } else { role = R_SP; id = 0; } }
        return;
    }
    if (grp == 0) { role = R_P; id = idx8; return; }
    if (grp == 1) { role = R_V; id = idx8; return; }
    if (RMAP == 0) {
        if (grp == 7) { if (idx8 < 27) { role = R_F1; id = idx8; } else { role = R_F2; id = idx8 - 27; } }
        else if (idx8 < 8) { role = R_C; id = (grp - 2) * 8 + idx8; }
        else if (idx8 < 16) { role = R_OUT; id = (grp - 2) * 8 + idx8 - 8; }
        else { const int wi = (idx8 - 16) * 5 + grp - 2;
            if (wi < 32) { role = R_QK; id = wi; } else if (wi < 64) { role = R_VO; id = wi - 32; } else if (wi < 79) { role = R_F2; id = wi - 59; } else { role = R_SP; id = 0; } }
    } else {
        if (grp < 4) {
            if (idx8 < 20) { role = R_C; id = (grp - 2) * 20 + idx8; }
            else if (idx8 < 30) { role = R_F2; id = (idx8 - 20) * 2 + grp - 2; }
            else { const int k = (idx8 - 30) * 2 + grp - 2; if (k < 3) { role = R_QK; id = 29 + k; } else { role = R_SP; id = 0; } }
        } else if (idx8 < 10) { role = R_OUT; id = (grp - 4) * 10 + idx8; }
        else { const int wi = (idx8 - 10) * 4 + grp - 4;
            if (wi < 32) { role = R_VO; id = wi; } else if (wi < 61) { role = R_QK; id = wi - 32; } else { role = R_F1; id = wi - 61; } }
    }
}
__global__ void __launch_bounds__(512, 2) mega(Args a) {
    extern __shared__ __attribute__((aligned(16))) unsigned char smem[];
    lptr L = (lptr)smem;
    const int tid = threadIdx.x, lane = tid & 63, wave = __builtin_amdgcn_readfirstlane(tid >> 6);
    int role, id; rolemap(blockIdx.x, role, id);
    const int hid = role == R_C ? id : role == R_OUT ? 40 + id : role == R_F1 ? 80 + id : role == R_F2 ? 107 + id : role == R_SP ? 127 : -1;
    const rsrc_t rs = mkrs(a.ws, WS_END);
    const int sidx = (lane & 2) ? 0xEEEE : 0x4444;
    bool dead = false;
    lptr stg = L + L_STG + wave * 4096;
    ST_INIT
    TL(0);
    const unsigned tstart = rt32();
    unsigned long long ep;
    asm volatile("s_load_dwordx2 %0, %1, 0x0\n\ts_waitcnt lgkmcnt(0)" : "=s"(ep) : "s"(a.ws + WS_EP) : "memory");
    ++ep;
    const u32x2 tag = {(unsigned)ep, (unsigned)(ep >> 32)};
    unsigned long long pol = 0;
    if (hid >= 0) {
        const int row = hid * 16 + wave * 2 + (lane >> 5), l32 = lane & 31;
        if (hid < 120) {
            #pragma unroll
            for (int i = 0; i < 2; ++i) { const unsigned u = (i * 120 + hid) * 8192 + tid * 16;
                st128((u32x4){~0u, ~0u, ~0u, ~0u}, rs, u < SL_END ? u : u < 2 * SL_END ? u - SL_END + SLOT : u < 2 * SL_END + 3 * CH ? u - 2 * SL_END + 2 * SLOT + 2 * CH : OOB, WS_SLOT); } }
        if (hid < 96 && tid < 128) st32(~0u, rs, WS_FLG + (hid * 128 + tid) * FS, 0);
        u32x2 xv[5];
        #pragma unroll
        for (int i = 0; i < 5; ++i) xv[i] = *(const u32x2*)(a.x + row * D + (l32 + 32 * i) * 4);
        if (hid < 120) {
            u32x4 wv[5]; float sv[5];
            #pragma unroll
            for (int i = 0; i < 5; ++i) { const int k = (tid >> 2) + 128 * i; wv[i] = *(const u32x4*)(a.wm + k * 3840 + hid * 32 + (tid & 3) * 8); sv[i] = b2f(a.c[k]); }
            float acc[8] = {0, 0, 0, 0, 0, 0, 0, 0};
            #pragma unroll
            for (int i = 0; i < 5; ++i) { const float s = rnd(sv[i] / (1.f + __expf(-sv[i])));
                #pragma unroll
                for (int j = 0; j < 4; ++j) { acc[2 * j] += s * blo(wv[i][j]); acc[2 * j + 1] += s * bhi(wv[i][j]); } }
            #pragma unroll
            for (int j = 0; j < 8; ++j) { float v = acc[j]; v += ror<8>(v); v += ror<4>(v); v = x16sum(v); acc[j] = x32sum(v); }
            if (lane < 4) { *(LAS f32x4*)(L + wave * 128 + lane * 32) = (f32x4){acc[0], acc[1], acc[2], acc[3]}; *(LAS f32x4*)(L + wave * 128 + lane * 32 + 16) = (f32x4){acc[4], acc[5], acc[6], acc[7]}; }
            drain(); bar();
            if (tid < 8) { f32x4 s = {0, 0, 0, 0};
                #pragma unroll
                for (int w = 0; w < 8; ++w) s += *(LAS f32x4*)(L + w * 128 + tid * 16);
                st128((u32x4){pk2(s[0], s[1]), pk2(s[2], s[3]), tag.x, tag.y}, rs, WS_MOD + hid * 128 + tid * 16, 0); }
        }
        float xf[20], s = 0;
        #pragma unroll
        for (int i = 0; i < 5; ++i) { xf[4 * i] = blo(xv[i].x); xf[4 * i + 1] = bhi(xv[i].x); xf[4 * i + 2] = blo(xv[i].y); xf[4 * i + 3] = bhi(xv[i].y); }
        #pragma unroll
        for (int i = 0; i < 20; ++i) s += xf[i];
        const float mean = x16sum(sum16(s)) * (1.f / D);
        float vs = 0;
        #pragma unroll
        for (int i = 0; i < 20; ++i) { const float d = xf[i] - mean; vs += d * d; }
        const float var = x16sum(sum16(vs)) * (1.f / D);
        const float mb = rnd(mean), rd = 1.f / rnd(__builtin_sqrtf(rnd(rnd(var) + 1e-5f)));
        #pragma unroll
        for (int i = 0; i < 20; ++i) xf[i] = rnd(rnd(xf[i] - mb) * rd);
        const bool cal = hid >= 8 && hid < 8 + NPG;
        if (cal) {
            const unsigned vo = lane < 32 ? WS_SLOT + SL_CAL + (wave & 3) * 2048 + (wave >> 2) * CH + lane * 4 : OOB;
            unsigned m[2] = {~0u, ~0u};
            for (int r = 0; r < NCAL; ++r)
                #pragma unroll
                for (int q = 0; q < 2; ++q) { unsigned long long t0, t1;
                    asm volatile("s_waitcnt vmcnt(0)\n\ts_memtime %0\n\ts_waitcnt lgkmcnt(0)" : "=s"(t0) :: "memory");
                    st32(0u, rs, vo, (hid - 8) * SLOT + q * 8192);
                    asm volatile("s_waitcnt vmcnt(0)\n\ts_memtime %0\n\ts_waitcnt lgkmcnt(0)" : "=s"(t1) :: "memory");
                    m[q] = min(m[q], (unsigned)(t1 - t0)); }
            *(LAS u32x2*)(L + 2048 + wave * 8) = (u32x2){m[0], m[1]};
        }
        {   u32x4 mv, mw; const int j = tid < 480 ? tid : 479;
            for (unsigned sp = 0;; ++sp) { asm volatile("" ::: "memory"); mv = ld_sc1(rs, WS_MOD + j * 32, 0); mw = ld_sc1(rs, WS_MOD + j * 32 + 16, 0);
                if (__builtin_amdgcn_ballot_w64(mv.z != tag.x || mv.w != tag.y || mw.z != tag.x || mw.w != tag.y) == 0ull) break;
                if (sp > SPIN_MAX) { dead = true; break; } dly(2); }
            lwr(L + L_MOD + j * 16, (u32x4){mv.x, mv.y, mw.x, mw.y}); }
        bar();
        if (cal && wave == 0) { const u32x2 mm = *(LAS u32x2*)(L + 2048 + (lane & 7) * 8);
            const unsigned bm = (unsigned)__builtin_amdgcn_ballot_w64(mm.y < mm.x);
            st64((u32x2){tag.x, tag.y ^ (__builtin_popcount(bm & 0xFEu) > 3 ? 0x80000000u : 0u)}, rs, lane ? OOB : WS_POL + (hid - 8) * 8, 0); }
        #pragma unroll
        for (int i = 0; i < 5; ++i) { const u32x2 sa = *(LAS u32x2*)(L + L_MOD + (l32 + 32 * i) * 8), ca = *(LAS u32x2*)(L + L_MOD + 1280 + (l32 + 32 * i) * 8);
            u32x2 o;
            o.x = pk2(rnd(xf[4 * i] * rnd(1.f + blo(ca.x))) + blo(sa.x), rnd(xf[4 * i + 1] * rnd(1.f + bhi(ca.x))) + bhi(sa.x));
            o.y = pk2(rnd(xf[4 * i + 2] * rnd(1.f + blo(ca.y))) + blo(sa.y), rnd(xf[4 * i + 3] * rnd(1.f + bhi(ca.y))) + bhi(sa.y));
            *(LAS u32x2*)(L + 4096 + ((l32 >> 1) + 16 * i) * 272 + (row & 15) * 16 + (l32 & 1) * 8) = o; }
        bar();
        #pragma unroll
        for (int i = 0; i < 3; ++i) { const int c = (tid >> 4) + 32 * i;
            st128(lrd(L + 4096 + c * 272 + (tid & 15) * 16), rs, c < 80 ? c * 512 + (tid & 15) * 16 : OOB, WS_H1 + (hid >> 1) * 40960 + (hid & 1) * 256); }
        drain(); bar();
        if (tid == 0) st64(tag, rs, WS_FH1 + hid * 8, 0);
        const u32x2 pv0 = ld_pol(rs, lane);
        TL(1);
        pol = get_pol(rs, lane, dead, tag, pv0);
        TL(4);
    }
    const bool gated = a.tp[0] > 0;
    const unsigned DLT = a.tp[0], DU = a.tp[2];
    int tagc = 0; bool again = false; unsigned nfail = 0, dg_ = 0;
#define DGL
    if (role == R_P) {
        const int h = id;
        u32x8 W[20];
        const Fm fm = ld_mem0(a, wave, lane);
        u32x2 pv0;
        load_w_smf<false>(W, mkrs(wave < 4 ? a.Wq : a.Wk, D * HE * 2), h * 256, wave, lane, L, rs, pv0);
        TL(1);
        if (tid == 0) *(LAS int*)(L + L_FAIL) = -1;
        lptr Asrc = L + L_A + lane * 16;
        lptr pdst = L + L_QK + (wave >> 2) * 2048 + proj2_dst(wave & 3, lane);
        first_mem(fm, wave, Asrc);
        pol = get_pol(rs, lane, dead, tag, pv0);
        asm volatile("" : "+v"(pdst));
        ST(0);
        if (wave == 0) {
            const int rho = lane & 15, g = lane >> 4;
            const Pc pc = mkpc(4, 4, lane);
            const int key = (((rho >> 2) & 1) << 3) | (((rho >> 1) & 1) << 2) | (((rho >> 3) & 1) << 1) | (rho & 1);
            lptr qsrc = L + L_QK + (g >> 1) * 256 + (g & 1) * 32 + tokoff(rho & 7);
            lptr msrc = L + L_QK + 2048 + (g >> 1) * 256 + (g & 1) * 32 + tokoff(2 * (rho >> 2) + (rho & 1));
            lptr ksrc = L + L_KQX + (g * 32 + key) * 16;
            asm volatile("" : "+v"(qsrc), "+v"(msrc));
            const bool ch = (lane & 8) != 0;
            const unsigned vpx = WS_SLOT + SL_PX + (h >> 4) * CH + (h & 15) * 512 + ((lane >> 3) & 1) * 256 + (g >> 1) * 32 + tokoff(lane & 7) + (g & 1) * 8;
            const unsigned vpm = ch ? OOB : WS_SLOT + SL_PM + ((lane >> 2) & 1) * 2048 + (h >> 1) * 128 + tokoff(lane & 3) + (g & 1) * 32 + (h & 1) * 8 + (g >> 1) * 4;
            unsigned long long rt0 = 0;
            for (int t = 0; t < NSEG; ++t) {
                ST_T(t); if (t == 2) { ST_RESET; }
                u32x4 k0[4], k1[4];
                int fl;
                unsigned so = SO(t);
                lptr kp = ksrc + (t & 1) * 16384;
                asm volatile("" : "+s"(so), "+v"(kp));
                auto ldk = [&]() {
                    #pragma unroll
                    for (int s = 0; s < 4; ++s) { k0[s] = lrd(kp + s * 2048); k1[s] = lrd(kp + s * 2048 + 256); } };
                auto scores = [&]() -> bool {
                    u32x4 q[4], km[4];
                    #pragma unroll
                    for (int s = 0; s < 4; ++s) { q[s] = lrd(qsrc + s * 512); SB(); }
                    #pragma unroll
                    for (int s = 0; s < 4; ++s) { km[s] = lrd(msrc + s * 512); SB(); }
                    fl = *(LAS int*)(L + L_FAIL);
                    SB();
                    f32x4 sm = {0, 0, 0, 0}, s0 = sm, s1 = sm;
                    #pragma unroll
                    for (int s = 0; s < 4; ++s) { s0 = mf16(k0[s], q[s], s0); s1 = mf16(k1[s], q[s], s1); }
                    asm volatile("" : "+v"(s0), "+v"(s1), "+v"(km[0]), "+v"(fl));
                    #pragma unroll
                    for (int s = 0; s < 4; ++s) sm = mf16(km[s], q[s], sm);
                    asm volatile("" : "+v"(sm));
                    if (__builtin_expect(__builtin_amdgcn_readfirstlane(fl) == tagc, 0)) return true;
                    const float mx = fmx(fmx(fmx(fmx(s0[0], s0[1]), s0[2]), fmx(fmx(s0[3], s1[0]), s1[1])), fmx(s1[2], s1[3]));
                    float v0 = ch ? s1[0] : s0[0], v1 = ch ? s1[1] : s0[1], v2 = ch ? s1[2] : s0[2], v3 = ch ? s1[3] : s0[3];
                    SB();
                    float ma = fmx(fmx(mx, sm[0]), sm[1]), mb = fmx(fmx(mx, sm[1]), sm[0]), m2, m3;
                    float vm = ch ? sm[1] : sm[0];
                    sw16(ma, mb); max3x2(ma, mb, mb, m2, m3); sw32(m2, m3);
                    const float mc = fmx(m2, m3) * CS;
                    v0 = __builtin_amdgcn_exp2f(v0 * CS - mc); v1 = __builtin_amdgcn_exp2f(v1 * CS - mc); v2 = __builtin_amdgcn_exp2f(v2 * CS - mc); v3 = __builtin_amdgcn_exp2f(v3 * CS - mc); vm = __builtin_amdgcn_exp2f(vm * CS - mc);
                    float sa, sb2, sc, sd;
                    addx2((v0 + v1) + v2, v3 + vm, sa, sb2); sw16(sa, sb2); addx2(sa, sb2, sc, sd); sw32(sc, sd);
                    float sum = sc + sd;
                    const float vmo = ror<8>(vm);
                    sum += ror<8>(sum);
                    const float inv = __builtin_amdgcn_rcpf(sum);
                    u32x2 px; px.x = pk2(v0 * inv, v1 * inv); px.y = pk2(v2 * inv, v3 * inv);
                    st64(px, rs, vpx, so);
                    st32(pk2(vm * inv, vmo * inv), rs, vpm, so);
                    return false; };
                FETCH(1, );
                proj2<2>(W, Asrc, pdst, sidx, t, pc, lane, L, false, dg_, ldk);
                ST(2);
                bar();
                ST(3);
                again = scores();
                if (again) { --t; ++nfail; continue; }
                ST(4); LG(0, rt32()); DGL;
            }
        } else if (wave == 1) {
            const int r32 = lane & 31, hf = lane >> 5;
            const int prow = (r32 & 19) | ((r32 & 4) << 1) | ((r32 & 8) >> 1);
            lptr qsrc = L + L_KQX + 8192 + (hf * 32 + r32) * 16;
            lptr ksrc = L + L_KQX + (hf * 32 + prow) * 16;
            lptr msrc = L + L_QK + 2048 + hf * 32 + tokoff(prow & 7);
            const unsigned vax = WS_AX + (h * 2 + (r32 >> 4)) * 1024 + (hf * 16 + (r32 & 15)) * 16;
            const unsigned vax1 = r32 < 16 ? OOB : vax + 512;
            const unsigned vam = WS_AM + ((h >> 2) * 2 + (r32 >> 4)) * 1024 + ((h & 3) * 16 + (r32 & 15)) * 16;
            const int lim = r32 - 8 * hf;
            const Pc pc = mkpc(5, 5, lane);
            for (int t = 0; t < NSEG; ++t) {
                ST_T(t); if (t == 2) { ST_RESET; }
                FETCH(1, );
                if (tagc < 0) drain(); else asm volatile("s_waitcnt vmcnt(1)" ::: "memory");
                st32(0u, rs, (lane || again || t == 0) ? OOB : WS_PP + h * FS - FSEG, t * FSEG);
                proj2<5>(W, Asrc, pdst, sidx, t, pc, lane, L, false, dg_); bar();
                again = failed(tagc, L); if (again) { --t; continue; } DGL;
                f32x16 sx, sm;
                #pragma unroll
                for (int i = 0; i < 16; ++i) { sx[i] = 0; sm[i] = 0; }
                const int po = (t & 1) * 16384;
                #pragma unroll 2
                for (int s = 0; s < 8; ++s) {
                    u32x4 q = lrd(qsrc + po + s * 1024), k = lrd(ksrc + po + s * 1024), km = lrd(msrc + s * 256);
                    sx = mf32(k, q, sx); sm = mf32(km, q, sm);
                }
                float m = sm[0];
                #pragma unroll
                for (int j = 1; j < 8; ++j) m = fmx(m, sm[j]);
                #pragma unroll
                for (int i = 0; i < 16; ++i) { if (16 * (i >> 3) + (i & 7) > lim) sx[i] = -3.0e38f; m = fmx(m, sx[i]); }
                m = x32max(m);
                const float mc = m * CS; float sum = 0, summ = 0;
                #pragma unroll
                for (int i = 0; i < 16; ++i) { sx[i] = __builtin_amdgcn_exp2f(sx[i] * CS - mc); sum += sx[i]; }
                #pragma unroll
                for (int j = 0; j < 8; ++j) { sm[j] = __builtin_amdgcn_exp2f(sm[j] * CS - mc); summ += sm[j]; }
                sum = x32sum(sum) + summ;
                const float inv = __builtin_amdgcn_rcpf(sum);
                #pragma unroll
                for (int p = 0; p < 2; ++p) { u32x4 o; o.x = pk2(sx[8 * p] * inv, sx[8 * p + 1] * inv); o.y = pk2(sx[8 * p + 2] * inv, sx[8 * p + 3] * inv); o.z = pk2(sx[8 * p + 4] * inv, sx[8 * p + 5] * inv); o.w = pk2(sx[8 * p + 6] * inv, sx[8 * p + 7] * inv);
                    st128(o, rs, p ? vax1 : vax, RI(t) * 65536); }
                if (hf == 0) { u32x4 o; o.x = pk2(sm[0] * inv, sm[1] * inv); o.y = pk2(sm[2] * inv, sm[3] * inv); o.z = pk2(sm[4] * inv, sm[5] * inv); o.w = pk2(sm[6] * inv, sm[7] * inv);
                    st128(o, rs, vam, RI(t) * 16384); }
                ST(4);
            }
            drain();
            if (lane == 0) st32(0u, rs, WS_PP + h * FS, (NSEG - 1) * FSEG);
        } else if (wave < 4) {
            const unsigned vsrc = WS_KQ + h * CH + lane * 16;
            lptr ldst = L + L_KQX + (wave - 2) * 8192;
            int seen = -1; unsigned pv = 1;
            const Pc pc = mkpc(0, 0, lane);
            auto fetch = [&](int t) {
                const int pace = seen >= t ? a.tp[7] : 0;
                wait_fl(rs, WS_PQK + h * FS, t, seen, dead, 2);
                pv = ld_fl(rs, t + 1 >= NSEG ? OOB : WS_PQK + h * FS, t + 1);
                const unsigned so = RO(t, wave - 2);
                for (int i = 0; i < 8; ++i) { dma1(rs, ldst + (t & 1) * 16384 + i * 1024, vsrc, so + i * 1024); dly(pace); } };
            fetch(0);
            for (int t = 0; t < NSEG; ++t) {
                ST_T(t); if (t == 2) { ST_RESET; }
                PROJ(4, , false);
                if (fl_ok(pv)) seen = t + 1;
                if (t + 1 < NSEG) fetch(t + 1);
                ST(4);
            }
        } else {
            unsigned tgt = rt32();
            const Pc pc = mkpc(wave - 4, wave + 2, lane);
            const unsigned vcx = WS_SLOT + SL_PX + (h >> 4) * CH + (h & 15) * 512 + lane * 8, vcm = lane < 16 ? WS_SLOT + SL_PM + (lane >> 3) * 2048 + (h >> 1) * 128 + (lane & 7) * 16 + (h & 1) * 8 : OOB;
            for (int t = 0; t < NSEG; ++t) {
                ST_T(t); if (t == 2) { ST_RESET; }
                PROJ(3, gate(tgt), false);
                if (wave == 7) LG(0, tgt);
                if (wave == 6) { st64((u32x2){~0u, ~0u}, rs, vcx, SO(t + 2)); st64((u32x2){~0u, ~0u}, rs, vcm, SO(t + 2)); }
                tgt = (wave == 7 && gated && t > 0) ? read_G(rs, SO(t - 1), lane, dead) + DLT : rt32();
                ST(4);
            }
        }
    } else if (role == R_V) {
        const int h = id;
        u32x8 W[20];
        if (wave < 4) {
            const Fm fm = ld_mem0(a, wave, lane);
            u32x2 pv0;
            load_w_smf<false>(W, mkrs(a.Wv, D * HE * 2), h * 256, wave, lane, L, rs, pv0);
            TL(1);
            if (tid == 0) *(LAS int*)(L + L_FAIL) = -1;
            lptr pdst = L + L_QK + proj2_dst(wave, lane);
            const Pc pc = mkpc(wave, wave, lane);
            lptr Asrc = L + L_A + lane * 16;
            first_mem(fm, wave, Asrc);
            pol = get_pol(rs, lane, dead, tag, pv0);
            unsigned tgt = rt32();
            for (int t = 0; t < NSEG; ++t) {
                ST_T(t); if (t == 2) { ST_RESET; }
                PROJ(1, gate(tgt), false);
                tgt = (wave == 3 && gated && t > 0) ? read_G(rs, SO(t - 1), lane, dead) + DLT : rt32();
                ST(4);
            }
        } else {
            const Fm fm = ld_mem0(a, wave, lane);
            u32x2 pv0;
            load_w_smf<true>(W, mkrs(a.Wo, D * HE * 2), h * E * D * 2, wave, lane, L, rs, pv0);
            TL(1);
            const unsigned vu = WS_SLOT + SL_U + (10 * (wave - 4) + 8 + ((lane >> 4) & 1)) * CH + h * 256 + (lane & 15) * 16 + (lane >> 5) * 8;
            const unsigned vw = WS_SLOT + SL_U + (10 * (wave - 4) + (lane >> 4)) * CH + h * 256 + (lane & 15) * 16;
            const Pc pc = mkpc(wave, wave + 2, lane);
            lptr Asrc = L + L_A + lane * 16;
            first_mem(fm, wave, Asrc);
            pol = get_pol(rs, lane, dead, tag, pv0);
            ST(0);
            for (int t = 0; t < NSEG; ++t) {
                ST_T(t); if (t == 2) { ST_RESET; }
                f32x4 dd[10];
                int fl;
                unsigned so = SO(t), so2 = so + 4 * CH;
                asm volatile("" : "+s"(so), "+s"(so2));
                auto zero = [&]() {
                    #pragma unroll
                    for (int j = 0; j < 10; ++j) { dd[j] = (f32x4){0, 0, 0, 0}; asm volatile("" : "+v"(dd[j])); } };
                auto ustage = [&]() -> bool {
                    const u32x4 a0 = lrd(L + L_QK + lane * 16); SB();
                    const u32x4 a1 = lrd(L + L_QK + 1024 + lane * 16); SB();
                    fl = *(LAS int*)(L + L_FAIL);
                    SB();
                    unsigned o[10];
                    auto fin = [&](int j) { o[j] = pk2(dd[j][0] + dd[j][2], dd[j][1] + dd[j][3]); };
                    auto s16 = [&](int j) { auto r = __builtin_amdgcn_permlane16_swap(o[j], o[j + 1], false, false); o[j] = r[0]; o[j + 1] = r[1]; };
                    auto s32 = [&](int j) { auto r = __builtin_amdgcn_permlane32_swap(o[j], o[j + 2], false, false); o[j] = r[0]; o[j + 2] = r[1]; };
                    auto m0 = [&](int j) { dd[j] = smf(a0, W[2 * j], dd[j], sidx); };
                    auto m1 = [&](int j) { dd[j] = smf(a1, W[2 * j + 1], dd[j], sidx); };
                    m0(0); m0(1); m0(2); m0(3); SB();
                    m1(0); SB(); const int flu = __builtin_amdgcn_readfirstlane(fl); m1(1); SB(); m1(2); m1(3); asm volatile("" : "+v"(dd[0]), "+v"(dd[1]), "+v"(dd[2]), "+v"(dd[3])); SB();
                    if (__builtin_expect(flu == tagc, 0)) return true;
                    m0(4); fin(0); SB(); m0(5); fin(1); SB(); m0(6); fin(2); SB(); m0(7); s16(0); SB();
                    m1(4); fin(3); SB(); m1(5); SB(); m1(6); s16(2); SB(); m1(7); SB();
                    m0(8); s32(0); s32(1); SB(); m0(9); fin(4); SB();
                    m1(8); st128((u32x4){o[0], o[1], o[2], o[3]}, rs, vw, so); fin(5); SB(); m1(9); fin(6); SB();
                    fin(7); s16(4); fin(8); fin(9); s16(6); s16(8); s32(4); s32(5);
                    st64((u32x2){o[8], o[9]}, rs, vu, so);
                    st128((u32x4){o[4], o[5], o[6], o[7]}, rs, vw, so2);
                    return false; };
                zero();
                if (wave >= 6) { FETCH(2, ); bar(); drain(); bar(); check_own<2>(t, lane, pc, Asrc, L); } else { FETCH(1, ); bar(); drain(); bar(); check_own<1>(t, lane, pc, Asrc, L); }
                bar();
                ST(3);
                again = ustage();
                if (again) { --t; continue; }
                ST(4); LG(0, rt32());
                dly(a.tp[9]);
                const unsigned sc = SO(t + 2);
                st128((u32x4){~0u, ~0u, ~0u, ~0u}, rs, vw, sc); dly(a.tp[13]); st128((u32x4){~0u, ~0u, ~0u, ~0u}, rs, vw, sc + 4 * CH); dly(a.tp[13]);
                st64((u32x2){~0u, ~0u}, rs, vu, sc);
            }
        }
    } else if (role == R_C) {
      const int c = id;
      LAS unsigned* gbox = (LAS unsigned*)(L + 98304);
      constexpr int L_CU = 98560;
      const unsigned cmem = WS_SLOT + SL_MEM + (c >> 5) * CH + (c & 31) * 256;
      if (wave >= 4) {
        const unsigned vcf = WS_CF + ((c % 20) * 4 + wave - 4) * CH + lane * 16;
        int seen = -1;
        const unsigned vpv = WS_PVO + (8 * (wave - 4) + (lane & 7)) * FS;
        lptr cdst = L + (wave - 4) * 8192;
        unsigned pv;
        auto ldcf = [&](int t) {
            const bool known = seen >= t;
            wait_fl(rs, vpv, t, seen, dead, 2);
            pv = ld_fl(rs, vpv, t + 1);
            const unsigned so = CO(t, c >= 20);
            for (int i = 0; i < 8; ++i) { dma1(rs, cdst + (t & 1) * 32768 + i * 1024, vcf, so + i * 1024); if (known && i < 7) dly(a.tp[8]); } };
        const unsigned vclr = wave != 7 ? OOB : lane < 32 ? cmem + lane * 8 : lane == 32 ? WS_SLOT + SL_STP + c * 32 : OOB;
        ldcf(0); drain(); bar();
        for (int t = 0;; ++t) {
            if (fl_ok(pv)) seen = t + 1;
            if (t > 0) st64((u32x2){~0u, ~0u}, rs, vclr, SO(t + 2));
            const bool early = seen > t;
            if (t < NSEG - 2 && early) ldcf(t + 1);
            DG(rt32());
            bar();
            bar();
            if (t == NSEG - 2) break;
            if (!early) ldcf(t + 1);
            drain();
            if (!fl_ok(pv)) pv = ld_fl(rs, vpv, t + 2);
            bar();
            DGL;
        }
      } else if (wave >= 2) {
        const unsigned vu = WS_SLOT + SL_U + c * CH + (wave - 2) * 4096 + lane * 16;
        unsigned G = rt32() - 100000u;
        *gbox = G;
        bar();
        for (int t = 0;; ++t) {
            const unsigned so = SO(t);
            unsigned d2_ = 0;
            DG(rt32());
            gate(G + DU);
            bar();
            dma4(rs, L + L_CU + (wave - 2) * 4096, vu, so);
            drain();
            bar();
            if (t == NSEG - 2) break;
            bar();
            if (wave == 2 && gated) {
                dly(a.tp[11]);
                G = read_G(rs, so, lane, dead);
                *gbox = G; }
            if (wave == 3 && c == 0 && t + 4 >= RS) { int sn = -1; wait_fl(rs, WS_PF2 + (lane < 20 ? lane : 0) * FS, t + 4 - RS, sn, dead, 4); }
        }
      } else {
        const int rh = wave, rho = lane & 15, kg = lane >> 4;
        u32x4 cf[32];
        auto ldcf = [&](int t) {
            #pragma unroll
            for (int i = 0; i < 32; ++i) cf[i] = lrd(L + (t & 1) * 32768 + lane * 16 + i * 1024); };
        bar();
        ldcf(0);
        const unsigned opx = WS_SLOT + SL_PX + (2 * (rho >> 3) + (kg >> 1)) * 512 + (kg & 1) * 256 + rh * 128 + (rho & 7) * 16;
        const unsigned opm = WS_SLOT + SL_PM + rh * 2048 + (4 * (rho >> 3) + kg) * 128 + (rho & 7) * 16;
        const unsigned ou = WS_SLOT + SL_U + c * CH + lane * 16;
        const unsigned vmem = (lane & 1) ? OOB : cmem + rh * 128 + (4 * (kg & 1) + 2 * (rho >> 3) + (lane >> 5)) * 16 + (rho & 7) * 2;
        const unsigned vstp = lane ? OOB : WS_SLOT + SL_STP + c * 32 + rh * 4;
        const bool hi = lane >= 32;
        unsigned nlate = 0;
        for (int t = 0; t < NSEG - 1; ++t) {
            ST_T(t); if (t == 2) { ST_RESET; }
            const unsigned sb = SO(t);
            u32x4 p[10], u[8];
            f32x4 ca, cb;
            unsigned res; float v;
            auto ldu = [&]() {
                #pragma unroll
                for (int i = 0; i < 8; ++i) u[i] = ld_sc1(rs, ou + (i & 3) * 1024, sb + (i >> 2) * 4096); };
            auto ldp = [&]() {
                #pragma unroll
                for (int i = 0; i < 8; ++i) { p[i] = ld_sc1(rs, opx + (i & 1) * 2048, sb + (i >> 2) * CH + ((i >> 1) & 1) * 4096); __builtin_amdgcn_sched_barrier(0); }
                p[8] = ld_sc1(rs, opm, sb); __builtin_amdgcn_sched_barrier(0); p[9] = ld_sc1(rs, opm + 1024, sb); };
            auto comb = [&](bool first) {
                #pragma unroll
                for (int f = 0; f < 8; ++f) {
                    ca = smf(p[f], __builtin_shufflevector(cf[4 * f], cf[4 * f + 1], 0, 1, 2, 3, 4, 5, 6, 7), ca, sidx);
                    cb = smf(p[f], __builtin_shufflevector(cf[4 * f + 2], cf[4 * f + 3], 0, 1, 2, 3, 4, 5, 6, 7), cb, sidx);
                    if (first && f == UBAR) { asm volatile("" : "+v"(ca), "+v"(cb)); bar();
                        #pragma unroll
                        for (int i = 0; i < 8; ++i) u[i] = lrd(L + L_CU + lane * 16 + i * 1024); }
                    __builtin_amdgcn_sched_barrier(0); }
                #pragma unroll
                for (int f = 0; f < 2; ++f) {
                    ca = smf(p[8 + f], __builtin_shufflevector(u[4 * f], u[4 * f + 1], 0, 1, 2, 3, 4, 5, 6, 7), ca, sidx);
                    cb = smf(p[8 + f], __builtin_shufflevector(u[4 * f + 2], u[4 * f + 3], 0, 1, 2, 3, 4, 5, 6, 7), cb, sidx);
                    __builtin_amdgcn_sched_barrier(0); }
                const float t0 = hi ? cb[0] + cb[2] : ca[0] + ca[2], t1 = hi ? cb[1] + cb[3] : ca[1] + ca[3];
                v = fold32(t0, t1);
                res = pk2(v, __int_as_float(__builtin_amdgcn_update_dpp(0, __float_as_int(v), 0xB1, 0xf, 0xf, true)));
                asm volatile("" : "+v"(res)); };
            ca = (f32x4){0, 0, 0, 0}; cb = ca;
            asm volatile("" : "+v"(ca), "+v"(cb));
            bar();
            ST(5);
            ldp();
            const unsigned G = __builtin_amdgcn_readfirstlane(*gbox);
            __builtin_amdgcn_sched_barrier(0);
            comb(true);
            if (__builtin_expect(__builtin_amdgcn_ballot_w64(v != v) != 0ull, 0)) {
                for (unsigned sp = 0;; ++sp) {
                    unsigned m = 0, mu = 0;
                    #pragma unroll
                    for (int i = 0; i < 10; ++i) m = mx4(p[i], m);
                    #pragma unroll
                    for (int i = 0; i < 8; ++i) mu = mx4(u[i], mu);
                    if (all_ok(m > mu ? m : mu) || dead) break;
                    if (sp == 0) nfail += all_ok(mu) ? 1 : 0x10000;
                    if (sp > SPIN_MAX) dead = true;
                    asm volatile("" ::: "memory");
                    if (mu == 0xFFFFFFFFu) ldu();
                    if (m == 0xFFFFFFFFu) ldp(); }
                ca = (f32x4){0, 0, 0, 0}; cb = ca;
                comb(false); }
            ST(0);
            st32(res, rs, vmem, sb);
            ST(3);
            const unsigned now = rt32();
            LG(0, now); LG(1, G);
            unsigned Sn = G + DLT;
            const int lt = (int)(now - Sn);
            if (lt > a.tp[5]) { ++nlate; Sn += max((lt + a.tp[6] + 3) & ~3, a.tp[12]); if (t == 0) Sn &= ~3u; }
            st32(nosent(Sn), rs, vstp, sb);
            if (t == NSEG - 2) break;
            bar();
            ldcf(t + 1);
            ST(4);
            if (t == 0) TL(2);
        }
      }
    } else if (role == R_QK || role == R_VO) {
        const int h = id; const bool vo = role == R_VO, st2 = vo && wave >= 4;
        u32x4 W[40];
        bool allh = false;
        int seen = -1;
        auto waith = [&](int s) {
            if (!allh) for (unsigned sp = 0;; ++sp) { asm volatile("" ::: "memory");
                const u32x4 f = ld_sc1(rs, WS_FH1 + lane * 16, 0);
                const unsigned long long bad = __builtin_amdgcn_ballot_w64(f.x != tag.x || f.y != tag.y || f.z != tag.x || f.w != tag.y);
                allh = bad == 0ull;
                if (!((bad >> s) & 1ull) || dead) break;
                if (sp > SPIN_MAX) { dead = true; break; }
                dly(2); }
            asm volatile("" ::: "memory"); };
        LAS unsigned* cnt = (LAS unsigned*)(L + 122880);
        auto img = [&](int s) { return L + ((s & 1) ? 0 : 81920); };
        auto dmah = [&](int s, int i, unsigned v) { __builtin_amdgcn_raw_ptr_buffer_load_lds(rs, (LAS void*)(img(s) + i * 1024), 16, v, WS_H1 + s * 40960 + i * 1024, 0, HAUX); };
        auto start = [&](auto kind) {
            u32x4 fh; u32x2 pv0;
            bool got;
            auto ldh = [&](unsigned v) {
                #pragma unroll
                for (int i = 0; i < 5; ++i) dmah(0, 5 * wave + i, v); };
            auto hook = [&](int r) {
                if (r == FHR) fh = ld_sc1(rs, WS_FH1 + lane * 16, 0);
                if (r == 6) { const unsigned long long bad = __builtin_amdgcn_ballot_w64(fh.x != tag.x || fh.y != tag.y || fh.z != tag.x || fh.w != tag.y);
                    allh = bad == 0ull; got = (bad & 1ull) == 0ull;
                    pv0 = ld_pol(rs, lane); ldh(got ? lane * 16 : OOB); } };
            if (kind.value == 1) load_w_32<false>(W, mkrs(vo ? a.Wv : wave < 4 ? a.Wq : a.Wk, D * HE * 2), h * 256, wave, lane, L, hook);
            if (kind.value == 2) load_w_32<true>(W, mkrs(a.Wo, D * HE * 2), h * E * D * 2, wave, lane, L, hook);
            TL(1);
            if (!got) { waith(0); ldh(lane * 16); }
            if (wave < 2) cnt[tid] = 0;
            pol = get_pol(rs, lane, dead, tag, pv0);
            __builtin_amdgcn_s_waitcnt(0x0F70);
            bar(); };
        if (!st2) {
            const unsigned vdst = WS_KQ + h * CH + (wave & 3) * 2048 + lane * 16;
            const bool ld = vo || wave >= 4;
            const unsigned vpr = wave != 1 ? OOB : vo ? WS_POUT + (lane < 40 ? lane : 0) * FS : WS_PP + h * FS;
            start(Ic<1>());
            if (!ld) __builtin_amdgcn_s_setprio(1);
            for (int s = 0; s < NSEG; ++s) {
                if (s == 1) { ST_RESET; }
                lptr b = img(s) + lane * 16;
                u32x4 f[FV];
                #pragma unroll
                for (int m = 0; m < FV; ++m) f[m] = lrd(b + m * 1024);
                SB();
                if (ld && (s > 0 || !vo)) { if (s + 1 < NSEG) waith(s + 1);
                    const unsigned vh = s + 1 < NSEG ? lane * 16 : OOB;
                    #pragma unroll
                    for (int i = 0; i < 10; ++i) dmah(s + 1, 10 * (wave & 3) + i, vh);
                    if (!vo) { asm volatile("s_waitcnt vmcnt(10)" ::: "memory");
                        unsigned old = lds_inc(cnt + lane); asm volatile("s_waitcnt lgkmcnt(0)" : "+v"(old));
                        st32(0u, rs, ((old & 3u) | lane * 4u) == 3u ? 0 : OOB, WS_PQK + h * FS + (s - 1) * FSEG);
                        if (s == 1) TL(2); } }
                const unsigned pr = ld_fl(rs, s < RC - 1 ? OOB : vpr - (RC - 1) * FSEG, s);
                SB();
                ST(0);
                f32x16 acc;
                #pragma unroll
                for (int i = 0; i < 16; ++i) acc[i] = 0;
                #pragma unroll
                for (int ks = 0; ks < 40; ++ks) { acc = mf32(W[ks], f[ks % FV], acc); if (ks + FV < 40) f[ks % FV] = lrd(b + (ks + FV) * 1024); SB(); }
                const bool rok = fl_ok(pr);
                const u32x4 o0 = pk8(acc, 0), o1 = pk8(acc, 1);
                if (vo) { lptr vw = L + 40960 + (s & 1) * 8192 + wave * 2048 + lane * 16; lwr(vw, o0); lwr(vw + 1024, o1); }
                else { const unsigned so = RO(s, wave < 4); st128(o0, rs, vdst, so); st128(o1, rs, vdst + 1024, so); }
                if (!rok) wait_ring(rs, vpr, s - (RC - 1), seen, dead);
                ST(1);
                if (wave < 4 || s == NSEG - 1) drain(); else asm volatile("s_waitcnt vmcnt(2)" ::: "memory");
                ST(3);
                bar();
                ST(4);
            }
            if (!vo && tid == 0) st32(0u, rs, 0, WS_PQK + h * FS + (NSEG - 1) * FSEG);
        } else {
            const int r32 = lane & 31, hf = lane >> 5;
            const int prow = (r32 & 19) | ((r32 & 4) << 1) | ((r32 & 8) >> 1);
            lptr asrc = L + 40960 + hf * 512 + prow * 16;
            const unsigned vdst = WS_CF + ((10 * (wave & 1) + (r32 >> 4)) * 4 + (h >> 3)) * CH + (h & 7) * 1024 + (hf * 16 + (r32 & 15)) * 16;
            start(Ic<2>());
            __builtin_amdgcn_s_setprio(1);
            waith(1);
            #pragma unroll
            for (int i = 0; i < 10; ++i) dmah(1, 10 * (wave - 4) + i, lane * 16);
            drain();
            for (int s = 0; s < NSEG; ++s) {
                ST(3);
                bar();
                if (s == 1) { ST_RESET; }
                ST(4);
                u32x4 af[8];
                #pragma unroll
                for (int ks = 0; ks < 8; ++ks) af[ks] = lrd(asrc + (s & 1) * 8192 + ks * 1024);
                SB();
                const unsigned so = CO(s, wave >= 6);
                f32x16 acc[2];
                auto fin = [&](int tt, int p) { const f32x16& c = acc[tt & 1];
                    st128((u32x4){pk2(c[8 * p], c[8 * p + 1]), pk2(c[8 * p + 2], c[8 * p + 3]), pk2(c[8 * p + 4], c[8 * p + 5]), pk2(c[8 * p + 6], c[8 * p + 7])}, rs, vdst + p * 512, so + tt * (8 * CH)); };
                #pragma unroll
                for (int tt = 0; tt < 5; ++tt) {
                    #pragma unroll
                    for (int i = 0; i < 16; ++i) acc[tt & 1][i] = 0;
                    #pragma unroll
                    for (int ks = 0; ks < 8; ++ks) { acc[tt & 1] = mf32(af[ks], W[8 * tt + ks], acc[tt & 1]);
                        asm volatile("" : "+v"(acc[tt & 1]));
                        if (tt > 0 && ks == 2) fin(tt - 1, 0);
                        if (tt > 0 && ks == 4) fin(tt - 1, 1);
                        SB(); }
                }
                fin(4, 0); fin(4, 1);
                ST(1);
                drain();
                unsigned old = lds_inc(cnt + lane); asm volatile("s_waitcnt lgkmcnt(0)" : "+v"(old));
                st32(0u, rs, ((old & 3u) | lane * 4u) == 3u ? 0 : OOB, WS_PVO + h * FS + s * FSEG);
                if (s == 0) TL(2);
            }
        }
    } else if (role == R_OUT) {
        const int c = id;
        const unsigned lo = (lane >> 5) * 1024 + (lane & 31) * 16;
        const unsigned vcf = WS_CF + ((c % 20) * 4 + (wave >> 1)) * CH + (wave & 1) * 4096 + lo;
        const unsigned vax = WS_AX + wave * 8192 + (lane >> 5) * 2048 + (lane & 31) * 16, vam = WS_AM + wave * 2048 + lane * 16;
        const unsigned vu = WS_SLOT + SL_U + c * CH + wave * 1024 + lane * 16;
        lptr part = L + wave * 2560 + (lane & 15) * 80 + (lane >> 4) * 16;
        const int tok = 4 * wave + (lane >> 4), col = lane & 15;
        lptr rsrc = L + tok * 80 + col * 4;
        LAS unsigned* cnt = (LAS unsigned*)(L + 49152);
        const unsigned vx = tok * (D * 2) + c * 32 + col * 2, vo = WS_X1 + c * 1024 + wave * 128 + (((col >> 3) ^ (wave >> 1)) & 1) * 64 + (lane >> 4) * 16 + (col & 7) * 2;
        const unsigned vl = col ? OOB : WS_LS + (tok >> 4) * 5120 + c * 128 + (tok & 15) * 8;
        const unsigned vpp = WS_PP + (4 * wave + (lane & 3)) * FS, vpv = WS_PVO + (4 * wave + (lane & 3)) * FS;
        const rsrc_t rx = mkrs(a.x, S * D * 2);
        const float ga = b2f(*(LAS u16*)(L + L_MOD + 2560 + c * 32 + col * 2));
        int seenv = -1, seenp = -1;
        u32x4 cf[4];
        auto ldcf = [&](int s) {
            wait_fl(rs, vpv, s, seenv, dead, 4);
            const unsigned so = CO(s, c >= 20);
            #pragma unroll
            for (int i = 0; i < 4; ++i) cf[i] = ld_sc1(rs, vcf + (i >> 1) * 2048 + (i & 1) * 512, so); };
        auto done = [&](int s) { if (lane == 0 && (__hip_atomic_fetch_add(cnt, 1u, __ATOMIC_RELAXED, __HIP_MEMORY_SCOPE_WORKGROUP) & 7u) == 7u) st32(0u, rs, WS_POUT + id * FS, s * FSEG); };
        if (tid == 0) *cnt = 0;
        ldcf(0);
        for (int s = 0; s < NSEG; ++s) {
            if (s == 2) { ST_RESET; }
            bool pub = s > 0;
            if (seenp < s) {
                if (pub) { drain(); done(s - 1); pub = false; }
                wait_fl(rs, vpp, s, seenp, dead, s < 63 ? a.tp[17] : 2); }
            ST(0);
            DG(rt32());
            u32x4 ax[6], am0, am1, u;
            #pragma unroll
            for (int j = 0; j < 2; ++j) { ax[3 * j] = ld_sc1(rs, vax, RI(s) * 65536 + j * 4096); ax[3 * j + 1] = ld_sc1(rs, vax + 1024, RI(s) * 65536 + j * 4096); ax[3 * j + 2] = ld_sc1(rs, vax + 1536, RI(s) * 65536 + j * 4096); }
            u = ld_sc1(rs, vu, SO(s));
            am0 = ld_sc1(rs, vam, RI(s) * 16384); am1 = ld_sc1(rs, vam + 1024, RI(s) * 16384);
            SB();
            const unsigned pw = ld_fl(rs, s + 2 < NSEG ? vpv : OOB, s + 2);
            const unsigned xr = __builtin_amdgcn_raw_buffer_load_b16(rx, vx, s * (32 * D * 2), 0);
            SB();
            if (pub) { asm volatile("s_waitcnt vmcnt(11)" ::: "memory"); done(s - 1); }
            ST(1);
            f32x4 d0 = {0, 0, 0, 0}, d1 = d0;
            #pragma unroll
            for (int j = 0; j < 2; ++j) { d0 = mf16(cf[2 * j], ax[3 * j], d0); d1 = mf16(cf[2 * j], ax[3 * j + 1], d1); d1 = mf16(cf[2 * j + 1], ax[3 * j + 2], d1); }
            if (__builtin_expect(!all_ok(mx4(u, 0)), 0))
                for (unsigned sp = 0;; ++sp) { asm volatile("" ::: "memory");
                    u = ld_sc1(rs, vu, SO(s));
                    if (all_ok(mx4(u, 0)) || dead) break;
                    if (sp > SPIN_MAX) { dead = true; break; }
                    dly(2); }
            d0 = mf16(u, am0, d0); d1 = mf16(u, am1, d1);
            lptr pp = part + (s & 1) * 20480;
            *(LAS f32x4*)pp = d0; *(LAS f32x4*)(pp + 1280) = d1;
            const unsigned pv = ld_fl(rs, s + 1 < NSEG ? vpp : OOB, s + 1);
            if (fl_ok(pw)) seenv = s + 2;
            if (s + 1 < NSEG) ldcf(s + 1);
            ST(3);
            bar();
            ST(4);
            float q[8];
            #pragma unroll
            for (int w = 0; w < 8; ++w) q[w] = *(LAS float*)(rsrc + (s & 1) * 20480 + w * 2560);
            const unsigned o = pk2(blo(xr) + rnd(ga * rnd(((q[0] + q[1]) + (q[2] + q[3])) + ((q[4] + q[5]) + (q[6] + q[7])))), 0.f);
            __builtin_amdgcn_raw_buffer_store_b16((unsigned short)o, rs, vo, RI(s) * (32 * D * 2), 16);
            const float of = blo(o);
            st64((u32x2){__float_as_uint(sum16(of)), __float_as_uint(sum16(of * of))}, rs, vl, RI(s) * 10240);
            if (fl_ok(pv)) seenp = s + 1;
            LS2(dg_, rt32());
            ST(5);
            if (s == 0) TL(2);
        }
        drain();
        done(NSEG - 1);
    } else if (role == R_F1) {
        u32x4 W[4][10];
        f32x4 cs[2], bs[2];
        const int kg = lane >> 4, t16 = lane & 15, kh = (wave >> 1) & 1, cw = 2 * (wave & 1) + kh, th = wave >> 2;
        gate(tstart + a.tp[23]);
        #pragma unroll
        for (int mat = 0; mat < 2; ++mat) {
            const rsrc_t rg = mkrs(mat ? a.wu : a.wg, D * F * 2);
            const unsigned vw = (wave * 80 + ((lane >> 1) & 7)) * (F * 2) + id * 128 + (lane >> 4) * 32 + (lane & 1) * 16;
            bar();
            #pragma unroll 1
            for (int i = 0; i < 10; ++i) __builtin_amdgcn_raw_ptr_buffer_load_lds(rg, (LAS void*)(L + (wave * 10 + i) * 1024), 16, vw, i * 8 * F * 2, 0, 0);
            drain(); if (mat) TL(6); else TL(4); bar();
            lptr rp = L + kg * 1024 + cw * 256 + t16 * 8;
            const u32x4 ones = {0x3F803F80u, 0x3F803F80u, 0x3F803F80u, 0x3F803F80u};
            cs[mat] = (f32x4){0, 0, 0, 0}; bs[mat] = cs[mat];
            #pragma unroll 4
            for (int ks = 0; ks < 20; ++ks) { const u32x2 a0 = trd(rp + ks * 4096), a1 = trd(rp + ks * 4096 + 128);
                bs[mat] = mf16((u32x4){a0.x, a0.y, a1.x, a1.y}, lrd(L + L_MOD + 3840 + (32 * ks + 8 * kg) * 2), bs[mat]); }
            bar();
            #pragma unroll 2
            for (int i = 0; i < 10; ++i) { lptr q = L + (wave * 10 + i) * 1024 + lane * 16;
                u32x4 w = lrd(q);
                const float sc = rnd(1.f + b2f(*(LAS u16*)(L + L_MOD + 5120 + (wave * 80 + i * 8 + ((lane >> 1) & 7)) * 2)));
                #pragma unroll
                for (int j = 0; j < 4; ++j) w[j] = pk2(blo(w[j]) * sc, bhi(w[j]) * sc);
                lwr(q, w); }
            bar();
            #pragma unroll 4
            for (int ks = 0; ks < 20; ++ks) { const u32x2 a0 = trd(rp + ks * 4096), a1 = trd(rp + ks * 4096 + 128);
                cs[mat] = mf16((u32x4){a0.x, a0.y, a1.x, a1.y}, ones, cs[mat]); }
            #pragma unroll
            for (int j = 0; j < 2; ++j) { lptr r2 = L + kh * 40960 + kg * 1024 + (cw ^ j) * 256 + t16 * 8;
                #pragma unroll
                for (int i = 0; i < 10; ++i) { const u32x2 a0 = trd(r2 + i * 4096), a1 = trd(r2 + i * 4096 + 128); W[2 * j + mat][i] = (u32x4){a0.x, a0.y, a1.x, a1.y}; } }
            if (!mat) TL(5); }
        TL(2);
        lptr bb = L + kh * 20480 + (kg >> 1) * 1024 + (th * 4 + (t16 >> 2)) * 128 + ((kg ^ (t16 >> 3)) & 1) * 64 + (t16 & 3) * 16;
        lptr stat = L + 81920 + wave * 1280 + lane * 8;
        lptr xw = L + 102400 + wave * 2048 + lane * 16, xr = L + 102400 + (wave ^ 2) * 2048 + lane * 16;
        lptr sw = L + 118784 + wave * 128 + t16 * 8, sr = L + 118784 + th * 512 + t16 * 8;
        volatile LAS int* rdy = (volatile LAS int*)(L + 119808);
        LAS unsigned* cnt = (LAS unsigned*)(L + 119812);
        const int ab = th * 54 + 2 * id + (cw >> 1);
        const unsigned vact = WS_ACT + (ab >> 3) * CH + (ab & 7) * 1024 + ((2 * (cw & 1) + (kg >> 1)) * 16 + t16) * 16 + (kg & 1) * 8;
        int seen = -1;
        const unsigned vpo = wave ? OOB : WS_POUT + (lane < 40 ? lane : 0) * FS;
        const unsigned vx1 = WS_X1 + wave * 5120 + lane * 16, vls = WS_LS + tid * 16;
        auto ldp = [&](int s, int i) {
            if (i < 5) dma1(rs, L + (s & 1) * 40960 + wave * 5120 + i * 1024, vx1, RI(s) * (32 * D * 2) + i * 1024);
            else if (i == 5) dma1(rs, L + 81920 + (s & 1) * 10240 + wave * 1024, vls, RI(s) * 10240);
            else if (wave < 2) dma1(rs, L + 81920 + (s & 1) * 10240 + 8192 + wave * 1024, vls, RI(s) * 10240 + 8192); };
        auto ldh = [&](int s) {
            #pragma unroll
            for (int i = 0; i < 7; ++i) ldp(s, i); };
        auto ready = [&](int s) { return __builtin_amdgcn_readfirstlane(*rdy) >= s; };
        wait_fl(rs, vpo, 0, seen, dead, 24);
        if (tid == 0) { *rdy = 0; *cnt = 0; }
        bar();
        ldh(0);
        drain();
        bool pend = false;
        auto done = [&](int s) { if (lane == 0) { unsigned old; asm volatile("ds_add_rtn_u32 %0, %1, %2\n\ts_waitcnt lgkmcnt(0)" : "=v"(old) : "v"(cnt), "v"(1u) : "memory");
            st32(0u, rs, (old & 7u) == 7u ? WS_PF1 + id * FS : OOB, s * FSEG); } };
        for (int s = 0; s < NSEG; ++s) {
            if (s == 2) { ST_RESET; }
            unsigned pv, pw;
            if (wave == 0) asm volatile("buffer_load_dword %0, %2, %4, %5 offen sc1\n\tbuffer_load_dword %1, %3, %4, %6 offen sc1" : "=&v"(pv), "=&v"(pw)
                : "v"((s + 1 < NSEG && seen <= s) ? vpo : OOB), "v"((s + 2 < NSEG && pend) ? vpo : OOB), "s"(rs), "s"((s + 1) * FSEG), "s"((s + 2) * FSEG) : "memory");
            ST(0);
            bar();
            ST(1);
            DG(rt32());
            const int bo = (s & 1) * 40960;
            f32x2 t2, t3, t4;
            u32x4 f[4];
            const int r0 = *rdy;
            #pragma unroll
            for (int m = 0; m < 4; ++m) f[m] = lrd(bb + bo + m * 2048);
            t2 = *(LAS f32x2*)(stat + (s & 1) * 10240); t3 = *(LAS f32x2*)(stat + (s & 1) * 10240 + 512); t4 = *(LAS f32x2*)(stat + (s & 1) * 10240 + 1024);
            SB();
            const bool early = __builtin_amdgcn_readfirstlane(r0) > s && s + 1 < NSEG;
            f32x4 d[4];
            #pragma unroll
            for (int j = 0; j < 4; ++j) d[j] = (f32x4){0, 0, 0, 0};
            #pragma unroll
            for (int i = 0; i < 10; ++i) {
                #pragma unroll
                for (int j = 3; j >= 0; --j) d[j] = mf16(W[j][i], f[i & 3], d[j]);
                asm volatile("" : "+v"(d[0]), "+v"(d[1]), "+v"(d[2]), "+v"(d[3]));
                if (i + 4 < 10) f[i & 3] = lrd(bb + bo + (i + 4) * 2048);
                if (i == 5 && pend) { if (early) asm volatile("s_waitcnt vmcnt(5)" ::: "memory"); else drain(); done(s - 1); }
                if (i < 7 && early) ldp(s + 1, i);
                if (i == 4) { t2 += t3; if (lane < 32) t2 += t4; }
                if (i == 5) { t2[0] = x16sum(t2[0]); t2[1] = x16sum(t2[1]); }
                if (i == 6) { t2[0] = x32sum(t2[0]); t2[1] = x32sum(t2[1]); *(LAS f32x2*)sw = t2; }
                SB(); }
            *(LAS f32x4*)(xw + 1024) = d[3]; *(LAS f32x4*)xw = d[2];
            if (wave == 0) {
                unsigned long long mv, mw;
                if (!pend) drain();
                asm volatile("v_cmp_ne_u32 %0, 0, %2\n\tv_cmp_ne_u32 %1, 0, %3" : "=&s"(mv), "=&s"(mw) : "v"(pv), "v"(pw) : "memory");
                if (pend && s + 2 < NSEG && mw == 0ull) seen = s + 2; else if (mv == 0ull) seen = max(seen, s + 1);
                *rdy = seen; }
            bar();
            const int rv = *rdy;
            t2 = *(LAS f32x2*)sr; t3 = *(LAS f32x2*)(sr + 128); t4 = *(LAS f32x2*)(sr + 256);
            const f32x2 t5 = *(LAS f32x2*)(sr + 384);
            d[2] = *(LAS f32x4*)xr; d[3] = *(LAS f32x4*)(xr + 1024);
            SB();
            bool got = s + 1 >= NSEG || early;
            if (!got && __builtin_amdgcn_readfirstlane(rv) > s) { ldh(s + 1); got = true; }
            ST(2);
            t2 = ((t2 + t3) + (t4 + t5)) * (1.f / D);
            const float rstd = __builtin_amdgcn_rcpf(rnd(__builtin_amdgcn_sqrtf(rnd(rnd(__builtin_fmaxf(t2[1] - t2[0] * t2[0], 0.f)) + 1e-5f)))), mean = rnd(t2[0]);
            const f32x4 g = (d[0] + d[2] - cs[0] * mean) * rstd + bs[0], u = (d[1] + d[3] - cs[1] * mean) * rstd + bs[1];
            float act[4];
            #pragma unroll
            for (int i = 0; i < 4; ++i) act[i] = g[i] * u[i] * __builtin_amdgcn_rcpf(1.f + __expf(-g[i]));
            LS2(dg_, (rt32() & ~1u) | (got ? 1u : 0u));
            st64((u32x2){pk2(act[0], act[1]), pk2(act[2], act[3])}, rs, vact, FO(s));
            ST(4);
            pend = got && s + 1 < NSEG;
            if (pend) asm volatile("s_waitcnt vmcnt(1)" ::: "memory");
            else {
                const bool ok = fl_ok(ld_fl(rs, got ? OOB : vpo, s + 1));
                drain();
                if (wave == 0 && ok) { seen = s + 1; *rdy = s + 1; }
                done(s);
                if (!got) {
                    if (wave == 0) { wait_fl(rs, vpo, s + 1, seen, dead, s < 61 ? a.tp[16] : 2); *rdy = s + 1; }
                    while (!ready(s + 1)) dly(1);
                    ldh(s + 1); drain(); } }
            ST(5);
            if (s == 0) TL(3);
        }
    } else if (role == R_F2) {
        const int cg = id >> 1, mt = id & 1;
        const int ks0 = wave < 6 ? 7 * wave : 42 + 6 * (wave - 6);
        u32x4 W[28];
        gate(tstart + a.tp[23]);
        {   const rsrc_t rw = mkrs(a.wd, F * D * 2);
            const unsigned vw = (32 * ks0 + 8 * (lane >> 4) + ((lane >> 2) & 3)) * (D * 2) + cg * 128 + (lane & 3) * 16;
            u32x4 v[28];
            #pragma unroll
            for (int i = 0; i < 28; ++i) v[i] = ld_pl(rw, vw, (i >= 24 && wave >= 6) ? 0 : (32 * (i >> 2) + 4 * (i & 1)) * (D * 2) + ((i >> 1) & 1) * 64);
            __builtin_amdgcn_sched_barrier(0);
            lptr wp = stg + lane * 16, rp = stg + (lane >> 2) * 64 + (lane & 3) * 8;
            #pragma unroll
            for (int i = 0; i < 28; ++i) { const int pi = i & 3;
                if (i >= 24 && wave >= 6) v[i] = (u32x4){0, 0, 0, 0};
                lwr(wp + pi * 1024, v[i]);
                const u32x2 r0 = trd(rp + pi * 1024), r1 = trd(rp + pi * 1024 + 32);
                const int i0 = (i & ~3) + (pi & 2);
                W[i0][2 * (pi & 1)] = r0.x; W[i0][2 * (pi & 1) + 1] = r0.y; W[i0 + 1][2 * (pi & 1)] = r1.x; W[i0 + 1][2 * (pi & 1) + 1] = r1.y; } }
        TL(2);
        bar();
        const int ab = mt * 54 + ks0;
        const unsigned vact = WS_ACT + lane * 16;
        const int pf = ((ks0 >> 1) + (lane & 3)) < 27 ? (ks0 >> 1) + (lane & 3) : 26;
        lptr part = L + wave * 4352 + (lane & 15) * 272 + (lane >> 4) * 16;
        lptr rsrc = L + (tid >> 5) * 272 + (tid & 31) * 8;
        const unsigned vx = (mt * 16 + (tid >> 5)) * (D * 2) + cg * 128 + (tid & 31) * 4;
        const unsigned vx1 = WS_X1 + (cg * 4 + ((tid & 31) >> 3)) * 1024 + (mt * 4 + (tid >> 7)) * 128 + (((tid >> 2) ^ (tid >> 8)) & 1) * 64 + ((tid >> 5) & 3) * 16 + (tid & 3) * 4;
        const unsigned gb = *(LAS unsigned*)(L + L_MOD + 6400 + cg * 128 + (tid & 31) * 4);
        const rsrc_t ro = mkrs(a.out, S * D * 2);
        int seen = -1;
        for (int s = 0; s < NSEG; ++s) {
            wait_fl(rs, WS_PF1 + pf * FS, s, seen, dead, s < 60 ? a.tp[18] : 2);
            DG(rt32());
            u32x4 af[7];
            #pragma unroll
            for (int i = 0; i < 7; ++i) { const int b = ab + ((i == 6 && wave >= 6) ? 0 : i); af[i] = ld_sc1(rs, vact, FO(s) + (b >> 3) * CH + (b & 7) * 1024); }
            const unsigned xr = __builtin_amdgcn_raw_buffer_load_b32(rs, vx1, RI(s) * (32 * D * 2), 16);
            f32x4 d[4];
            #pragma unroll
            for (int j = 0; j < 4; ++j) d[j] = (f32x4){0, 0, 0, 0};
            #pragma unroll
            for (int i = 0; i < 7; ++i)
                #pragma unroll
                for (int j = 0; j < 4; ++j) d[j] = mf16(W[4 * i + j], af[i], d[j]);
            #pragma unroll
            for (int j = 0; j < 4; ++j) *(LAS f32x4*)(part + (s & 1) * 34816 + j * 64) = d[j];
            const unsigned pv = ld_fl(rs, s + 1 < NSEG ? WS_PF1 + pf * FS : OOB, s + 1);
            bar();
            if (tid == 0 && s > 0) st32(0u, rs, WS_PF2 + id * FS, (s - 1) * FSEG);
            f32x2 r = {0, 0};
            #pragma unroll
            for (int w = 0; w < 8; ++w) r += *(LAS f32x2*)(rsrc + (s & 1) * 34816 + w * 4352);
            __builtin_amdgcn_raw_buffer_store_b32(pk2(blo(xr) + rnd(blo(gb) * rnd(r[0])), bhi(xr) + rnd(bhi(gb) * rnd(r[1]))), ro, vx, s * (32 * D * 2), 0);
            if (fl_ok(pv)) seen = s + 1;
            LS2(dg_, rt32());
            if (s == 0) TL(3);
        }
        if (id == 0 && tid == 0) st64(tag, rs, WS_EP, 0);
    }
    TL(7);
    ST_OUT;
}

#define WOFF 0
extern "C" void kernel_launch(void* const* d_in, const int* in_sizes, int n_in, void* d_out, int out_size, void* d_ws, size_t ws_size, hipStream_t stream) {
    unsigned char* wsb = (unsigned char*)(((size_t)d_ws + WOFF + 0x1FFFFF) & ~(size_t)0x1FFFFF);
    Args a{(const u16*)d_in[0], (const u16*)d_in[1], (const u16*)d_in[2], (const u16*)d_in[3], (const u16*)d_in[4], (const u16*)d_in[5],
           (const u16*)d_in[8], (const u16*)d_in[9], (const u16*)d_in[10], (const u16*)d_in[11], (const u16*)d_in[12], (u16*)d_out, wsb, {184, 0, 112, 0, 0, -4, 56, 3, 1, 8, 0, 6, 96, 3, 0, 0, 8, 4, 4, 0, 63, 0, 0, 1400}};
    hipFuncSetAttribute((const void*)mega, hipFuncAttributeMaxDynamicSharedMemorySize, L_TOT);
    mega<<<256, 512, L_TOT, stream>>>(a);
}
```

```cpp
#include <hip/hip_runtime.h>
#include <hip/hip_bf16.h>

typedef unsigned short u16;
typedef __bf16 bf16x8 __attribute__((ext_vector_type(8)));
typedef __bf16 bf16x16 __attribute__((ext_vector_type(16)));
typedef __bf16 bf16x2 __attribute__((ext_vector_type(2)));
typedef short s16x4 __attribute__((ext_vector_type(4)));
typedef float f32x2 __attribute__((ext_vector_type(2)));
typedef float f32x4 __attribute__((ext_vector_type(4)));
typedef float f32x16 __attribute__((ext_vector_type(16)));
typedef unsigned u32x2 __attribute__((ext_vector_type(2)));
typedef unsigned u32x4 __attribute__((ext_vector_type(4)));
typedef unsigned u32x8 __attribute__((ext_vector_type(8)));
typedef __amdgpu_buffer_rsrc_t rsrc_t;
#define LAS __attribute__((address_space(3)))
typedef LAS unsigned char* lptr;

constexpr int S = 2048, D = 640, H = 32, E = 128, HE = 4096, NSEG = 64, F = 1728;
constexpr unsigned WS_EP = 0;
constexpr unsigned WS_FH1 = 1024;
#define FS 32
constexpr unsigned WS_FLG = 262144, FSEG = 192 * FS;
constexpr unsigned WS_PVO = WS_FLG, WS_PQK = WS_FLG + 32 * FS, WS_PP = WS_FLG + 64 * FS, WS_POUT = WS_FLG + 96 * FS, WS_PF2 = WS_FLG + 136 * FS, WS_PF1 = WS_FLG + 160 * FS;
#define RC 16
#define RS 16
constexpr int NPG = 2 * RC > RS ? 2 * RC : RS;
constexpr unsigned WS_POL = 2048;
constexpr unsigned WS_MOD = 4096;
constexpr unsigned WS_SLOT = 2u << 20, SLOT = 2u << 20, CH = 16384;
constexpr unsigned SL_PX = 0, SL_PM = 2 * CH, SL_STP = 2 * CH + 4096, SL_MEM = 3 * CH, SL_U = 5 * CH, SL_END = 45 * CH, SL_CAL = 126 * CH;
#define CFN 0
constexpr unsigned WS_CF = WS_SLOT + (CFN ? 45 * CH : 0), WS_KQ = WS_SLOT + 80 * CH, WS_ACT = WS_SLOT + 112 * CH;
constexpr unsigned WS_H1  = WS_SLOT + NPG * SLOT;
constexpr unsigned WS_AX  = WS_H1 + S * D * 2;
constexpr unsigned WS_AM  = WS_AX + RS * 32 * 2048;
constexpr unsigned WS_X1  = WS_AM + RS * 8 * 2048;
constexpr unsigned WS_LS  = WS_X1 + RS * 32 * D * 2;
constexpr unsigned WS_DBG = WS_LS + RS * 10240;
constexpr unsigned WS_END = WS_DBG + (2 << 20);

__device__ __forceinline__ float b2f(u16 b) { return __uint_as_float((unsigned)b << 16); }
__device__ __forceinline__ float blo(unsigned x) { return __uint_as_float(x << 16); }
__device__ __forceinline__ float bhi(unsigned x) { return __uint_as_float(x & 0xffff0000u); }
__device__ __forceinline__ unsigned pk2(float lo, float hi) { bf16x2 v; v[0] = (__bf16)lo; v[1] = (__bf16)hi; return __builtin_bit_cast(unsigned, v); }
__device__ __forceinline__ float rnd(float f) { return blo(pk2(f, 0.f)); }
__device__ __forceinline__ unsigned nosent(unsigned x) { return x < 0xFFFFFFFEu ? x : 0xFFFFFFFEu; }

struct Args { const u16 *x, *mem0, *Wq, *Wk, *Wv, *Wo, *wg, *wu, *wd, *c, *wm; u16* out; unsigned char* ws; int tp[24]; };
__device__ __forceinline__ void dly(int n) { for (int i = 0; i < n; ++i) __builtin_amdgcn_s_sleep(1); }
__device__ __forceinline__ unsigned long long rtime() { unsigned long long t; asm volatile("s_memrealtime %0\n\ts_waitcnt lgkmcnt(0)" : "=s"(t) :: "memory"); return t; }
constexpr unsigned SPIN_MAX = 200000u, OOB = 0x7FFFF000u;
#define TL(i)
#define ST(i)
#define ST_INIT
#define ST_RESET
#define ST_OUT
#define ST_T(t)
#define LG(k, val)
__device__ __forceinline__ rsrc_t mkrs(const void* p, unsigned bytes) { return __builtin_amdgcn_make_buffer_rsrc((void*)p, 0, (int)bytes, 0x00020000); }
__device__ __forceinline__ u32x4 ld_sc1(rsrc_t r, unsigned voff, unsigned soff) { return __builtin_amdgcn_raw_buffer_load_b128(r, voff, soff, 16); }
__device__ __forceinline__ u32x4 ld_pl(rsrc_t r, unsigned voff, unsigned soff) { return __builtin_amdgcn_raw_buffer_load_b128(r, voff, soff, 0); }
__device__ __forceinline__ void st128(u32x4 v, rsrc_t r, unsigned voff, unsigned soff) { __builtin_amdgcn_raw_buffer_store_b128(v, r, voff, soff, 16); asm volatile("s_nop 1" :: "v"(v)); }
__device__ __forceinline__ void st64(u32x2 v, rsrc_t r, unsigned voff, unsigned soff) { __builtin_amdgcn_raw_buffer_store_b64(v, r, voff, soff, 16); }
__device__ __forceinline__ void st32(unsigned v, rsrc_t r, unsigned voff, unsigned soff) { __builtin_amdgcn_raw_buffer_store_b32(v, r, voff, soff, 16); }
__device__ __forceinline__ unsigned umax3(unsigned a, unsigned b, unsigned c) { unsigned m = a > b ? a : b; return m > c ? m : c; }
__device__ __forceinline__ unsigned mx4(u32x4 v, unsigned m) { return umax3(umax3(v.x, v.y, m), v.z, v.w); }
__device__ __forceinline__ bool all_ok(unsigned m) { return __builtin_amdgcn_ballot_w64(m == 0xFFFFFFFFu) == 0ull; }
__device__ __forceinline__ f32x4 smf(u32x4 a, u32x8 b, f32x4 c, int idx) { return __builtin_amdgcn_smfmac_f32_16x16x64_bf16(__builtin_bit_cast(bf16x8, a), __builtin_bit_cast(bf16x16, b), c, idx, 0, 0); }
__device__ __forceinline__ f32x4 mf16(u32x4 a, u32x4 b, f32x4 c) { return __builtin_amdgcn_mfma_f32_16x16x32_bf16(__builtin_bit_cast(bf16x8, a), __builtin_bit_cast(bf16x8, b), c, 0, 0, 0); }
__device__ __forceinline__ f32x16 mf32(u32x4 a, u32x4 b, f32x16 c) { return __builtin_amdgcn_mfma_f32_32x32x16_bf16(__builtin_bit_cast(bf16x8, a), __builtin_bit_cast(bf16x8, b), c, 0, 0, 0); }
__device__ __forceinline__ float fmx(float a, float b) { return __builtin_elementwise_maximum(a, b); }
__device__ __forceinline__ float fold32(float a, float b) { auto r = __builtin_amdgcn_permlane32_swap(__float_as_uint(a), __float_as_uint(b), false, false); return __uint_as_float(r[0]) + __uint_as_float(r[1]); }
__device__ __forceinline__ float x32max(float a) { auto r = __builtin_amdgcn_permlane32_swap(__float_as_uint(a), __float_as_uint(a), false, false); return fmx(__uint_as_float(r[0]), __uint_as_float(r[1])); }
__device__ __forceinline__ float x32sum(float a) { return fold32(a, a); }
__device__ __forceinline__ float x16max(float a) { auto r = __builtin_amdgcn_permlane16_swap(__float_as_uint(a), __float_as_uint(a), false, false); return fmx(__uint_as_float(r[0]), __uint_as_float(r[1])); }
__device__ __forceinline__ float x16sum(float a) { auto r = __builtin_amdgcn_permlane16_swap(__float_as_uint(a), __float_as_uint(a), false, false); return __uint_as_float(r[0]) + __uint_as_float(r[1]); }
template <int N> __device__ __forceinline__ float ror(float a) { return __int_as_float(__builtin_amdgcn_update_dpp(0, __float_as_int(a), 0x120 + N, 0xf, 0xf, true)); }
template <int C> __device__ __forceinline__ float qp(float a) { return __int_as_float(__builtin_amdgcn_update_dpp(0, __float_as_int(a), C, 0xf, 0xf, true)); }
__device__ __forceinline__ float sum16(float a) { a += ror<8>(a); a += ror<4>(a); a += ror<2>(a); a += ror<1>(a); return a; }
__device__ __forceinline__ void bar() { asm volatile("s_waitcnt lgkmcnt(0)\n\ts_barrier" ::: "memory"); __builtin_amdgcn_s_waitcnt(0xC07F); }
__device__ __forceinline__ void drain() { asm volatile("s_waitcnt vmcnt(0)" ::: "memory"); }
__device__ __forceinline__ u32x2 trd(lptr p) { return __builtin_bit_cast(u32x2, __builtin_amdgcn_ds_read_tr16_b64_v4i16((LAS s16x4*)p)); }
__device__ __forceinline__ u32x4 lrd(lptr p) { return *(LAS u32x4*)p; }
__device__ __forceinline__ void lwr(lptr p, u32x4 v) { *(LAS u32x4*)p = v; }
__device__ __forceinline__ float dot2(unsigned a, unsigned b, float c) { return __builtin_amdgcn_fdot2_f32_bf16(__builtin_bit_cast(bf16x2, a), __builtin_bit_cast(bf16x2, b), c, false); }

__device__ __forceinline__ unsigned ld_fl(rsrc_t rs, unsigned voff, int seg) { asm volatile("" ::: "memory"); return __builtin_amdgcn_raw_buffer_load_b32(rs, voff, seg * FSEG, 16); }
__device__ __forceinline__ bool fl_ok(unsigned v) { const bool r = __builtin_amdgcn_ballot_w64(v != 0u) == 0ull; asm volatile("" ::: "memory"); return r; }
__device__ __forceinline__ void wait_fl(rsrc_t rs, unsigned voff, int seg, int& seen, bool& dead, int slp) {
    if (seen >= seg) return;
    for (unsigned sp = 0;; ++sp) {
        if (fl_ok(ld_fl(rs, voff, seg)) || dead) break;
        if (sp > SPIN_MAX) { dead = true; break; }
        dly(slp);
    }
    seen = seg;
}
__device__ __forceinline__ void wait_ring(rsrc_t rs, unsigned voff, int seg, int& seen, bool& dead) {
    if (seen >= seg) return;
    wait_fl(rs, voff, seg, seen, dead, 8);
}

constexpr int L_A = 0;
constexpr int L_QK = 10240;
constexpr int L_KQX = 14336;
constexpr int L_STG = 49152;
constexpr int L_MOD = 98304;
constexpr int L_TOT = 123392;

#define FHR 4
#define WD 3
#define WAUX 2
template <int KIND> __device__ __forceinline__ unsigned wsoff(int r, int j) {
    const int i = 5 * r + j;
    return KIND == 0 ? (64 * (i >> 2) + 32 * ((i & 3) >> 1) + 2 * (i & 1)) * (HE * 2) : KIND == 1 ? (64 * (r >> 2) + 32 * ((r & 3) >> 1) + (r & 1)) * (D * 2) + j * 256
         : KIND == 2 ? 16 * i * (HE * 2) : (32 * (r >> 1) + 8 * (r & 1)) * (D * 2) + j * 256; }
template <int KIND> __device__ __forceinline__ unsigned wvoff(unsigned base, int wq, int lane) {
    const int q = lane >> 4, jj = 4 * (wq & 1) + q;
    const int row = KIND == 0 ? 16 * (wq >> 1) + 8 * (q >> 1) + 4 * (wq & 1) + (q & 1) : KIND == 1 ? 4 * (q >> 1) + 8 * (wq >> 1) + 2 * (wq & 1) + 16 * (q & 1)
                  : KIND == 2 ? 4 * wq + q : 4 * (wq >> 1) + (jj >> 1) + 16 * (jj & 1);
    return row * ((KIND & 1) ? D * 2 : HE * 2) + base + (lane & 15) * 16;
}
template <int KIND> __device__ __forceinline__ void wissue(u32x4 (&v)[40], rsrc_t rw, unsigned voff, int r0, int r1) {
    __builtin_amdgcn_sched_barrier(0);
    #pragma unroll
    for (int i = 0; i < 40; ++i) if (i >= 5 * r0 && i < 5 * r1) v[i] = __builtin_amdgcn_raw_buffer_load_b128(rw, voff, wsoff<KIND>(i / 5, i % 5), WAUX);
    __builtin_amdgcn_sched_barrier(0);
}
__device__ __forceinline__ u32x2 ld_pol(rsrc_t rs, int lane) { asm volatile("" ::: "memory"); return __builtin_amdgcn_raw_buffer_load_b64(rs, WS_POL + (lane & (NPG - 1)) * 8, 0, 16); }
template <bool WO>
__device__ __forceinline__ void load_w_smf(u32x8 (&W)[20], rsrc_t rw, unsigned base, int wave, int lane, lptr L, rsrc_t rs, u32x2& pv) {
    const int wq = wave & 3;
    u32x4 v[40];
    const unsigned voff = wvoff<WO ? 1 : 0>(base, wq, lane);
    wissue<WO ? 1 : 0>(v, rw, voff, 0, WD);
    lptr wp = L + (wave >> 2) * 20480 + ((lane >> 2) & 3) * 1024 + (4 * wq + (lane >> 4)) * 64 + (lane & 3) * 16;
    lptr rp = L + (wave >> 2) * 20480 + (WO ? 5 * wq : wq) * 1024 + (lane >> 2) * 64 + (lane & 3) * 8;
    #pragma unroll
    for (int r = 0; r < 8; ++r) {
        #pragma unroll
        for (int j = 0; j < 5; ++j) lwr(wp + (r & 1) * 40960 + j * 4096, v[5 * r + j]);
        bar();
        #pragma unroll
        for (int j = 0; j < 5; ++j) { const int i = 5 * r + j, pi = WO ? r & 3 : i & 3;
            lptr p = rp + (r & 1) * 40960 + j * (WO ? 1024 : 4096);
            const u32x2 r0 = trd(p), r1 = trd(p + 32);
            const int i0 = WO ? 4 * j + (r >> 2) : 2 * (i >> 2), i1 = WO ? i0 + 2 : i0 + 1;
            W[i0][2 * pi] = r0.x; W[i0][2 * pi + 1] = r0.y; W[i1][2 * pi] = r1.x; W[i1][2 * pi + 1] = r1.y; }
        wissue<WO ? 1 : 0>(v, rw, voff, r + WD, r + WD + 1);
        if (r == 6) { pv = ld_pol(rs, lane); __builtin_amdgcn_sched_barrier(0); }
    }
    bar();
}
template <bool WO, class FN>
__device__ __forceinline__ void load_w_32(u32x4 (&W)[40], rsrc_t rw, unsigned base, int wave, int lane, lptr L, FN hook) {
    const int wq = wave & 3, G = lane >> 4;
    u32x4 v[40];
    const unsigned voff = wvoff<WO ? 3 : 2>(base, wq, lane);
    wissue<WO ? 3 : 2>(v, rw, voff, 0, WD);
    lptr wp = L + (wave >> 2) * 20480 + ((lane >> 2) & 3) * 1024 + (4 * wq + (lane >> 4)) * 64 + (lane & 3) * 16;
    lptr rp = L + (wave >> 2) * 20480 + (WO ? 5 * wq : wq) * 1024 + (8 * (G >> 1) + ((lane >> 2) & 3)) * 64 + (G & 1) * 32 + (lane & 3) * 8;
    #pragma unroll
    for (int r = 0; r < 8; ++r) {
        #pragma unroll
        for (int j = 0; j < 5; ++j) lwr(wp + (r & 1) * 40960 + j * 4096, v[5 * r + j]);
        bar();
        #pragma unroll
        for (int j = 0; j < 5; ++j) { lptr p = rp + (r & 1) * 40960 + j * (WO ? 1024 : 4096);
            const u32x2 r0 = trd(p), r1 = trd(p + 256);
            W[WO ? 8 * j + r : 5 * r + j] = (u32x4){r0.x, r0.y, r1.x, r1.y}; }
        __builtin_amdgcn_sched_barrier(0); hook(r);
        wissue<WO ? 3 : 2>(v, rw, voff, r + WD, r + WD + 1);
    }
    bar();
}

struct Pc { unsigned g1, g2; int p1, p2; };
__device__ __forceinline__ Pc mkpc(int p1, int p2, int lane) { Pc r; r.p1 = p1; r.p2 = p2; r.g1 = WS_SLOT + SL_MEM + (p1 >> 3) * CH + (p1 & 7) * 1024 + lane * 16; r.g2 = WS_SLOT + SL_MEM + (p2 >> 3) * CH + (p2 & 7) * 1024 + lane * 16; return r; }
template <int NP>
__device__ __forceinline__ void gather_mem(const Args& a, rsrc_t rs, unsigned so, bool fresh, const Pc& pc, lptr A, bool& dead) {
    if (NP == 0) return;
    u32x4 v0 = {~0u, ~0u, ~0u, ~0u}, v1 = {0, 0, 0, 0};
    if (!fresh) { v0 = lrd(A + pc.p1 * 1024); if (NP == 2) v1 = lrd(A + pc.p2 * 1024); }
    for (unsigned sp = 0;; ++sp) {
        unsigned m = mx4(v0, 0); if (NP == 2) m = mx4(v1, m);
        if (all_ok(m) || dead) break;
        if (sp > SPIN_MAX) { dead = true; break; }
        asm volatile("" ::: "memory");
        if (m == 0xFFFFFFFFu) { v0 = ld_sc1(rs, pc.g1, so); if (NP == 2) v1 = ld_sc1(rs, pc.g2, so); }
    }
    lwr(A + pc.p1 * 1024, v0);
    if (NP == 2) lwr(A + pc.p2 * 1024, v1);
}
struct Fm { u32x4 a, b; };
__device__ __forceinline__ Fm ld_mem0(const Args& a, int wave, int l) {
    const u16* s = a.mem0 + (2 * ((l >> 2) & 3) + (l & 1)) * D + 16 * (l >> 4) + 8 * ((l >> 1) & 1);
    Fm r; r.a = *(const u32x4*)(s + 64 * wave); r.b = *(const u32x4*)(s + 64 * ((wave & 1) + 8));
    return r;
}
__device__ __forceinline__ void first_mem(const Fm& m, int wave, lptr A) { lwr(A + wave * 1024, m.a); lwr(A + ((wave & 1) + 8) * 1024, m.b); }
__device__ __forceinline__ unsigned long long get_pol(rsrc_t rs, int lane, bool& dead, u32x2 tag, u32x2 v) {
    for (unsigned sp = 0;; ++sp) {
        if (__builtin_amdgcn_ballot_w64(v.x != tag.x || ((v.y ^ tag.y) << 1) != 0u) == 0ull || dead) break;
        if (sp > SPIN_MAX) { dead = true; break; }
        if (sp) dly(8);
        v = ld_pol(rs, lane); }
    asm volatile("" ::: "memory");
    return __builtin_amdgcn_ballot_w64(v.y != tag.y);
}
#define PG(p) (((unsigned)(p) << 21) + (((unsigned)(pol >> (p)) & 1u) << 13))
#define SO(t) PG((t) & (RS - 1))
#define FO(t) (SO(t) ^ 8192u)
#define RO(s, odd) (PG(2 * ((unsigned)(s) % RC) + (odd)) ^ 8192u)
#define AO(s) (SO(s) ^ ((s) >= NSEG - 2 ? 0u : 8192u))
#define RI(s) ((s) & (RS - 1))
#define CO(s, odd) (RO(s, odd) ^ (CFN ? 8192u : 0u))
__device__ __forceinline__ unsigned rt32() { return (unsigned)__builtin_amdgcn_s_memrealtime(); }
__device__ __forceinline__ void gate(unsigned target) {
    asm volatile("1:\n\ts_memrealtime vcc\n\ts_waitcnt lgkmcnt(0)\n\ts_sub_i32 vcc_lo, %0, vcc_lo\n\ts_cmp_gt_i32 vcc_lo, 0\n\ts_cbranch_scc1 1b" :: "s"(target) : "vcc", "scc", "memory"); }
template <int N> __device__ __forceinline__ int rori(int a) { return __builtin_amdgcn_update_dpp(0, a, 0x120 + N, 0xf, 0xf, true); }
__device__ __forceinline__ unsigned read_G(rsrc_t rs, unsigned so, int lane, bool& dead) {
    u32x2 v;
    for (unsigned sp = 0;; ++sp) { asm volatile("" ::: "memory");
        v = __builtin_amdgcn_raw_buffer_load_b64(rs, WS_SLOT + SL_STP + (lane < 40 ? lane : 0) * 32, so, 16);
        if (all_ok(v.x > v.y ? v.x : v.y) || dead) break;
        if (sp > SPIN_MAX) { dead = true; break; }
        dly(2); }
    const unsigned ref = rt32();
    int d = max((int)(v.x - ref), (int)(v.y - ref));
    d = max(d, rori<8>(d)); d = max(d, rori<4>(d)); d = max(d, rori<2>(d)); d = max(d, rori<1>(d));
    { auto r = __builtin_amdgcn_permlane16_swap(d, d, false, false); d = max((int)r[0], (int)r[1]); }
    { auto r = __builtin_amdgcn_permlane32_swap(d, d, false, false); d = max((int)r[0], (int)r[1]); }
    d = min(max(d, -100000), 600);
    return ref + (unsigned)__builtin_amdgcn_readfirstlane(d);
}
constexpr int L_FAIL = 63488;
constexpr int L_DUM = 63744;
#define LS2(x0, x1)
#define DG(x)
__device__ __forceinline__ void dma1(rsrc_t rs, lptr d, unsigned vo, unsigned so) { __builtin_amdgcn_raw_ptr_buffer_load_lds(rs, (LAS void*)d, 16, vo, so, 0, 16); }
__device__ __forceinline__ void dma4(rsrc_t rs, lptr d, unsigned vo, unsigned so) {
    __builtin_amdgcn_raw_ptr_buffer_load_lds(rs, (LAS void*)d, 16, vo, so, 0, 16); __builtin_amdgcn_raw_ptr_buffer_load_lds(rs, (LAS void*)d, 16, vo, so, 1024, 16);
    __builtin_amdgcn_raw_ptr_buffer_load_lds(rs, (LAS void*)d, 16, vo, so, 2048, 16); __builtin_amdgcn_raw_ptr_buffer_load_lds(rs, (LAS void*)d, 16, vo, so, 3072, 16); }
template <int NP>
__device__ __forceinline__ void dma_mem(rsrc_t rs, unsigned so, const Pc& pc, lptr L) {
    if (NP > 0) __builtin_amdgcn_raw_ptr_buffer_load_lds(rs, (LAS void*)(L + L_A + pc.p1 * 1024), 16, pc.g1, so, 0, 16);
    if (NP > 1) __builtin_amdgcn_raw_ptr_buffer_load_lds(rs, (LAS void*)(L + L_A + pc.p2 * 1024), 16, pc.g2, so, 0, 16);
}
#define FETCH(NP, GATE) do { const unsigned so_ = SO(t > 0 ? t - 1 : 0); \
    if (__builtin_expect(!again && gated && t > 1, 1)) { tagc = t; GATE; ST(0); bar(); ST(5); dma_mem<NP>(rs, so_, pc, L); } \
    else { if (t > 0) gather_mem<NP>(a, rs, so_, !again, pc, Asrc, dead); tagc = -5; } } while (0)
__device__ __forceinline__ void flag(unsigned m, int t, int lane, lptr L) { *(LAS int*)(L + (m == 0xFFFFFFFFu ? L_FAIL : L_DUM + lane * 4)) = t; }
template <int NP>
__device__ __forceinline__ void check_own(int t, int lane, const Pc& pc, lptr A, lptr L) {
    unsigned m = mx4(lrd(A + pc.p1 * 1024), 0);
    if (NP == 2) m = mx4(lrd(A + pc.p2 * 1024), m);
    flag(m, t, lane, L);
}
__device__ __forceinline__ bool failed(int t, lptr L) { return __builtin_expect(__builtin_amdgcn_readfirstlane(*(LAS int*)(L + L_FAIL)) == t, 0); }
#define SB() __builtin_amdgcn_sched_barrier(0)
#define VPT 1
#define QPT 2
#define VE 1
#define VD0 10
#define HAUX 16
#define FQ 6
#define FV 10
#define UBAR 3
#define NCAL 3
#define NB1 8
#define NB2 0
#define PH1 2
template <int B> struct Ic { static constexpr int value = B; };
__device__ __forceinline__ unsigned lds_inc(LAS unsigned* p) { unsigned old; asm volatile("ds_add_rtn_u32 %0, %1, %2" : "=v"(old) : "v"(p), "v"(1u) : "memory"); return old; }
struct Nop { __device__ void operator()() const {} };
template <int KIND, class FN = Nop>
__device__ __forceinline__ void proj2(const u32x8 (&W)[20], lptr Asrc, lptr dstp, int idx, int t, const Pc& pc, int lane, lptr L, bool young, unsigned& dg_, FN mid = Nop()) {
    f32x4 e = {0, 0, 0, 0}, o = {0, 0, 0, 0};
    u32x4 af[10], own, own2;
    asm volatile("" : "+v"(e), "+v"(o));
    if (KIND == 1) { drain(); DG(rt32()); }
    if (KIND == 3) { asm volatile("s_waitcnt vmcnt(1)" ::: "memory"); DG(rt32()); }
    if (KIND == 4) { if (young) asm volatile("s_waitcnt vmcnt(8)" ::: "memory"); else drain(); DG(rt32()); }
    bar();
    #pragma unroll
    for (int ks = 0; ks < 4; ++ks) af[ks] = lrd(Asrc + ks * 1024);
    SB();
    #pragma unroll
    for (int ks = 0; ks < PH1; ++ks) { e = smf(af[ks], W[2 * ks], e, idx); o = smf(af[ks], W[2 * ks + 1], o, idx); }
    asm volatile("" : "+v"(e), "+v"(o));
    if (KIND == 2 || KIND == 3) drain();
    if (KIND == 5) asm volatile("s_waitcnt vmcnt(1)" ::: "memory");
    if (KIND == 2 || KIND == 5) DG(rt32());
    bar();
    if (KIND != 4) own = lrd(Asrc + pc.p1 * 1024);
    if (KIND == 3) own2 = lrd(Asrc + pc.p2 * 1024);
    SB();
    #pragma unroll
    for (int ks = 4; ks < 10; ++ks) af[ks] = lrd(Asrc + ks * 1024);
    SB();
    #pragma unroll
    for (int ks = PH1; ks < 4; ++ks) { e = smf(af[ks], W[2 * ks], e, idx); o = smf(af[ks], W[2 * ks + 1], o, idx); }
    SB();
    if (KIND != 4) { asm volatile("" : "+v"(own)); unsigned m = mx4(own, 0); if (KIND == 3) m = mx4(own2, m); flag(m, t, lane, L); }
    #pragma unroll
    for (int ks = 4; ks < 10; ++ks) { e = smf(af[ks], W[2 * ks], e, idx); o = smf(af[ks], W[2 * ks + 1], o, idx); if (ks == 5) { asm volatile("" : "+v"(e), "+v"(o)); SB(); mid(); SB(); } }
    *(LAS unsigned*)dstp = pk2(e[0] + e[2], o[0] + o[2]); *(LAS unsigned*)(dstp + 16) = pk2(e[1] + e[3], o[1] + o[3]);
}
#define PROJ(KIND, GATE, YOUNG) FETCH((KIND == 3 ? 2 : KIND == 4 ? 0 : 1), GATE); proj2<KIND>(W, Asrc, pdst, sidx, t, pc, lane, L, YOUNG, dg_); ST(2); bar(); ST(3); \
        again = failed(tagc, L); if (again) { --t; continue; } DGL;
__device__ __forceinline__ void max3x2(float a, float b, float c, float& r0, float& r1) { asm("v_maximum3_f32 %0, %2, %3, %4\n\tv_maximum3_f32 %1, %2, %3, %4\n\ts_nop 1" : "=&v"(r0), "=&v"(r1) : "v"(a), "v"(b), "v"(c)); }
__device__ __forceinline__ void addx2(float a, float b, float& r0, float& r1) { asm("v_add_f32 %0, %2, %3\n\tv_add_f32 %1, %2, %3\n\ts_nop 1" : "=&v"(r0), "=&v"(r1) : "v"(a), "v"(b)); }
__device__ __forceinline__ void sw16(float& a, float& b) { auto r = __builtin_amdgcn_permlane16_swap(__float_as_uint(a), __float_as_uint(b), false, false); a = __uint_as_float(r[0]); b = __uint_as_float(r[1]); }
__device__ __forceinline__ void sw32(float& a, float& b) { auto r = __builtin_amdgcn_permlane32_swap(__float_as_uint(a), __float_as_uint(b), false, false); a = __uint_as_float(r[0]); b = __uint_as_float(r[1]); }
__device__ __forceinline__ int proj2_dst(int wq, int lane) { const int c = lane & 15; return (2 * wq + (c >> 3)) * 256 + (4 * (lane >> 4) + 2 * ((c >> 2) & 1)) * 16 + (c & 3) * 4; }
__device__ __forceinline__ int tokoff(int tok) { return (4 * (tok >> 1) + (tok & 1)) * 16; }

__device__ __forceinline__ u32x4 pk8(const f32x16& a, int p) { return (u32x4){pk2(a[4 * p], a[4 * p + 8]), pk2(a[4 * p + 1], a[4 * p + 9]), pk2(a[4 * p + 2], a[4 * p + 10]), pk2(a[4 * p + 3], a[4 * p + 11])}; }

constexpr float CS = 0.08838834764831845f * 1.4426950408889634f;
enum { R_P, R_V, R_C, R_OUT, R_QK, R_VO, R_F1, R_F2, R_SP };
#define FSW 1
#define HM 1
#define RMAP 2
__host__ __device__ inline void rolemap(int b, int& role, int& id) {
    const int grp = b & 7, idx8 = b >> 3;
    if (RMAP == 2) {
        if (grp < 4) {
            if (idx8 < 8) { role = R_P; id = HM ? grp * 8 + idx8 : idx8 * 4 + grp; } else if (idx8 < 16) { role = R_V; id = HM ? grp * 8 + idx8 - 8 : (idx8 - 8) * 4 + grp; } else if (idx8 < 26) { role = R_C; id = grp * 10 + idx8 - 16; }
            else if (FSW) { const int k = (idx8 - 26) * 4 + grp; if (k < 23) { role = R_F1; id = k; } else { role = R_SP; id = 0; } }
            else if (idx8 < 31) { role = R_F2; id = (idx8 - 26) * 4 + grp; } else if (grp < 3) { role = R_F1; id = 24 + grp; } else { role = R_SP; id = 0; }
        } else if (idx8 < 10) { role = R_OUT; id = (grp - 4) * 10 + idx8; }
        else { const int wi = (idx8 - 10) * 4 + grp - 4;
            if (wi < 32) { role = R_VO; id = HM ? (wi & 3) * 8 + (wi >> 2) : wi; } else if (wi < 64) { role = R_QK; id = HM ? (wi & 3) * 8 + ((wi - 32) >> 2) : wi - 32; } else if (!FSW) { role = R_F1; id = wi - 64; }
            else if (wi < 84) { role = R_F2; id = wi - 64; } else { role = R_F1; id = wi - 61; } }
        return;
    }
    if (RMAP == 3) {
        if (idx8 < 16) { role = idx8 < 4 ? R_P : idx8 < 8 ? R_QK : idx8 < 12 ? R_V : R_VO; id = (idx8 & 3) * 8 + grp; }
        else if (idx8 < 21) { role = R_C; id = grp * 5 + idx8 - 16; }
        else if (idx8 < 25) { role = R_OUT; id = grp * 4 + idx8 - 21; }
        else if (idx8 < 27 && grp < 4) { role = R_OUT; id = 32 + grp * 2 + idx8 - 25; }
        else { const int k = idx8 < 27 ? (idx8 - 25) * 4 + grp - 4 : 8 + (idx8 - 27) * 8 + grp;
            if (k < 27) { role = R_F1; id = k; } else if (k < 47) { role = R_F2; id = k - 27; } else { role = R_SP; id = 0; } }
        return;
    }
    if (grp == 0) { role = R_P; id = idx8; return; }
    if (grp == 1) { role = R_V; id = idx8; return; }
    if (RMAP == 0) {
        if (grp == 7) { if (idx8 < 27) { role = R_F1; id = idx8; } else { role = R_F2; id = idx8 - 27; } }
        else if (idx8 < 8) { role = R_C; id = (grp - 2) * 8 + idx8; }
        else if (idx8 < 16) { role = R_OUT; id = (grp - 2) * 8 + idx8 - 8; }
        else { const int wi = (idx8 - 16) * 5 + grp - 2;
            if (wi < 32) { role = R_QK; id = wi; } else if (wi < 64) { role = R_VO; id = wi - 32; } else if (wi < 79) { role = R_F2; id = wi - 59; } else { role = R_SP; id = 0; } }
    } else {
        if (grp < 4) {
            if (idx8 < 20) { role = R_C; id = (grp - 2) * 20 + idx8; }
            else if (idx8 < 30) { role = R_F2; id = (idx8 - 20) * 2 + grp - 2; }
            else { const int k = (idx8 - 30) * 2 + grp - 2; if (k < 3) { role = R_QK; id = 29 + k; } else { role = R_SP; id = 0; } }
        } else if (idx8 < 10) { role = R_OUT; id = (grp - 4) * 10 + idx8; }
        else { const int wi = (idx8 - 10) * 4 + grp - 4;
            if (wi < 32) { role = R_VO; id = wi; } else if (wi < 61) { role = R_QK; id = wi - 32; } else { role = R_F1; id = wi - 61; } }
    }
}
__global__ void __launch_bounds__(512, 2) mega(Args a) {
    extern __shared__ __attribute__((aligned(16))) unsigned char smem[];
    lptr L = (lptr)smem;
    const int tid = threadIdx.x, lane = tid & 63, wave = __builtin_amdgcn_readfirstlane(tid >> 6);
    int role, id; rolemap(blockIdx.x, role, id);
    const int hid = role == R_C ? id : role == R_OUT ? 40 + id : role == R_F1 ? 80 + id : role == R_F2 ? 107 + id : role == R_SP ? 127 : -1;
    const rsrc_t rs = mkrs(a.ws, WS_END);
    const int sidx = (lane & 2) ? 0xEEEE : 0x4444;
    bool dead = false;
    lptr stg = L + L_STG + wave * 4096;
    ST_INIT
    TL(0);
    const unsigned tstart = rt32();
    unsigned long long ep;
    asm volatile("s_load_dwordx2 %0, %1, 0x0\n\ts_waitcnt lgkmcnt(0)" : "=s"(ep) : "s"(a.ws + WS_EP) : "memory");
    ++ep;
    const u32x2 tag = {(unsigned)ep, (unsigned)(ep >> 32)};
    unsigned long long pol = 0;
    if (hid >= 0) {
        const int row = hid * 16 + wave * 2 + (lane >> 5), l32 = lane & 31;
        if (hid < 120) {
            #pragma unroll
            for (int i = 0; i < 2; ++i) { const unsigned u = (i * 120 + hid) * 8192 + tid * 16;
                st128((u32x4){~0u, ~0u, ~0u, ~0u}, rs, u < SL_END ? u : u < 2 * SL_END ? u - SL_END + SLOT : u < 2 * SL_END + 3 * CH ? u - 2 * SL_END + 2 * SLOT + 2 * CH : OOB, WS_SLOT); } }
        if (hid < 96 && tid < 128) st32(~0u, rs, WS_FLG + (hid * 128 + tid) * FS, 0);
        u32x2 xv[5];
        #pragma unroll
        for (int i = 0; i < 5; ++i) xv[i] = *(const u32x2*)(a.x + row * D + (l32 + 32 * i) * 4);
        if (hid < 120) {
            u32x4 wv[5]; float sv[5];
            #pragma unroll
            for (int i = 0; i < 5; ++i) { const int k = (tid >> 2) + 128 * i; wv[i] = *(const u32x4*)(a.wm + k * 3840 + hid * 32 + (tid & 3) * 8); sv[i] = b2f(a.c[k]); }
            float acc[8] = {0, 0, 0, 0, 0, 0, 0, 0};
            #pragma unroll
            for (int i = 0; i < 5; ++i) { const float s = rnd(sv[i] / (1.f + __expf(-sv[i])));
                #pragma unroll
                for (int j = 0; j < 4; ++j) { acc[2 * j] += s * blo(wv[i][j]); acc[2 * j + 1] += s * bhi(wv[i][j]); } }
            #pragma unroll
            for (int j = 0; j < 8; ++j) { float v = acc[j]; v += ror<8>(v); v += ror<4>(v); v = x16sum(v); acc[j] = x32sum(v); }
            if (lane < 4) { *(LAS f32x4*)(L + wave * 128 + lane * 32) = (f32x4){acc[0], acc[1], acc[2], acc[3]}; *(LAS f32x4*)(L + wave * 128 + lane * 32 + 16) = (f32x4){acc[4], acc[5], acc[6], acc[7]}; }
            drain(); bar();
            if (tid < 8) { f32x4 s = {0, 0, 0, 0};
                #pragma unroll
                for (int w = 0; w < 8; ++w) s += *(LAS f32x4*)(L + w * 128 + tid * 16);
                st128((u32x4){pk2(s[0], s[1]), pk2(s[2], s[3]), tag.x, tag.y}, rs, WS_MOD + hid * 128 + tid * 16, 0); }
        }
        float xf[20], s = 0;
        #pragma unroll
        for (int i = 0; i < 5; ++i) { xf[4 * i] = blo(xv[i].x); xf[4 * i + 1] = bhi(xv[i].x); xf[4 * i + 2] = blo(xv[i].y); xf[4 * i + 3] = bhi(xv[i].y); }
        #pragma unroll
        for (int i = 0; i < 20; ++i) s += xf[i];
        const float mean = x16sum(sum16(s)) * (1.f / D);
        float vs = 0;
        #pragma unroll
        for (int i = 0; i < 20; ++i) { const float d = xf[i] - mean; vs += d * d; }
        const float var = x16sum(sum16(vs)) * (1.f / D);
        const float mb = rnd(mean), rd = 1.f / rnd(__builtin_sqrtf(rnd(rnd(var) + 1e-5f)));
        #pragma unroll
        for (int i = 0; i < 20; ++i) xf[i] = rnd(rnd(xf[i] - mb) * rd);
        const bool cal = hid >= 8 && hid < 8 + NPG;
        if (cal) {
            const unsigned vo = lane < 32 ? WS_SLOT + SL_CAL + (wave & 3) * 2048 + (wave >> 2) * CH + lane * 4 : OOB;
            unsigned m[2] = {~0u, ~0u};
            for (int r = 0; r < NCAL; ++r)
                #pragma unroll
                for (int q = 0; q < 2; ++q) { unsigned long long t0, t1;
                    asm volatile("s_waitcnt vmcnt(0)\n\ts_memtime %0\n\ts_waitcnt lgkmcnt(0)" : "=s"(t0) :: "memory");
                    st32(0u, rs, vo, (hid - 8) * SLOT + q * 8192);
                    asm volatile("s_waitcnt vmcnt(0)\n\ts_memtime %0\n\ts_waitcnt lgkmcnt(0)" : "=s"(t1) :: "memory");
                    m[q] = min(m[q], (unsigned)(t1 - t0)); }
            *(LAS u32x2*)(L + 2048 + wave * 8) = (u32x2){m[0], m[1]};
        }
        {   u32x4 mv, mw; const int j = tid < 480 ? tid : 479;
            for (unsigned sp = 0;; ++sp) { asm volatile("" ::: "memory"); mv = ld_sc1(rs, WS_MOD + j * 32, 0); mw = ld_sc1(rs, WS_MOD + j * 32 + 16, 0);
                if (__builtin_amdgcn_ballot_w64(mv.z != tag.x || mv.w != tag.y || mw.z != tag.x || mw.w != tag.y) == 0ull) break;
                if (sp > SPIN_MAX) { dead = true; break; } dly(2); }
            lwr(L + L_MOD + j * 16, (u32x4){mv.x, mv.y, mw.x, mw.y}); }
        bar();
        if (cal && wave == 0) { const u32x2 mm = *(LAS u32x2*)(L + 2048 + (lane & 7) * 8);
            const unsigned bm = (unsigned)__builtin_amdgcn_ballot_w64(mm.y < mm.x);
            st64((u32x2){tag.x, tag.y ^ (__builtin_popcount(bm & 0xFEu) > 3 ? 0x80000000u : 0u)}, rs, lane ? OOB : WS_POL + (hid - 8) * 8, 0); }
        #pragma unroll
        for (int i = 0; i < 5; ++i) { const u32x2 sa = *(LAS u32x2*)(L + L_MOD + (l32 + 32 * i) * 8), ca = *(LAS u32x2*)(L + L_MOD + 1280 + (l32 + 32 * i) * 8);
            u32x2 o;
            o.x = pk2(rnd(xf[4 * i] * rnd(1.f + blo(ca.x))) + blo(sa.x), rnd(xf[4 * i + 1] * rnd(1.f + bhi(ca.x))) + bhi(sa.x));
            o.y = pk2(rnd(xf[4 * i + 2] * rnd(1.f + blo(ca.y))) + blo(sa.y), rnd(xf[4 * i + 3] * rnd(1.f + bhi(ca.y))) + bhi(sa.y));
            *(LAS u32x2*)(L + 4096 + ((l32 >> 1) + 16 * i) * 272 + (row & 15) * 16 + (l32 & 1) * 8) = o; }
        bar();
        #pragma unroll
        for (int i = 0; i < 3; ++i) { const int c = (tid >> 4) + 32 * i;
            st128(lrd(L + 4096 + c * 272 + (tid & 15) * 16), rs, c < 80 ? c * 512 + (tid & 15) * 16 : OOB, WS_H1 + (hid >> 1) * 40960 + (hid & 1) * 256); }
        drain(); bar();
        if (tid == 0) st64(tag, rs, WS_FH1 + hid * 8, 0);
        const u32x2 pv0 = ld_pol(rs, lane);
        TL(1);
        pol = get_pol(rs, lane, dead, tag, pv0);
        TL(4);
    }
    const bool gated = a.tp[0] > 0;
    const unsigned DLT = a.tp[0], DU = a.tp[2];
    int fs0 = 0, fst = 1, fse = a.tp[19];
    int tagc = 0; bool again = false; unsigned nfail = 0, dg_ = 0;
#define DGL
    if (role == R_P) {
        const int h = id;
        u32x8 W[20];
        const Fm fm = ld_mem0(a, wave, lane);
        u32x2 pv0;
        load_w_smf<false>(W, mkrs(wave < 4 ? a.Wq : a.Wk, D * HE * 2), h * 256, wave, lane, L, rs, pv0);
        TL(1);
        if (tid == 0) *(LAS int*)(L + L_FAIL) = -1;
        lptr Asrc = L + L_A + lane * 16;
        lptr pdst = L + L_QK + (wave >> 2) * 2048 + proj2_dst(wave & 3, lane);
        first_mem(fm, wave, Asrc);
        pol = get_pol(rs, lane, dead, tag, pv0);
        asm volatile("" : "+v"(pdst));
        ST(0);
        if (wave == 0) {
            const int rho = lane & 15, g = lane >> 4;
            const Pc pc = mkpc(4, 4, lane);
            const int key = (((rho >> 2) & 1) << 3) | (((rho >> 1) & 1) << 2) | (((rho >> 3) & 1) << 1) | (rho & 1);
            lptr qsrc = L + L_QK + (g >> 1) * 256 + (g & 1) * 32 + tokoff(rho & 7);
            lptr msrc = L + L_QK + 2048 + (g >> 1) * 256 + (g & 1) * 32 + tokoff(2 * (rho >> 2) + (rho & 1));
            lptr ksrc = L + L_KQX + (g * 32 + key) * 16;
            asm volatile("" : "+v"(qsrc), "+v"(msrc));
            const bool ch = (lane & 8) != 0;
            const unsigned vpx = WS_SLOT + SL_PX + (h >> 4) * CH + (h & 15) * 512 + ((lane >> 3) & 1) * 256 + (g >> 1) * 32 + tokoff(lane & 7) + (g & 1) * 8;
            const unsigned vpm = ch ? OOB : WS_SLOT + SL_PM + ((lane >> 2) & 1) * 2048 + (h >> 1) * 128 + tokoff(lane & 3) + (g & 1) * 32 + (h & 1) * 8 + (g >> 1) * 4;
            unsigned long long rt0 = 0;
            for (int t = 0; t < NSEG; ++t) {
                ST_T(t); if (t == 2) { ST_RESET; }
                u32x4 k0[4], k1[4];
                int fl;
                unsigned so = SO(t);
                lptr kp = ksrc + (t & 1) * 16384;
                asm volatile("" : "+s"(so), "+v"(kp));
                auto ldk = [&]() {
                    #pragma unroll
                    for (int s = 0; s < 4; ++s) { k0[s] = lrd(kp + s * 2048); k1[s] = lrd(kp + s * 2048 + 256); } };
                auto scores = [&]() -> bool {
                    u32x4 q[4], km[4];
                    #pragma unroll
                    for (int s = 0; s < 4; ++s) { q[s] = lrd(qsrc + s * 512); SB(); }
                    #pragma unroll
                    for (int s = 0; s < 4; ++s) { km[s] = lrd(msrc + s * 512); SB(); }
                    fl = *(LAS int*)(L + L_FAIL);
                    SB();
                    f32x4 sm = {0, 0, 0, 0}, s0 = sm, s1 = sm;
                    #pragma unroll
                    for (int s = 0; s < 4; ++s) { s0 = mf16(k0[s], q[s], s0); s1 = mf16(k1[s], q[s], s1); }
                    asm volatile("" : "+v"(s0), "+v"(s1), "+v"(km[0]), "+v"(fl));
                    #pragma unroll
                    for (int s = 0; s < 4; ++s) sm = mf16(km[s], q[s], sm);
                    asm volatile("" : "+v"(sm));
                    if (__builtin_expect(__builtin_amdgcn_readfirstlane(fl) == tagc, 0)) return true;
                    const float mx = fmx(fmx(fmx(fmx(s0[0], s0[1]), s0[2]), fmx(fmx(s0[3], s1[0]), s1[1])), fmx(s1[2], s1[3]));
                    float v0 = ch ? s1[0] : s0[0], v1 = ch ? s1[1] : s0[1], v2 = ch ? s1[2] : s0[2], v3 = ch ? s1[3] : s0[3];
                    SB();
                    float ma = fmx(fmx(mx, sm[0]), sm[1]), mb = fmx(fmx(mx, sm[1]), sm[0]), m2, m3;
                    float vm = ch ? sm[1] : sm[0];
                    sw16(ma, mb); max3x2(ma, mb, mb, m2, m3); sw32(m2, m3);
                    const float mc = fmx(m2, m3) * CS;
                    v0 = __builtin_amdgcn_exp2f(v0 * CS - mc); v1 = __builtin_amdgcn_exp2f(v1 * CS - mc); v2 = __builtin_amdgcn_exp2f(v2 * CS - mc); v3 = __builtin_amdgcn_exp2f(v3 * CS - mc); vm = __builtin_amdgcn_exp2f(vm * CS - mc);
                    float sa, sb2, sc, sd;
                    addx2((v0 + v1) + v2, v3 + vm, sa, sb2); sw16(sa, sb2); addx2(sa, sb2, sc, sd); sw32(sc, sd);
                    float sum = sc + sd;
                    const float vmo = ror<8>(vm);
                    sum += ror<8>(sum);
                    const float inv = __builtin_amdgcn_rcpf(sum);
                    u32x2 px; px.x = pk2(v0 * inv, v1 * inv); px.y = pk2(v2 * inv, v3 * inv);
                    st64(px, rs, vpx, so);
                    st32(pk2(vm * inv, vmo * inv), rs, vpm, so);
                    return false; };
                FETCH(1, );
                proj2<2>(W, Asrc, pdst, sidx, t, pc, lane, L, false, dg_, ldk);
                ST(2);
                bar();
                ST(3);
                again = scores();
                if (again) { --t; ++nfail; continue; }
                ST(4); LG(0, rt32()); DGL;
            }
        } else if (wave == 1) {
            const int r32 = lane & 31, hf = lane >> 5;
            const int prow = (r32 & 19) | ((r32 & 4) << 1) | ((r32 & 8) >> 1);
            lptr qsrc = L + L_KQX + 8192 + (hf * 32 + r32) * 16;
            lptr ksrc = L + L_KQX + (hf * 32 + prow) * 16;
            lptr msrc = L + L_QK + 2048 + hf * 32 + tokoff(prow & 7);
            const unsigned vax = WS_AX + (h * 2 + (r32 >> 4)) * 1024 + (hf * 16 + (r32 & 15)) * 16;
            const unsigned vax1 = r32 < 16 ? OOB : vax + 512;
            const unsigned vam = WS_AM + ((h >> 2) * 2 + (r32 >> 4)) * 1024 + ((h & 3) * 16 + (r32 & 15)) * 16;
            const int lim = r32 - 8 * hf;
            const Pc pc = mkpc(5, 5, lane);
            for (int t = 0; t < NSEG; ++t) {
                ST_T(t); if (t == 2) { ST_RESET; }
                FETCH(1, );
                if (tagc < 0) drain(); else asm volatile("s_waitcnt vmcnt(1)" ::: "memory");
                st32(0u, rs, (lane || again || t == 0) ? OOB : WS_PP + h * FS - FSEG, t * FSEG);
                proj2<5>(W, Asrc, pdst, sidx, t, pc, lane, L, false, dg_); bar();
                again = failed(tagc, L); if (again) { --t; continue; } DGL;
                f32x16 sx, sm;
                #pragma unroll
                for (int i = 0; i < 16; ++i) { sx[i] = 0; sm[i] = 0; }
                const int po = (t & 1) * 16384;
                #pragma unroll 2
                for (int s = 0; s < 8; ++s) {
                    u32x4 q = lrd(qsrc + po + s * 1024), k = lrd(ksrc + po + s * 1024), km = lrd(msrc + s * 256);
                    sx = mf32(k, q, sx); sm = mf32(km, q, sm);
                }
                float m = sm[0];
                #pragma unroll
                for (int j = 1; j < 8; ++j) m = fmx(m, sm[j]);
                #pragma unroll
                for (int i = 0; i < 16; ++i) { if (16 * (i >> 3) + (i & 7) > lim) sx[i] = -3.0e38f; m = fmx(m, sx[i]); }
                m = x32max(m);
                const float mc = m * CS; float sum = 0, summ = 0;
                #pragma unroll
                for (int i = 0; i < 16; ++i) { sx[i] = __builtin_amdgcn_exp2f(sx[i] * CS - mc); sum += sx[i]; }
                #pragma unroll
                for (int j = 0; j < 8; ++j) { sm[j] = __builtin_amdgcn_exp2f(sm[j] * CS - mc); summ += sm[j]; }
                sum = x32sum(sum) + summ;
                const float inv = __builtin_amdgcn_rcpf(sum);
                #pragma unroll
                for (int p = 0; p < 2; ++p) { u32x4 o; o.x = pk2(sx[8 * p] * inv, sx[8 * p + 1] * inv); o.y = pk2(sx[8 * p + 2] * inv, sx[8 * p + 3] * inv); o.z = pk2(sx[8 * p + 4] * inv, sx[8 * p + 5] * inv); o.w = pk2(sx[8 * p + 6] * inv, sx[8 * p + 7] * inv);
                    st128(o, rs, p ? vax1 : vax, RI(t) * 65536); }
                if (hf == 0) { u32x4 o; o.x = pk2(sm[0] * inv, sm[1] * inv); o.y = pk2(sm[2] * inv, sm[3] * inv); o.z = pk2(sm[4] * inv, sm[5] * inv); o.w = pk2(sm[6] * inv, sm[7] * inv);
                    st128(o, rs, vam, RI(t) * 16384); }
                ST(4);
            }
            drain();
            if (lane == 0) st32(0u, rs, WS_PP + h * FS, (NSEG - 1) * FSEG);
        } else if (wave < 4) {
            const unsigned vsrc = WS_KQ + h * CH + lane * 16;
            lptr ldst = L + L_KQX + (wave - 2) * 8192;
            int seen = -1; unsigned pv = 1;
            const Pc pc = mkpc(0, 0, lane);
            auto fetch = [&](int t) {
                const int pace = seen >= t ? a.tp[7] : 0;
                wait_fl(rs, WS_PQK + h * FS, t, seen, dead, 2);
                pv = ld_fl(rs, t + 1 >= NSEG ? OOB : WS_PQK + h * FS, t + 1);
                const unsigned so = RO(t, wave - 2);
                for (int i = 0; i < 8; ++i) { dma1(rs, ldst + (t & 1) * 16384 + i * 1024, vsrc, so + i * 1024); dly(pace); } };
            fetch(0);
            for (int t = 0; t < NSEG; ++t) {
                ST_T(t); if (t == 2) { ST_RESET; }
                PROJ(4, , false);
                if (fl_ok(pv)) seen = t + 1;
                if (t + 1 < NSEG) fetch(t + 1);
                ST(4);
            }
        } else {
            unsigned tgt = rt32();
            const Pc pc = mkpc(wave - 4, wave + 2, lane);
            const unsigned vcx = WS_SLOT + SL_PX + (h >> 4) * CH + (h & 15) * 512 + lane * 8, vcm = lane < 16 ? WS_SLOT + SL_PM + (lane >> 3) * 2048 + (h >> 1) * 128 + (lane & 7) * 16 + (h & 1) * 8 : OOB;
            for (int t = 0; t < NSEG; ++t) {
                ST_T(t); if (t == 2) { ST_RESET; }
                PROJ(3, gate(tgt), false);
                if (wave == 7) LG(0, tgt);
                if (wave == 6) { st64((u32x2){~0u, ~0u}, rs, vcx, SO(t + 2)); st64((u32x2){~0u, ~0u}, rs, vcm, SO(t + 2)); }
                tgt = (wave == 7 && gated && t > 0) ? read_G(rs, SO(t - 1), lane, dead) + DLT : rt32();
                ST(4);
            }
        }
    } else if (role == R_V) {
        const int h = id;
        u32x8 W[20];
        if (wave < 4) {
            const Fm fm = ld_mem0(a, wave, lane);
            u32x2 pv0;
            load_w_smf<false>(W, mkrs(a.Wv, D * HE * 2), h * 256, wave, lane, L, rs, pv0);
            TL(1);
            if (tid == 0) *(LAS int*)(L + L_FAIL) = -1;
            lptr pdst = L + L_QK + proj2_dst(wave, lane);
            const Pc pc = mkpc(wave, wave, lane);
            lptr Asrc = L + L_A + lane * 16;
            first_mem(fm, wave, Asrc);
            pol = get_pol(rs, lane, dead, tag, pv0);
            unsigned tgt = rt32();
            for (int t = 0; t < NSEG; ++t) {
                ST_T(t); if (t == 2) { ST_RESET; }
                PROJ(1, gate(tgt), false);
                tgt = (wave == 3 && gated && t > 0) ? read_G(rs, SO(t - 1), lane, dead) + DLT : rt32();
                ST(4);
            }
        } else {
            const Fm fm = ld_mem0(a, wave, lane);
            u32x2 pv0;
            load_w_smf<true>(W, mkrs(a.Wo, D * HE * 2), h * E * D * 2, wave, lane, L, rs, pv0);
            TL(1);
            const unsigned vu = WS_SLOT + SL_U + (10 * (wave - 4) + 8 + ((lane >> 4) & 1)) * CH + h * 256 + (lane & 15) * 16 + (lane >> 5) * 8;
            const unsigned vw = WS_SLOT + SL_U + (10 * (wave - 4) + (lane >> 4)) * CH + h * 256 + (lane & 15) * 16;
            const Pc pc = mkpc(wave, wave + 2, lane);
            lptr Asrc = L + L_A + lane * 16;
            first_mem(fm, wave, Asrc);
            pol = get_pol(rs, lane, dead, tag, pv0);
            ST(0);
            for (int t = 0; t < NSEG; ++t) {
                ST_T(t); if (t == 2) { ST_RESET; }
                f32x4 dd[10];
                int fl;
                unsigned so = SO(t), so2 = so + 4 * CH;
                asm volatile("" : "+s"(so), "+s"(so2));
                auto zero = [&]() {
                    #pragma unroll
                    for (int j = 0; j < 10; ++j) { dd[j] = (f32x4){0, 0, 0, 0}; asm volatile("" : "+v"(dd[j])); } };
                auto ustage = [&]() -> bool {
                    const u32x4 a0 = lrd(L + L_QK + lane * 16); SB();
                    const u32x4 a1 = lrd(L + L_QK + 1024 + lane * 16); SB();
                    fl = *(LAS int*)(L + L_FAIL);
                    SB();
                    unsigned o[10];
                    auto fin = [&](int j) { o[j] = pk2(dd[j][0] + dd[j][2], dd[j][1] + dd[j][3]); };
                    auto s16 = [&](int j) { auto r = __builtin_amdgcn_permlane16_swap(o[j], o[j + 1], false, false); o[j] = r[0]; o[j + 1] = r[1]; };
                    auto s32 = [&](int j) { auto r = __builtin_amdgcn_permlane32_swap(o[j], o[j + 2], false, false); o[j] = r[0]; o[j + 2] = r[1]; };
                    auto m0 = [&](int j) { dd[j] = smf(a0, W[2 * j], dd[j], sidx); };
                    auto m1 = [&](int j) { dd[j] = smf(a1, W[2 * j + 1], dd[j], sidx); };
                    m0(0); m0(1); m0(2); m0(3); SB();
                    m1(0); SB(); const int flu = __builtin_amdgcn_readfirstlane(fl); m1(1); SB(); m1(2); m1(3); asm volatile("" : "+v"(dd[0]), "+v"(dd[1]), "+v"(dd[2]), "+v"(dd[3])); SB();
                    if (__builtin_expect(flu == tagc, 0)) return true;
                    m0(4); fin(0); SB(); m0(5); fin(1); SB(); m0(6); fin(2); SB(); m0(7); s16(0); SB();
                    m1(4); fin(3); SB(); m1(5); SB(); m1(6); s16(2); SB(); m1(7); SB();
                    m0(8); s32(0); s32(1); SB(); m0(9); fin(4); SB();
                    m1(8); st128((u32x4){o[0], o[1], o[2], o[3]}, rs, vw, so); fin(5); SB(); m1(9); fin(6); SB();
                    fin(7); s16(4); fin(8); fin(9); s16(6); s16(8); s32(4); s32(5);
                    st64((u32x2){o[8], o[9]}, rs, vu, so);
                    st128((u32x4){o[4], o[5], o[6], o[7]}, rs, vw, so2);
                    return false; };
                zero();
                if (wave >= 6) { FETCH(2, ); bar(); drain(); bar(); check_own<2>(t, lane, pc, Asrc, L); } else { FETCH(1, ); bar(); drain(); bar(); check_own<1>(t, lane, pc, Asrc, L); }
                bar();
                ST(3);
                again = ustage();
                if (again) { --t; continue; }
                ST(4); LG(0, rt32());
                dly(a.tp[9]);
                const unsigned sc = SO(t + 2);
                st128((u32x4){~0u, ~0u, ~0u, ~0u}, rs, vw, sc); dly(a.tp[13]); st128((u32x4){~0u, ~0u, ~0u, ~0u}, rs, vw, sc + 4 * CH); dly(a.tp[13]);
                st64((u32x2){~0u, ~0u}, rs, vu, sc);
            }
        }
    } else if (role == R_C) {
      const int c = id;
      LAS unsigned* gbox = (LAS unsigned*)(L + 98304);
      constexpr int L_CU = 98560;
      const unsigned cmem = WS_SLOT + SL_MEM + (c >> 5) * CH + (c & 31) * 256;
      if (wave >= 4) {
        const unsigned vcf = WS_CF + ((c % 20) * 4 + wave - 4) * CH + lane * 16;
        int seen = -1;
        const unsigned vpv = WS_PVO + (8 * (wave - 4) + (lane & 7)) * FS;
        lptr cdst = L + (wave - 4) * 8192;
        unsigned pv;
        auto ldcf = [&](int t) {
            const bool known = seen >= t;
            wait_fl(rs, vpv, t, seen, dead, 2);
            pv = ld_fl(rs, vpv, t + 1);
            const unsigned so = CO(t, c >= 20);
            for (int i = 0; i < 8; ++i) { dma1(rs, cdst + (t & 1) * 32768 + i * 1024, vcf, so + i * 1024); if (known && i < 7) dly(a.tp[8]); } };
        const unsigned vclr = wave != 7 ? OOB : lane < 32 ? cmem + lane * 8 : lane == 32 ? WS_SLOT + SL_STP + c * 32 : OOB;
        ldcf(0); drain(); bar();
        for (int t = 0;; ++t) {
            if (fl_ok(pv)) seen = t + 1;
            if (t > 0) st64((u32x2){~0u, ~0u}, rs, vclr, SO(t + 2));
            const bool early = seen > t;
            if (t < NSEG - 2 && early) ldcf(t + 1);
            DG(rt32());
            bar();
            bar();
            if (t == NSEG - 2) break;
            if (!early) ldcf(t + 1);
            drain();
            if (!fl_ok(pv)) pv = ld_fl(rs, vpv, t + 2);
            bar();
            DGL;
        }
      } else if (wave >= 2) {
        const unsigned vu = WS_SLOT + SL_U + c * CH + (wave - 2) * 4096 + lane * 16;
        unsigned G = rt32() - 100000u;
        *gbox = G;
        bar();
        for (int t = 0;; ++t) {
            const unsigned so = SO(t);
            unsigned d2_ = 0;
            DG(rt32());
            gate(G + DU);
            bar();
            dma4(rs, L + L_CU + (wave - 2) * 4096, vu, so);
            drain();
            bar();
            if (t == NSEG - 2) break;
            bar();
            if (wave == 2 && gated) {
                dly(a.tp[11]);
                G = read_G(rs, so, lane, dead);
                *gbox = G; }
            if (wave == 3 && c == 0 && t + 4 >= RS) { int sn = -1; wait_fl(rs, WS_PF2 + (lane < 20 ? lane : 0) * FS, t + 4 - RS, sn, dead, 4); }
        }
      } else {
        const int rh = wave, rho = lane & 15, kg = lane >> 4;
        u32x4 cf[32];
        auto ldcf = [&](int t) {
            #pragma unroll
            for (int i = 0; i < 32; ++i) cf[i] = lrd(L + (t & 1) * 32768 + lane * 16 + i * 1024); };
        bar();
        ldcf(0);
        const unsigned opx = WS_SLOT + SL_PX + (2 * (rho >> 3) + (kg >> 1)) * 512 + (kg & 1) * 256 + rh * 128 + (rho & 7) * 16;
        const unsigned opm = WS_SLOT + SL_PM + rh * 2048 + (4 * (rho >> 3) + kg) * 128 + (rho & 7) * 16;
        const unsigned ou = WS_SLOT + SL_U + c * CH + lane * 16;
        const unsigned vmem = (lane & 1) ? OOB : cmem + rh * 128 + (4 * (kg & 1) + 2 * (rho >> 3) + (lane >> 5)) * 16 + (rho & 7) * 2;
        const unsigned vstp = lane ? OOB : WS_SLOT + SL_STP + c * 32 + rh * 4;
        const bool hi = lane >= 32;
        unsigned nlate = 0;
        for (int t = 0; t < NSEG - 1; ++t) {
            ST_T(t); if (t == 2) { ST_RESET; }
            const unsigned sb = SO(t);
            u32x4 p[10], u[8];
            f32x4 ca, cb;
            unsigned res; float v;
            auto ldu = [&]() {
                #pragma unroll
                for (int i = 0; i < 8; ++i) u[i] = ld_sc1(rs, ou + (i & 3) * 1024, sb + (i >> 2) * 4096); };
            auto ldp = [&]() {
                #pragma unroll
                for (int i = 0; i < 8; ++i) { p[i] = ld_sc1(rs, opx + (i & 1) * 2048, sb + (i >> 2) * CH + ((i >> 1) & 1) * 4096); __builtin_amdgcn_sched_barrier(0); }
                p[8] = ld_sc1(rs, opm, sb); __builtin_amdgcn_sched_barrier(0); p[9] = ld_sc1(rs, opm + 1024, sb); };
            auto comb = [&](bool first) {
                #pragma unroll
                for (int f = 0; f < 8; ++f) {
                    ca = smf(p[f], __builtin_shufflevector(cf[4 * f], cf[4 * f + 1], 0, 1, 2, 3, 4, 5, 6, 7), ca, sidx);
                    cb = smf(p[f], __builtin_shufflevector(cf[4 * f + 2], cf[4 * f + 3], 0, 1, 2, 3, 4, 5, 6, 7), cb, sidx);
                    if (first && f == UBAR) { asm volatile("" : "+v"(ca), "+v"(cb)); bar();
                        #pragma unroll
                        for (int i = 0; i < 8; ++i) u[i] = lrd(L + L_CU + lane * 16 + i * 1024); }
                    __builtin_amdgcn_sched_barrier(0); }
                #pragma unroll
                for (int f = 0; f < 2; ++f) {
                    ca = smf(p[8 + f], __builtin_shufflevector(u[4 * f], u[4 * f + 1], 0, 1, 2, 3, 4, 5, 6, 7), ca, sidx);
                    cb = smf(p[8 + f], __builtin_shufflevector(u[4 * f + 2], u[4 * f + 3], 0, 1, 2, 3, 4, 5, 6, 7), cb, sidx);
                    __builtin_amdgcn_sched_barrier(0); }
                const float t0 = hi ? cb[0] + cb[2] : ca[0] + ca[2], t1 = hi ? cb[1] + cb[3] : ca[1] + ca[3];
                v = fold32(t0, t1);
                res = pk2(v, __int_as_float(__builtin_amdgcn_update_dpp(0, __float_as_int(v), 0xB1, 0xf, 0xf, true)));
                asm volatile("" : "+v"(res)); };
            ca = (f32x4){0, 0, 0, 0}; cb = ca;
            asm volatile("" : "+v"(ca), "+v"(cb));
            bar();
            ST(5);
            ldp();
            const unsigned G = __builtin_amdgcn_readfirstlane(*gbox);
            __builtin_amdgcn_sched_barrier(0);
            comb(true);
            if (__builtin_expect(__builtin_amdgcn_ballot_w64(v != v) != 0ull, 0)) {
                for (unsigned sp = 0;; ++sp) {
                    unsigned m = 0, mu = 0;
                    #pragma unroll
                    for (int i = 0; i < 10; ++i) m = mx4(p[i], m);
                    #pragma unroll
                    for (int i = 0; i < 8; ++i) mu = mx4(u[i], mu);
                    if (all_ok(m > mu ? m : mu) || dead) break;
                    if (sp == 0) nfail += all_ok(mu) ? 1 : 0x10000;
                    if (sp > SPIN_MAX) dead = true;
                    asm volatile("" ::: "memory");
                    if (mu == 0xFFFFFFFFu) ldu();
                    if (m == 0xFFFFFFFFu) ldp(); }
                ca = (f32x4){0, 0, 0, 0}; cb = ca;
                comb(false); }
            ST(0);
            st32(res, rs, vmem, sb);
            ST(3);
            const unsigned now = rt32();
            LG(0, now); LG(1, G);
            unsigned Sn = G + DLT;
            const int lt = (int)(now - Sn);
            if (lt > a.tp[5]) { ++nlate; Sn += max((lt + a.tp[6] + 3) & ~3, a.tp[12]); if (t == 0) Sn &= ~3u; }
            st32(nosent(Sn), rs, vstp, sb);
            if (t == NSEG - 2) break;
            bar();
            ldcf(t + 1);
            ST(4);
            if (t == 0) TL(2);
        }
      }
    } else if (role == R_QK || role == R_VO) {
        const int h = id; const bool vo = role == R_VO, st2 = vo && wave >= 4;
        u32x4 W[40];
        bool allh = false;
        int seen = -1;
        auto waith = [&](int s) {
            if (!allh) for (unsigned sp = 0;; ++sp) { asm volatile("" ::: "memory");
                const u32x4 f = ld_sc1(rs, WS_FH1 + lane * 16, 0);
                const unsigned long long bad = __builtin_amdgcn_ballot_w64(f.x != tag.x || f.y != tag.y || f.z != tag.x || f.w != tag.y);
                allh = bad == 0ull;
                if (!((bad >> s) & 1ull) || dead) break;
                if (sp > SPIN_MAX) { dead = true; break; }
                dly(2); }
            asm volatile("" ::: "memory"); };
        LAS unsigned* cnt = (LAS unsigned*)(L + 122880);
        auto img = [&](int s) { return L + ((s & 1) ? 0 : 81920); };
        auto dmah = [&](int s, int i, unsigned v) { __builtin_amdgcn_raw_ptr_buffer_load_lds(rs, (LAS void*)(img(s) + i * 1024), 16, v, WS_H1 + s * 40960 + i * 1024, 0, HAUX); };
        auto start = [&](auto kind) {
            u32x4 fh; u32x2 pv0;
            bool got;
            auto ldh = [&](unsigned v) {
                #pragma unroll
                for (int i = 0; i < 5; ++i) dmah(0, 5 * wave + i, v); };
            auto hook = [&](int r) {
                if (r == FHR) fh = ld_sc1(rs, WS_FH1 + lane * 16, 0);
                if (r == 6) { const unsigned long long bad = __builtin_amdgcn_ballot_w64(fh.x != tag.x || fh.y != tag.y || fh.z != tag.x || fh.w != tag.y);
                    allh = bad == 0ull; got = (bad & 1ull) == 0ull;
                    pv0 = ld_pol(rs, lane); ldh(got ? lane * 16 : OOB); } };
            if (kind.value == 1) load_w_32<false>(W, mkrs(vo ? a.Wv : wave < 4 ? a.Wq : a.Wk, D * HE * 2), h * 256, wave, lane, L, hook);
            if (kind.value == 2) load_w_32<true>(W, mkrs(a.Wo, D * HE * 2), h * E * D * 2, wave, lane, L, hook);
            TL(1);
            if (!got) { waith(0); ldh(lane * 16); }
            if (wave < 2) cnt[tid] = 0;
            pol = get_pol(rs, lane, dead, tag, pv0);
            __builtin_amdgcn_s_waitcnt(0x0F70);
            bar(); };
        if (!st2) {
            const unsigned vdst = WS_KQ + h * CH + (wave & 3) * 2048 + lane * 16;
            const bool ld = vo || wave >= 4;
            const unsigned vpr = wave != 1 ? OOB : vo ? WS_POUT + (lane < 40 ? lane : 0) * FS : WS_PP + h * FS;
            start(Ic<1>());
            if (!ld) __builtin_amdgcn_s_setprio(1);
            for (int s = 0; s < NSEG; ++s) {
                if (s == 1) { ST_RESET; }
                lptr b = img(s) + lane * 16;
                u32x4 f[FV];
                #pragma unroll
                for (int m = 0; m < FV; ++m) f[m] = lrd(b + m * 1024);
                SB();
                if (ld && (s > 0 || !vo)) { if (s + 1 < NSEG) waith(s + 1);
                    const unsigned vh = s + 1 < NSEG ? lane * 16 : OOB;
                    #pragma unroll
                    for (int i = 0; i < 10; ++i) dmah(s + 1, 10 * (wave & 3) + i, vh);
                    if (!vo) { asm volatile("s_waitcnt vmcnt(10)" ::: "memory");
                        unsigned old = lds_inc(cnt + lane); asm volatile("s_waitcnt lgkmcnt(0)" : "+v"(old));
                        st32(0u, rs, ((old & 3u) | lane * 4u) == 3u ? 0 : OOB, WS_PQK + h * FS + (s - 1) * FSEG);
                        if (s == 1) TL(2); } }
                const unsigned pr = ld_fl(rs, s < RC - 1 ? OOB : vpr - (RC - 1) * FSEG, s);
                SB();
                ST(0);
                f32x16 acc;
                #pragma unroll
                for (int i = 0; i < 16; ++i) acc[i] = 0;
                #pragma unroll
                for (int ks = 0; ks < 40; ++ks) { acc = mf32(W[ks], f[ks % FV], acc); if (ks + FV < 40) f[ks % FV] = lrd(b + (ks + FV) * 1024); SB(); }
                const bool rok = fl_ok(pr);
                const u32x4 o0 = pk8(acc, 0), o1 = pk8(acc, 1);
                if (vo) { lptr vw = L + 40960 + (s & 1) * 8192 + wave * 2048 + lane * 16; lwr(vw, o0); lwr(vw + 1024, o1); }
                else { const unsigned so = RO(s, wave < 4); st128(o0, rs, vdst, so); st128(o1, rs, vdst + 1024, so); }
                if (!rok) wait_ring(rs, vpr, s - (RC - 1), seen, dead);
                ST(1);
                if (wave < 4 || s == NSEG - 1) drain(); else asm volatile("s_waitcnt vmcnt(2)" ::: "memory");
                ST(3);
                bar();
                ST(4);
            }
            if (!vo && tid == 0) st32(0u, rs, 0, WS_PQK + h * FS + (NSEG - 1) * FSEG);
        } else {
            const int r32 = lane & 31, hf = lane >> 5;
            const int prow = (r32 & 19) | ((r32 & 4) << 1) | ((r32 & 8) >> 1);
            lptr asrc = L + 40960 + hf * 512 + prow * 16;
            const unsigned vdst = WS_CF + ((10 * (wave & 1) + (r32 >> 4)) * 4 + (h >> 3)) * CH + (h & 7) * 1024 + (hf * 16 + (r32 & 15)) * 16;
            start(Ic<2>());
            __builtin_amdgcn_s_setprio(1);
            waith(1);
            #pragma unroll
            for (int i = 0; i < 10; ++i) dmah(1, 10 * (wave - 4) + i, lane * 16);
            drain();
            for (int s = 0; s < NSEG; ++s) {
                ST(3);
                bar();
                if (s == 1) { ST_RESET; }
                ST(4);
                u32x4 af[8];
                #pragma unroll
                for (int ks = 0; ks < 8; ++ks) af[ks] = lrd(asrc + (s & 1) * 8192 + ks * 1024);
                SB();
                const unsigned so = CO(s, wave >= 6);
                f32x16 acc[2];
                auto fin = [&](int tt, int p) { const f32x16& c = acc[tt & 1];
                    st128((u32x4){pk2(c[8 * p], c[8 * p + 1]), pk2(c[8 * p + 2], c[8 * p + 3]), pk2(c[8 * p + 4], c[8 * p + 5]), pk2(c[8 * p + 6], c[8 * p + 7])}, rs, vdst + p * 512, so + tt * (8 * CH)); };
                #pragma unroll
                for (int tt = 0; tt < 5; ++tt) {
                    #pragma unroll
                    for (int i = 0; i < 16; ++i) acc[tt & 1][i] = 0;
                    #pragma unroll
                    for (int ks = 0; ks < 8; ++ks) { acc[tt & 1] = mf32(af[ks], W[8 * tt + ks], acc[tt & 1]);
                        asm volatile("" : "+v"(acc[tt & 1]));
                        if (tt > 0 && ks == 2) fin(tt - 1, 0);
                        if (tt > 0 && ks == 4) fin(tt - 1, 1);
                        SB(); }
                }
                fin(4, 0); fin(4, 1);
                ST(1);
                drain();
                unsigned old = lds_inc(cnt + lane); asm volatile("s_waitcnt lgkmcnt(0)" : "+v"(old));
                st32(0u, rs, ((old & 3u) | lane * 4u) == 3u ? 0 : OOB, WS_PVO + h * FS + s * FSEG);
                if (s == 0) TL(2);
            }
        }
        if (id < 27) {
            __builtin_amdgcn_s_setprio(0);
            int lane; asm volatile("v_mbcnt_lo_u32_b32 %0, -1, 0\n\tv_mbcnt_hi_u32_b32 %0, -1, %0" : "=v"(lane)); lane &= 63; const int tid = wave * 64 + lane;
            const int j = tid < 480 ? tid : 479;
            const u32x4 mv = ld_sc1(rs, WS_MOD + j * 32, 0), mw = ld_sc1(rs, WS_MOD + j * 32 + 16, 0);
            lwr(L + L_MOD + j * 16, (u32x4){mv.x, mv.y, mw.x, mw.y});
            role = R_F1; fs0 = a.tp[19] + (vo ? 1 : 0); fst = 2; fse = NSEG; }
    } else if (role == R_OUT) {
        const int c = id;
        const unsigned lo = (lane >> 5) * 1024 + (lane & 31) * 16;
        const unsigned vcf = WS_CF + ((c % 20) * 4 + (wave >> 1)) * CH + (wave & 1) * 4096 + lo;
        const unsigned vax = WS_AX + wave * 8192 + (lane >> 5) * 2048 + (lane & 31) * 16, vam = WS_AM + wave * 2048 + lane * 16;
        const unsigned vu = WS_SLOT + SL_U + c * CH + wave * 1024 + lane * 16;
        lptr part = L + wave * 2560 + (lane & 15) * 80 + (lane >> 4) * 16;
        const int tok = 4 * wave + (lane >> 4), col = lane & 15;
        lptr rsrc = L + tok * 80 + col * 4;
        LAS unsigned* cnt = (LAS unsigned*)(L + 49152);
        const unsigned vx = tok * (D * 2) + c * 32 + col * 2, vo = WS_X1 + c * 1024 + wave * 128 + (((col >> 3) ^ (wave >> 1)) & 1) * 64 + (lane >> 4) * 16 + (col & 7) * 2;
        const unsigned vl = col ? OOB : WS_LS + (tok >> 4) * 5120 + c * 128 + (tok & 15) * 8;
        const unsigned vpp = WS_PP + (4 * wave + (lane & 3)) * FS, vpv = WS_PVO + (4 * wave + (lane & 3)) * FS;
        const rsrc_t rx = mkrs(a.x, S * D * 2);
        const float ga = b2f(*(LAS u16*)(L + L_MOD + 2560 + c * 32 + col * 2));
        int seenv = -1, seenp = -1;
        u32x4 cf[4];
        auto ldcf = [&](int s) {
            wait_fl(rs, vpv, s, seenv, dead, 4);
            const unsigned so = CO(s, c >= 20);
            #pragma unroll
            for (int i = 0; i < 4; ++i) cf[i] = ld_sc1(rs, vcf + (i >> 1) * 2048 + (i & 1) * 512, so); };
        auto tick = [&]() { unsigned old = 0; if (lane == 0) old = __hip_atomic_fetch_add(cnt, 1u, __ATOMIC_RELAXED, __HIP_MEMORY_SCOPE_WORKGROUP); return old; };
        auto done = [&](int s) { if (lane == 0 && (__hip_atomic_fetch_add(cnt, 1u, __ATOMIC_RELAXED, __HIP_MEMORY_SCOPE_WORKGROUP) & 7u) == 7u) st32(0u, rs, WS_POUT + id * FS, s * FSEG); };
        if (tid == 0) *cnt = 0;
        ldcf(0);
        bool pub = false;
        unsigned hold = 0;
        for (int s = 0; s < NSEG; ++s) {
            if (s == 2) { ST_RESET; }
            wait_fl(rs, vpp, s, seenp, dead, s < 63 ? a.tp[17] : 2);
            ST(0);
            DG(rt32());
            u32x4 ax[6], am0, am1, u;
            unsigned xr = __builtin_amdgcn_raw_buffer_load_b16(rx, vx, s * (32 * D * 2), 0);
            SB();
            #pragma unroll
            for (int j = 0; j < 2; ++j) { ax[3 * j] = ld_sc1(rs, vax, RI(s) * 65536 + j * 4096); ax[3 * j + 1] = ld_sc1(rs, vax + 1024, RI(s) * 65536 + j * 4096); ax[3 * j + 2] = ld_sc1(rs, vax + 1536, RI(s) * 65536 + j * 4096); }
            u = ld_sc1(rs, vu, SO(s));
            am0 = ld_sc1(rs, vam, RI(s) * 16384); am1 = ld_sc1(rs, vam + 1024, RI(s) * 16384);
            SB();
            const unsigned pw = ld_fl(rs, s + 2 < NSEG ? vpv : OOB, s + 2);
            SB();
            unsigned old = 0;
            if (pub) { asm volatile("s_waitcnt vmcnt(11)" :: "v"(hold) : "memory"); old = tick(); }
            st32(0u, rs, (old & 7u) == 7u ? WS_POUT + id * FS : OOB, (s - 1) * FSEG);
            SB();
            ST(1);
            f32x4 d0 = {0, 0, 0, 0}, d1 = d0;
            #pragma unroll
            for (int j = 0; j < 2; ++j) { d0 = mf16(cf[2 * j], ax[3 * j], d0); d1 = mf16(cf[2 * j], ax[3 * j + 1], d1); d1 = mf16(cf[2 * j + 1], ax[3 * j + 2], d1); }
            if (__builtin_expect(!all_ok(mx4(u, 0)), 0))
                for (unsigned sp = 0;; ++sp) { asm volatile("" ::: "memory");
                    u = ld_sc1(rs, vu, SO(s));
                    if (all_ok(mx4(u, 0)) || dead) break;
                    if (sp > SPIN_MAX) { dead = true; break; }
                    dly(2); }
            d0 = mf16(u, am0, d0); d1 = mf16(u, am1, d1);
            lptr pp = part + (s & 1) * 20480;
            *(LAS f32x4*)pp = d0; *(LAS f32x4*)(pp + 1280) = d1;
            if (fl_ok(pw)) seenv = s + 2;
            unsigned pv; unsigned long long mk;
            asm volatile("buffer_load_dword %0, %2, %3, %4 offen sc1" : "=&v"(pv), "+v"(xr) : "v"(s + 1 < NSEG ? vpp : OOB), "s"(rs), "s"((s + 1) * FSEG) : "memory");
            if (s + 1 < NSEG) ldcf(s + 1);
            ST(3);
            bar();
            ST(4);
            float q[8];
            #pragma unroll
            for (int w = 0; w < 8; ++w) q[w] = *(LAS float*)(rsrc + (s & 1) * 20480 + w * 2560);
            const unsigned o = pk2(blo(xr) + rnd(ga * rnd(((q[0] + q[1]) + (q[2] + q[3])) + ((q[4] + q[5]) + (q[6] + q[7])))), 0.f);
            __builtin_amdgcn_raw_buffer_store_b16((unsigned short)o, rs, vo, RI(s) * (32 * D * 2), 16);
            const float of = blo(o);
            st64((u32x2){__float_as_uint(sum16(of)), __float_as_uint(sum16(of * of))}, rs, vl, RI(s) * 10240);
            if (s + 1 == NSEG) drain();
            unsigned p2;
            asm volatile("buffer_load_dword %1, %3, %4, %5 offen sc1\n\ts_waitcnt vmcnt(7)\n\tv_cmp_ne_u32 %0, 0, %2" : "=s"(mk), "=&v"(p2) : "v"(pv), "v"(s + 1 < NSEG ? vpp : OOB), "s"(rs), "s"((s + 1) * FSEG) : "memory");
            pub = mk == 0ull;
            if (!pub) { asm volatile("s_waitcnt vmcnt(0)\n\tv_cmp_ne_u32 %0, 0, %1" : "=s"(mk) : "v"(p2) : "memory"); done(s); }
            if (mk == 0ull) seenp = s + 1;
            hold = p2;
            LS2(dg_, te_);
            ST(5);
            if (s == 0) TL(2);
        }
        drain();
        done(NSEG - 1);
    } else if (role == R_F2) {
        const int cg = id >> 1, mt = id & 1;
        const int ks0 = wave < 6 ? 7 * wave : 42 + 6 * (wave - 6);
        u32x4 W[28];
        gate(tstart + a.tp[23]);
        {   const rsrc_t rw = mkrs(a.wd, F * D * 2);
            const unsigned vw = (32 * ks0 + 8 * (lane >> 4) + ((lane >> 2) & 3)) * (D * 2) + cg * 128 + (lane & 3) * 16;
            u32x4 v[28];
            #pragma unroll
            for (int i = 0; i < 28; ++i) v[i] = ld_pl(rw, vw, (i >= 24 && wave >= 6) ? 0 : (32 * (i >> 2) + 4 * (i & 1)) * (D * 2) + ((i >> 1) & 1) * 64);
            __builtin_amdgcn_sched_barrier(0);
            lptr wp = stg + lane * 16, rp = stg + (lane >> 2) * 64 + (lane & 3) * 8;
            #pragma unroll
            for (int i = 0; i < 28; ++i) { const int pi = i & 3;
                if (i >= 24 && wave >= 6) v[i] = (u32x4){0, 0, 0, 0};
                lwr(wp + pi * 1024, v[i]);
                const u32x2 r0 = trd(rp + pi * 1024), r1 = trd(rp + pi * 1024 + 32);
                const int i0 = (i & ~3) + (pi & 2);
                W[i0][2 * (pi & 1)] = r0.x; W[i0][2 * (pi & 1) + 1] = r0.y; W[i0 + 1][2 * (pi & 1)] = r1.x; W[i0 + 1][2 * (pi & 1) + 1] = r1.y; } }
        TL(2);
        bar();
        const int ab = mt * 54 + ks0;
        const unsigned vact = WS_ACT + lane * 16;
        const int pf = ((ks0 >> 1) + (lane & 3)) < 27 ? (ks0 >> 1) + (lane & 3) : 26;
        lptr part = L + wave * 4352 + (lane & 15) * 272 + (lane >> 4) * 16;
        lptr rsrc = L + (tid >> 5) * 272 + (tid & 31) * 8;
        const unsigned vx = (mt * 16 + (tid >> 5)) * (D * 2) + cg * 128 + (tid & 31) * 4;
        const unsigned vx1 = WS_X1 + (cg * 4 + ((tid & 31) >> 3)) * 1024 + (mt * 4 + (tid >> 7)) * 128 + (((tid >> 2) ^ (tid >> 8)) & 1) * 64 + ((tid >> 5) & 3) * 16 + (tid & 3) * 4;
        const unsigned gb = *(LAS unsigned*)(L + L_MOD + 6400 + cg * 128 + (tid & 31) * 4);
        const rsrc_t ro = mkrs(a.out, S * D * 2);
        int seen = -1;
        auto vfl = [&](int s) { return s + 2 < NSEG ? WS_PF1 + pf * FS : s < NSEG ? WS_POUT + (4 * cg + (lane & 3)) * FS : OOB; };
        unsigned xr = 0, ts_ = 0;
        auto epi = [&](int s) {
            bar();
            st32(0u, rs, (tid == 0 && s > 0) ? WS_PF2 + id * FS : OOB, (s - 1) * FSEG);
            f32x2 r = {0, 0};
            #pragma unroll
            for (int w = 0; w < 8; ++w) r += *(LAS f32x2*)(rsrc + (s & 1) * 34816 + w * 4352);
            __builtin_amdgcn_raw_buffer_store_b32(pk2(blo(xr) + rnd(blo(gb) * rnd(r[0])), bhi(xr) + rnd(bhi(gb) * rnd(r[1]))), ro, vx, s * (32 * D * 2), 0);
            LS2(ts_, rt32());
            SB(); };
        bool pre = false;
        for (int s = 0; s < NSEG; ++s) {
            if (!pre) {
                if (s > 0) { const unsigned pv = ld_fl(rs, vfl(s), s); epi(s - 1); if (fl_ok(pv)) seen = s; }
                wait_fl(rs, vfl(s), s, seen, dead, s < 60 ? a.tp[18] : 2); }
            DG(rt32());
            u32x4 af[7];
            auto lda = [&]() {
                #pragma unroll
                for (int i = 0; i < 7; ++i) { const int b = ab + ((i == 6 && wave >= 6) ? 0 : i); af[i] = ld_sc1(rs, vact, AO(s) + (b >> 3) * CH + (b & 7) * 1024); } };
            lda();
            const unsigned xn = __builtin_amdgcn_raw_buffer_load_b32(rs, vx1, RI(s) * (32 * D * 2), 16), pe = ld_fl(rs, vfl(s + 1), s + 1);
            SB();
            if (pre) epi(s - 1);
            f32x4 d[4];
            for (unsigned sp = 0;; ++sp) {
                #pragma unroll
                for (int j = 0; j < 4; ++j) d[j] = (f32x4){0, 0, 0, 0};
                unsigned m = 0;
                #pragma unroll
                for (int i = 0; i < 7; ++i) {
                    #pragma unroll
                    for (int j = 0; j < 4; ++j) d[j] = mf16(W[4 * i + j], af[i], d[j]);
                    m = mx4(af[i], m);
                    SB(); }
                if (all_ok(m) || dead) break;
                if (sp > SPIN_MAX) { dead = true; break; }
                asm volatile("" ::: "memory");
                if (m == 0xFFFFFFFFu) lda(); }
            #pragma unroll
            for (int j = 0; j < 4; ++j) *(LAS f32x4*)(part + (s & 1) * 34816 + j * 64) = d[j];
            SB();
            xr = xn; ts_ = dg_;
            pre = fl_ok(pe);
        }
        epi(NSEG - 1);
        if (id == 0 && tid == 0) st64(tag, rs, WS_EP, 0);
    }
    if (role == R_F1) {
        int lane; asm volatile("v_mbcnt_lo_u32_b32 %0, -1, 0\n\tv_mbcnt_hi_u32_b32 %0, -1, %0" : "=v"(lane)); lane &= 63; const int tid = wave * 64 + lane;
        u32x4 W[4][10];
        f32x4 cs[2], bs[2];
        const int kg = lane >> 4, t16 = lane & 15, kh = (wave >> 1) & 1, cw = 2 * (wave & 1) + kh, th = wave >> 2;
        gate(tstart + a.tp[23]);
        #pragma unroll
        for (int mat = 0; mat < 2; ++mat) {
            const rsrc_t rg = mkrs(mat ? a.wu : a.wg, D * F * 2);
            const unsigned vw = (wave * 80 + ((lane >> 1) & 7)) * (F * 2) + id * 128 + (lane >> 4) * 32 + (lane & 1) * 16;
            bar();
            #pragma unroll 1
            for (int i = 0; i < 10; ++i) { __builtin_amdgcn_raw_ptr_buffer_load_lds(rg, (LAS void*)(L + (wave * 10 + i) * 1024), 16, vw, i * 8 * F * 2, 0, 0); if (fs0) dly(a.tp[21]); }
            drain();  bar();
            lptr rp = L + kg * 1024 + cw * 256 + t16 * 8;
            const u32x4 ones = {0x3F803F80u, 0x3F803F80u, 0x3F803F80u, 0x3F803F80u};
            cs[mat] = (f32x4){0, 0, 0, 0}; bs[mat] = cs[mat];
            #pragma unroll 4
            for (int ks = 0; ks < 20; ++ks) { const u32x2 a0 = trd(rp + ks * 4096), a1 = trd(rp + ks * 4096 + 128);
                bs[mat] = mf16((u32x4){a0.x, a0.y, a1.x, a1.y}, lrd(L + L_MOD + 3840 + (32 * ks + 8 * kg) * 2), bs[mat]); }
            bar();
            #pragma unroll 2
            for (int i = 0; i < 10; ++i) { lptr q = L + (wave * 10 + i) * 1024 + lane * 16;
                u32x4 w = lrd(q);
                const float sc = rnd(1.f + b2f(*(LAS u16*)(L + L_MOD + 5120 + (wave * 80 + i * 8 + ((lane >> 1) & 7)) * 2)));
                #pragma unroll
                for (int j = 0; j < 4; ++j) w[j] = pk2(blo(w[j]) * sc, bhi(w[j]) * sc);
                lwr(q, w); }
            bar();
            #pragma unroll 4
            for (int ks = 0; ks < 20; ++ks) { const u32x2 a0 = trd(rp + ks * 4096), a1 = trd(rp + ks * 4096 + 128);
                cs[mat] = mf16((u32x4){a0.x, a0.y, a1.x, a1.y}, ones, cs[mat]); }
            #pragma unroll
            for (int j = 0; j < 2; ++j) { lptr r2 = L + kh * 40960 + kg * 1024 + (cw ^ j) * 256 + t16 * 8;
                #pragma unroll
                for (int i = 0; i < 10; ++i) { const u32x2 a0 = trd(r2 + i * 4096), a1 = trd(r2 + i * 4096 + 128); W[2 * j + mat][i] = (u32x4){a0.x, a0.y, a1.x, a1.y}; } }
             }
        if (fs0) { TL(6); } else { TL(2); }
        lptr bb = L + kh * 20480 + (kg >> 1) * 1024 + (th * 4 + (t16 >> 2)) * 128 + ((kg ^ (t16 >> 3)) & 1) * 64 + (t16 & 3) * 16;
        lptr stat = L + 81920 + wave * 1280 + lane * 8;
        lptr xw = L + 102400 + wave * 2048 + lane * 16, xr = L + 102400 + (wave ^ 2) * 2048 + lane * 16;
        lptr sw = L + 118784 + wave * 128 + t16 * 8, sr = L + 118784 + th * 512 + t16 * 8;
        volatile LAS int* rdy = (volatile LAS int*)(L + 119808);
        LAS unsigned* cnt = (LAS unsigned*)(L + 119812);
        const int ab = th * 54 + 2 * id + (cw >> 1);
        const unsigned vact = WS_ACT + (ab >> 3) * CH + (ab & 7) * 1024 + ((2 * (cw & 1) + (kg >> 1)) * 16 + t16) * 16 + (kg & 1) * 8;
        int seen = -1;
        const unsigned vpo = wave ? OOB : WS_POUT + (lane < 40 ? lane : 0) * FS;
        const unsigned vx1 = WS_X1 + wave * 5120 + lane * 16, vls = WS_LS + tid * 16;
        auto ldp = [&](int s, int k, int i) {
            if (i < 5) dma1(rs, L + k * 40960 + wave * 5120 + i * 1024, vx1, RI(s) * (32 * D * 2) + i * 1024);
            else if (i == 5) dma1(rs, L + 81920 + k * 10240 + wave * 1024, vls, RI(s) * 10240);
            else if (wave < 2) dma1(rs, L + 81920 + k * 10240 + 8192 + wave * 1024, vls, RI(s) * 10240 + 8192); };
        auto ldh = [&](int s, int k) {
            #pragma unroll
            for (int i = 0; i < 7; ++i) ldp(s, k, i); };
        auto ready = [&](int s) { return __builtin_amdgcn_readfirstlane(*rdy) >= s; };
        if (fs0) st64((u32x2){~0u, ~0u}, rs, vact, AO(NSEG - 2 + (fs0 & 1)));
        if (fs0) { wait_fl(rs, vpo, fs0 - 1, seen, dead, 24); dly(a.tp[20]); }
        wait_fl(rs, vpo, fs0, seen, dead, fs0 ? a.tp[22] : 24);
        if (tid == 0) { *rdy = 0; *cnt = 0; }
        bar();
        ldh(fs0, 0);
        drain();
        bool pend = false;
        auto done = [&](int s) { if (lane == 0) { unsigned old; asm volatile("ds_add_rtn_u32 %0, %1, %2\n\ts_waitcnt lgkmcnt(0)" : "=v"(old) : "v"(cnt), "v"(1u) : "memory");
            st32(0u, rs, (old & 7u) == 7u ? WS_PF1 + id * FS : OOB, s * FSEG); } };
        for (int s = fs0, k = 0; s < fse; s += fst, k ^= 1) {
            const int n1 = s + fst, n2 = n1 + fst;
            if (s == 2) { ST_RESET; }
            unsigned pv, pw;
            if (wave == 0) asm volatile("buffer_load_dword %0, %2, %4, %5 offen sc1\n\tbuffer_load_dword %1, %3, %4, %6 offen sc1" : "=&v"(pv), "=&v"(pw)
                : "v"((n1 < fse && seen < n1) ? vpo : OOB), "v"((n2 < fse && pend) ? vpo : OOB), "s"(rs), "s"((n1) * FSEG), "s"((n2) * FSEG) : "memory");
            ST(0);
            bar();
            ST(1);
            DG(rt32());
            const int bo = k * 40960;
            f32x2 t2, t3, t4;
            u32x4 f[4];
            const int r0 = *rdy;
            #pragma unroll
            for (int m = 0; m < 4; ++m) f[m] = lrd(bb + bo + m * 2048);
            t2 = *(LAS f32x2*)(stat + k * 10240); t3 = *(LAS f32x2*)(stat + k * 10240 + 512); t4 = *(LAS f32x2*)(stat + k * 10240 + 1024);
            SB();
            const bool early = __builtin_amdgcn_readfirstlane(r0) >= n1 && n1 < fse;
            f32x4 d[4];
            #pragma unroll
            for (int j = 0; j < 4; ++j) d[j] = (f32x4){0, 0, 0, 0};
            #pragma unroll
            for (int i = 0; i < 10; ++i) {
                #pragma unroll
                for (int j = 3; j >= 0; --j) d[j] = mf16(W[j][i], f[i & 3], d[j]);
                asm volatile("" : "+v"(d[0]), "+v"(d[1]), "+v"(d[2]), "+v"(d[3]));
                if (i + 4 < 10) f[i & 3] = lrd(bb + bo + (i + 4) * 2048);
                if (i == 5 && pend) { if (early) asm volatile("s_waitcnt vmcnt(5)" ::: "memory"); else drain(); done(s - fst); }
                if (i < 7 && early) ldp(n1, k ^ 1, i);
                if (i == 4) { t2 += t3; if (lane < 32) t2 += t4; }
                if (i == 5) { t2[0] = x16sum(t2[0]); t2[1] = x16sum(t2[1]); }
                if (i == 6) { t2[0] = x32sum(t2[0]); t2[1] = x32sum(t2[1]); *(LAS f32x2*)sw = t2; }
                SB(); }
            *(LAS f32x4*)(xw + 1024) = d[3]; *(LAS f32x4*)xw = d[2];
            if (wave == 0) {
                unsigned long long mv, mw;
                if (!pend) drain();
                asm volatile("v_cmp_ne_u32 %0, 0, %2\n\tv_cmp_ne_u32 %1, 0, %3" : "=&s"(mv), "=&s"(mw) : "v"(pv), "v"(pw) : "memory");
                if (pend && n2 < fse && mw == 0ull) seen = n2; else if (mv == 0ull) seen = max(seen, n1);
                *rdy = seen; }
            bar();
            const int rv = *rdy;
            t2 = *(LAS f32x2*)sr; t3 = *(LAS f32x2*)(sr + 128); t4 = *(LAS f32x2*)(sr + 256);
            const f32x2 t5 = *(LAS f32x2*)(sr + 384);
            d[2] = *(LAS f32x4*)xr; d[3] = *(LAS f32x4*)(xr + 1024);
            SB();
            bool got = n1 >= fse || early;
            if (!got && __builtin_amdgcn_readfirstlane(rv) >= n1) { ldh(n1, k ^ 1); got = true; }
            ST(2);
            t2 = ((t2 + t3) + (t4 + t5)) * (1.f / D);
            const float rstd = __builtin_amdgcn_rcpf(rnd(__builtin_amdgcn_sqrtf(rnd(rnd(__builtin_fmaxf(t2[1] - t2[0] * t2[0], 0.f)) + 1e-5f)))), mean = rnd(t2[0]);
            const f32x4 g = (d[0] + d[2] - cs[0] * mean) * rstd + bs[0], u = (d[1] + d[3] - cs[1] * mean) * rstd + bs[1];
            float act[4];
            #pragma unroll
            for (int i = 0; i < 4; ++i) act[i] = g[i] * u[i] * __builtin_amdgcn_rcpf(1.f + __expf(-g[i]));
            LS2(dg_, (rt32() & ~1u) | (got ? 1u : 0u));
            st64((u32x2){nosent(pk2(act[0], act[1])), nosent(pk2(act[2], act[3]))}, rs, vact, AO(s));
            ST(4);
            pend = got && n1 < fse;
            if (pend) asm volatile("s_waitcnt vmcnt(1)" ::: "memory");
            else {
                const bool ok = fl_ok(ld_fl(rs, got ? OOB : vpo, n1));
                drain();
                if (wave == 0 && ok) { seen = n1; *rdy = n1; }
                done(s);
                if (!got) {
                    if (wave == 0) { wait_fl(rs, vpo, n1, seen, dead, fs0 ? a.tp[22] : a.tp[16]); *rdy = n1; }
                    while (!ready(n1)) dly(1);
                    ldh(n1, k ^ 1); drain(); } }
            ST(5);
            if (s == 0) TL(3);
        }
    }
    TL(7);
    ST_OUT;
}

#define WOFF 0
extern "C" void kernel_launch(void* const* d_in, const int* in_sizes, int n_in, void* d_out, int out_size, void* d_ws, size_t ws_size, hipStream_t stream) {
    unsigned char* wsb = (unsigned char*)(((size_t)d_ws + WOFF + 0x1FFFFF) & ~(size_t)0x1FFFFF);
    Args a{(const u16*)d_in[0], (const u16*)d_in[1], (const u16*)d_in[2], (const u16*)d_in[3], (const u16*)d_in[4], (const u16*)d_in[5],
           (const u16*)d_in[8], (const u16*)d_in[9], (const u16*)d_in[10], (const u16*)d_in[11], (const u16*)d_in[12], (u16*)d_out, wsb, {184, 0, 112, 0, 0, -4, 72, 3, 1, 8, 0, 6, 112, 3, 0, 0, 8, 4, 4, 60, 16, 1, 2, 1400}};
    hipFuncSetAttribute((const void*)mega, hipFuncAttributeMaxDynamicSharedMemorySize, L_TOT);
    mega<<<256, 512, L_TOT, stream>>>(a);
}
```

```cpp
#include <hip/hip_runtime.h>
#include <hip/hip_bf16.h>

typedef unsigned short u16;
typedef __bf16 bf16x8 __attribute__((ext_vector_type(8)));
typedef __bf16 bf16x16 __attribute__((ext_vector_type(16)));
typedef __bf16 bf16x2 __attribute__((ext_vector_type(2)));
typedef short s16x4 __attribute__((ext_vector_type(4)));
typedef float f32x2 __attribute__((ext_vector_type(2)));
typedef float f32x4 __attribute__((ext_vector_type(4)));
typedef float f32x16 __attribute__((ext_vector_type(16)));
typedef unsigned u32x2 __attribute__((ext_vector_type(2)));
typedef unsigned u32x4 __attribute__((ext_vector_type(4)));
typedef unsigned u32x8 __attribute__((ext_vector_type(8)));
typedef __amdgpu_buffer_rsrc_t rsrc_t;
#define LAS __attribute__((address_space(3)))
typedef LAS unsigned char* lptr;

constexpr int S = 2048, D = 640, H = 32, E = 128, HE = 4096, NSEG = 64, F = 1728;
constexpr unsigned WS_EP = 0;
constexpr unsigned WS_FH1 = 1024;
#define FS 32
constexpr unsigned WS_FLG = 262144, FSEG = 192 * FS;
constexpr unsigned WS_PVO = WS_FLG, WS_PQK = WS_FLG + 32 * FS, WS_PP = WS_FLG + 64 * FS, WS_POUT = WS_FLG + 96 * FS, WS_PF2 = WS_FLG + 136 * FS, WS_PF1 = WS_FLG + 160 * FS;
#define RC 16
#define RS 16
constexpr int NPG = 2 * RC > RS ? 2 * RC : RS;
constexpr unsigned WS_POL = 2048;
constexpr unsigned WS_MOD = 4096;
constexpr unsigned WS_SLOT = 2u << 20, SLOT = 2u << 20, CH = 16384;
constexpr unsigned SL_PX = 0, SL_PM = 2 * CH, SL_STP = 2 * CH + 4096, SL_MEM = 3 * CH, SL_U = 5 * CH, SL_END = 45 * CH, SL_CAL = 126 * CH;
#define CFN 0
constexpr unsigned WS_CF = WS_SLOT + (CFN ? 45 * CH : 0), WS_KQ = WS_SLOT + 80 * CH, WS_ACT = WS_SLOT + 112 * CH;
constexpr unsigned WS_H1  = WS_SLOT + NPG * SLOT;
constexpr unsigned WS_AX  = WS_H1 + S * D * 2;
constexpr unsigned WS_AM  = WS_AX + RS * 32 * 2048;
constexpr unsigned WS_X1  = WS_AM + RS * 8 * 2048;
constexpr unsigned WS_LS  = WS_X1 + RS * 32 * D * 2;
constexpr unsigned WS_FW = WS_LS + RS * 10240;
constexpr unsigned WS_DBG = WS_FW + 27 * 4 * 45056;
constexpr unsigned WS_END = WS_DBG + (2 << 20);

__device__ __forceinline__ float b2f(u16 b) { return __uint_as_float((unsigned)b << 16); }
__device__ __forceinline__ float blo(unsigned x) { return __uint_as_float(x << 16); }
__device__ __forceinline__ float bhi(unsigned x) { return __uint_as_float(x & 0xffff0000u); }
__device__ __forceinline__ unsigned pk2(float lo, float hi) { bf16x2 v; v[0] = (__bf16)lo; v[1] = (__bf16)hi; return __builtin_bit_cast(unsigned, v); }
__device__ __forceinline__ float rnd(float f) { return blo(pk2(f, 0.f)); }
__device__ __forceinline__ unsigned nosent(unsigned x) { return x < 0xFFFFFFFEu ? x : 0xFFFFFFFEu; }

struct Args { const u16 *x, *mem0, *Wq, *Wk, *Wv, *Wo, *wg, *wu, *wd, *c, *wm; u16* out; unsigned char* ws; int tp[24]; };
__device__ __forceinline__ void dly(int n) { for (int i = 0; i < n; ++i) __builtin_amdgcn_s_sleep(1); }
__device__ __forceinline__ unsigned long long rtime() { unsigned long long t; asm volatile("s_memrealtime %0\n\ts_waitcnt lgkmcnt(0)" : "=s"(t) :: "memory"); return t; }
constexpr unsigned SPIN_MAX = 200000u, OOB = 0x7FFFF000u;
#define TL(i)
#define ST(i)
#define ST_INIT
#define ST_RESET
#define ST_OUT
#define ST_T(t)
#define LG(k, val)
__device__ __forceinline__ rsrc_t mkrs(const void* p, unsigned bytes) { return __builtin_amdgcn_make_buffer_rsrc((void*)p, 0, (int)bytes, 0x00020000); }
__device__ __forceinline__ u32x4 ld_sc1(rsrc_t r, unsigned voff, unsigned soff) { return __builtin_amdgcn_raw_buffer_load_b128(r, voff, soff, 16); }
__device__ __forceinline__ u32x4 ld_pl(rsrc_t r, unsigned voff, unsigned soff) { return __builtin_amdgcn_raw_buffer_load_b128(r, voff, soff, 0); }
__device__ __forceinline__ void st128(u32x4 v, rsrc_t r, unsigned voff, unsigned soff) { __builtin_amdgcn_raw_buffer_store_b128(v, r, voff, soff, 16); asm volatile("s_nop 1" :: "v"(v)); }
__device__ __forceinline__ void st64(u32x2 v, rsrc_t r, unsigned voff, unsigned soff) { __builtin_amdgcn_raw_buffer_store_b64(v, r, voff, soff, 16); }
__device__ __forceinline__ void st32(unsigned v, rsrc_t r, unsigned voff, unsigned soff) { __builtin_amdgcn_raw_buffer_store_b32(v, r, voff, soff, 16); }
__device__ __forceinline__ unsigned umax3(unsigned a, unsigned b, unsigned c) { unsigned m = a > b ? a : b; return m > c ? m : c; }
__device__ __forceinline__ unsigned mx4(u32x4 v, unsigned m) { return umax3(umax3(v.x, v.y, m), v.z, v.w); }
__device__ __forceinline__ bool all_ok(unsigned m) { return __builtin_amdgcn_ballot_w64(m == 0xFFFFFFFFu) == 0ull; }
__device__ __forceinline__ f32x4 smf(u32x4 a, u32x8 b, f32x4 c, int idx) { return __builtin_amdgcn_smfmac_f32_16x16x64_bf16(__builtin_bit_cast(bf16x8, a), __builtin_bit_cast(bf16x16, b), c, idx, 0, 0); }
__device__ __forceinline__ f32x4 mf16(u32x4 a, u32x4 b, f32x4 c) { return __builtin_amdgcn_mfma_f32_16x16x32_bf16(__builtin_bit_cast(bf16x8, a), __builtin_bit_cast(bf16x8, b), c, 0, 0, 0); }
__device__ __forceinline__ f32x16 mf32(u32x4 a, u32x4 b, f32x16 c) { return __builtin_amdgcn_mfma_f32_32x32x16_bf16(__builtin_bit_cast(bf16x8, a), __builtin_bit_cast(bf16x8, b), c, 0, 0, 0); }
__device__ __forceinline__ float fmx(float a, float b) { return __builtin_elementwise_maximum(a, b); }
__device__ __forceinline__ float fold32(float a, float b) { auto r = __builtin_amdgcn_permlane32_swap(__float_as_uint(a), __float_as_uint(b), false, false); return __uint_as_float(r[0]) + __uint_as_float(r[1]); }
__device__ __forceinline__ float x32max(float a) { auto r = __builtin_amdgcn_permlane32_swap(__float_as_uint(a), __float_as_uint(a), false, false); return fmx(__uint_as_float(r[0]), __uint_as_float(r[1])); }
__device__ __forceinline__ float x32sum(float a) { return fold32(a, a); }
__device__ __forceinline__ float x16max(float a) { auto r = __builtin_amdgcn_permlane16_swap(__float_as_uint(a), __float_as_uint(a), false, false); return fmx(__uint_as_float(r[0]), __uint_as_float(r[1])); }
__device__ __forceinline__ float x16sum(float a) { auto r = __builtin_amdgcn_permlane16_swap(__float_as_uint(a), __float_as_uint(a), false, false); return __uint_as_float(r[0]) + __uint_as_float(r[1]); }
template <int N> __device__ __forceinline__ float ror(float a) { return __int_as_float(__builtin_amdgcn_update_dpp(0, __float_as_int(a), 0x120 + N, 0xf, 0xf, true)); }
template <int C> __device__ __forceinline__ float qp(float a) { return __int_as_float(__builtin_amdgcn_update_dpp(0, __float_as_int(a), C, 0xf, 0xf, true)); }
__device__ __forceinline__ float sum16(float a) { a += ror<8>(a); a += ror<4>(a); a += ror<2>(a); a += ror<1>(a); return a; }
__device__ __forceinline__ void bar() { asm volatile("s_waitcnt lgkmcnt(0)\n\ts_barrier" ::: "memory"); __builtin_amdgcn_s_waitcnt(0xC07F); }
__device__ __forceinline__ void drain() { asm volatile("s_waitcnt vmcnt(0)" ::: "memory"); }
__device__ __forceinline__ u32x2 trd(lptr p) { return __builtin_bit_cast(u32x2, __builtin_amdgcn_ds_read_tr16_b64_v4i16((LAS s16x4*)p)); }
__device__ __forceinline__ u32x4 lrd(lptr p) { return *(LAS u32x4*)p; }
__device__ __forceinline__ void lwr(lptr p, u32x4 v) { *(LAS u32x4*)p = v; }
__device__ __forceinline__ float dot2(unsigned a, unsigned b, float c) { return __builtin_amdgcn_fdot2_f32_bf16(__builtin_bit_cast(bf16x2, a), __builtin_bit_cast(bf16x2, b), c, false); }

__device__ __forceinline__ unsigned ld_fl(rsrc_t rs, unsigned voff, int seg) { asm volatile("" ::: "memory"); return __builtin_amdgcn_raw_buffer_load_b32(rs, voff, seg * FSEG, 16); }
__device__ __forceinline__ bool fl_ok(unsigned v) { const bool r = __builtin_amdgcn_ballot_w64(v != 0u) == 0ull; asm volatile("" ::: "memory"); return r; }
__device__ __forceinline__ void wait_fl(rsrc_t rs, unsigned voff, int seg, int& seen, bool& dead, int slp) {
    if (seen >= seg) return;
    for (unsigned sp = 0;; ++sp) {
        if (fl_ok(ld_fl(rs, voff, seg)) || dead) break;
        if (sp > SPIN_MAX) { dead = true; break; }
        dly(slp);
    }
    seen = seg;
}
__device__ __forceinline__ void wait_ring(rsrc_t rs, unsigned voff, int seg, int& seen, bool& dead) {
    if (seen >= seg) return;
    wait_fl(rs, voff, seg, seen, dead, 8);
}

constexpr int L_A = 0;
constexpr int L_QK = 10240;
constexpr int L_KQX = 14336;
constexpr int L_STG = 49152;
constexpr int L_MOD = 98304;
constexpr int L_TOT = 123392;

#define FHR 4
#define WD 3
#define WAUX 2
template <int KIND> __device__ __forceinline__ unsigned wsoff(int r, int j) {
    const int i = 5 * r + j;
    return KIND == 0 ? (64 * (i >> 2) + 32 * ((i & 3) >> 1) + 2 * (i & 1)) * (HE * 2) : KIND == 1 ? (64 * (r >> 2) + 32 * ((r & 3) >> 1) + (r & 1)) * (D * 2) + j * 256
         : KIND == 2 ? 16 * i * (HE * 2) : (32 * (r >> 1) + 8 * (r & 1)) * (D * 2) + j * 256; }
template <int KIND> __device__ __forceinline__ unsigned wvoff(unsigned base, int wq, int lane) {
    const int q = lane >> 4, jj = 4 * (wq & 1) + q;
    const int row = KIND == 0 ? 16 * (wq >> 1) + 8 * (q >> 1) + 4 * (wq & 1) + (q & 1) : KIND == 1 ? 4 * (q >> 1) + 8 * (wq >> 1) + 2 * (wq & 1) + 16 * (q & 1)
                  : KIND == 2 ? 4 * wq + q : 4 * (wq >> 1) + (jj >> 1) + 16 * (jj & 1);
    return row * ((KIND & 1) ? D * 2 : HE * 2) + base + (lane & 15) * 16;
}
template <int KIND> __device__ __forceinline__ void wissue(u32x4 (&v)[40], rsrc_t rw, unsigned voff, int r0, int r1) {
    __builtin_amdgcn_sched_barrier(0);
    #pragma unroll
    for (int i = 0; i < 40; ++i) if (i >= 5 * r0 && i < 5 * r1) v[i] = __builtin_amdgcn_raw_buffer_load_b128(rw, voff, wsoff<KIND>(i / 5, i % 5), WAUX);
    __builtin_amdgcn_sched_barrier(0);
}
__device__ __forceinline__ u32x2 ld_pol(rsrc_t rs, int lane) { asm volatile("" ::: "memory"); return __builtin_amdgcn_raw_buffer_load_b64(rs, WS_POL + (lane & (NPG - 1)) * 8, 0, 16); }
template <bool WO>
__device__ __forceinline__ void load_w_smf(u32x8 (&W)[20], rsrc_t rw, unsigned base, int wave, int lane, lptr L, rsrc_t rs, u32x2& pv) {
    const int wq = wave & 3;
    u32x4 v[40];
    const unsigned voff = wvoff<WO ? 1 : 0>(base, wq, lane);
    wissue<WO ? 1 : 0>(v, rw, voff, 0, WD);
    lptr wp = L + (wave >> 2) * 20480 + ((lane >> 2) & 3) * 1024 + (4 * wq + (lane >> 4)) * 64 + (lane & 3) * 16;
    lptr rp = L + (wave >> 2) * 20480 + (WO ? 5 * wq : wq) * 1024 + (lane >> 2) * 64 + (lane & 3) * 8;
    #pragma unroll
    for (int r = 0; r < 8; ++r) {
        #pragma unroll
        for (int j = 0; j < 5; ++j) lwr(wp + (r & 1) * 40960 + j * 4096, v[5 * r + j]);
        bar();
        #pragma unroll
        for (int j = 0; j < 5; ++j) { const int i = 5 * r + j, pi = WO ? r & 3 : i & 3;
            lptr p = rp + (r & 1) * 40960 + j * (WO ? 1024 : 4096);
            const u32x2 r0 = trd(p), r1 = trd(p + 32);
            const int i0 = WO ? 4 * j + (r >> 2) : 2 * (i >> 2), i1 = WO ? i0 + 2 : i0 + 1;
            W[i0][2 * pi] = r0.x; W[i0][2 * pi + 1] = r0.y; W[i1][2 * pi] = r1.x; W[i1][2 * pi + 1] = r1.y; }
        wissue<WO ? 1 : 0>(v, rw, voff, r + WD, r + WD + 1);
        if (r == 6) { pv = ld_pol(rs, lane); __builtin_amdgcn_sched_barrier(0); }
    }
    bar();
}
template <bool WO, class FN>
__device__ __forceinline__ void load_w_32(u32x4 (&W)[40], rsrc_t rw, unsigned base, int wave, int lane, lptr L, FN hook) {
    const int wq = wave & 3, G = lane >> 4;
    u32x4 v[40];
    const unsigned voff = wvoff<WO ? 3 : 2>(base, wq, lane);
    wissue<WO ? 3 : 2>(v, rw, voff, 0, WD);
    lptr wp = L + (wave >> 2) * 20480 + ((lane >> 2) & 3) * 1024 + (4 * wq + (lane >> 4)) * 64 + (lane & 3) * 16;
    lptr rp = L + (wave >> 2) * 20480 + (WO ? 5 * wq : wq) * 1024 + (8 * (G >> 1) + ((lane >> 2) & 3)) * 64 + (G & 1) * 32 + (lane & 3) * 8;
    #pragma unroll
    for (int r = 0; r < 8; ++r) {
        #pragma unroll
        for (int j = 0; j < 5; ++j) lwr(wp + (r & 1) * 40960 + j * 4096, v[5 * r + j]);
        bar();
        #pragma unroll
        for (int j = 0; j < 5; ++j) { lptr p = rp + (r & 1) * 40960 + j * (WO ? 1024 : 4096);
            const u32x2 r0 = trd(p), r1 = trd(p + 256);
            W[WO ? 8 * j + r : 5 * r + j] = (u32x4){r0.x, r0.y, r1.x, r1.y}; }
        __builtin_amdgcn_sched_barrier(0); hook(r);
        wissue<WO ? 3 : 2>(v, rw, voff, r + WD, r + WD + 1);
    }
    bar();
}

struct Pc { unsigned g1, g2; int p1, p2; };
__device__ __forceinline__ Pc mkpc(int p1, int p2, int lane) { Pc r; r.p1 = p1; r.p2 = p2; r.g1 = WS_SLOT + SL_MEM + (p1 >> 3) * CH + (p1 & 7) * 1024 + lane * 16; r.g2 = WS_SLOT + SL_MEM + (p2 >> 3) * CH + (p2 & 7) * 1024 + lane * 16; return r; }
template <int NP>
__device__ __forceinline__ void gather_mem(const Args& a, rsrc_t rs, unsigned so, bool fresh, const Pc& pc, lptr A, bool& dead) {
    if (NP == 0) return;
    u32x4 v0 = {~0u, ~0u, ~0u, ~0u}, v1 = {0, 0, 0, 0};
    if (!fresh) { v0 = lrd(A + pc.p1 * 1024); if (NP == 2) v1 = lrd(A + pc.p2 * 1024); }
    for (unsigned sp = 0;; ++sp) {
        unsigned m = mx4(v0, 0); if (NP == 2) m = mx4(v1, m);
        if (all_ok(m) || dead) break;
        if (sp > SPIN_MAX) { dead = true; break; }
        asm volatile("" ::: "memory");
        if (m == 0xFFFFFFFFu) { v0 = ld_sc1(rs, pc.g1, so); if (NP == 2) v1 = ld_sc1(rs, pc.g2, so); }
    }
    lwr(A + pc.p1 * 1024, v0);
    if (NP == 2) lwr(A + pc.p2 * 1024, v1);
}
struct Fm { u32x4 a, b; };
__device__ __forceinline__ Fm ld_mem0(const Args& a, int wave, int l) {
    const u16* s = a.mem0 + (2 * ((l >> 2) & 3) + (l & 1)) * D + 16 * (l >> 4) + 8 * ((l >> 1) & 1);
    Fm r; r.a = *(const u32x4*)(s + 64 * wave); r.b = *(const u32x4*)(s + 64 * ((wave & 1) + 8));
    return r;
}
__device__ __forceinline__ void first_mem(const Fm& m, int wave, lptr A) { lwr(A + wave * 1024, m.a); lwr(A + ((wave & 1) + 8) * 1024, m.b); }
__device__ __forceinline__ unsigned long long get_pol(rsrc_t rs, int lane, bool& dead, u32x2 tag, u32x2 v) {
    for (unsigned sp = 0;; ++sp) {
        if (__builtin_amdgcn_ballot_w64(v.x != tag.x || ((v.y ^ tag.y) << 1) != 0u) == 0ull || dead) break;
        if (sp > SPIN_MAX) { dead = true; break; }
        if (sp) dly(8);
        v = ld_pol(rs, lane); }
    asm volatile("" ::: "memory");
    return __builtin_amdgcn_ballot_w64(v.y != tag.y);
}
#define PG(p) (((unsigned)(p) << 21) + (((unsigned)(pol >> (p)) & 1u) << 13))
#define SO(t) PG((t) & (RS - 1))
#define FO(t) (SO(t) ^ 8192u)
#define RO(s, odd) (PG(2 * ((unsigned)(s) % RC) + (odd)) ^ 8192u)
#define AO(s) (SO(s) ^ ((s) >= NSEG - 2 ? 0u : 8192u))
#define RI(s) ((s) & (RS - 1))
#define CO(s, odd) (RO(s, odd) ^ (CFN ? 8192u : 0u))
__device__ __forceinline__ unsigned rt32() { return (unsigned)__builtin_amdgcn_s_memrealtime(); }
__device__ __forceinline__ void gate(unsigned target) {
    asm volatile("1:\n\ts_memrealtime vcc\n\ts_waitcnt lgkmcnt(0)\n\ts_sub_i32 vcc_lo, %0, vcc_lo\n\ts_cmp_gt_i32 vcc_lo, 0\n\ts_cbranch_scc1 1b" :: "s"(target) : "vcc", "scc", "memory"); }
template <int N> __device__ __forceinline__ int rori(int a) { return __builtin_amdgcn_update_dpp(0, a, 0x120 + N, 0xf, 0xf, true); }
__device__ __forceinline__ unsigned read_G(rsrc_t rs, unsigned so, int lane, bool& dead) {
    u32x2 v;
    for (unsigned sp = 0;; ++sp) { asm volatile("" ::: "memory");
        v = __builtin_amdgcn_raw_buffer_load_b64(rs, WS_SLOT + SL_STP + (lane < 40 ? lane : 0) * 32, so, 16);
        if (all_ok(v.x > v.y ? v.x : v.y) || dead) break;
        if (sp > SPIN_MAX) { dead = true; break; }
        dly(2); }
    const unsigned ref = rt32();
    int d = max((int)(v.x - ref), (int)(v.y - ref));
    d = max(d, rori<8>(d)); d = max(d, rori<4>(d)); d = max(d, rori<2>(d)); d = max(d, rori<1>(d));
    { auto r = __builtin_amdgcn_permlane16_swap(d, d, false, false); d = max((int)r[0], (int)r[1]); }
    { auto r = __builtin_amdgcn_permlane32_swap(d, d, false, false); d = max((int)r[0], (int)r[1]); }
    d = min(max(d, -100000), 600);
    return ref + (unsigned)__builtin_amdgcn_readfirstlane(d);
}
constexpr int L_FAIL = 63488;
constexpr int L_DUM = 63744;
#define LS2(x0, x1)
#define DG(x)
__device__ __forceinline__ void dma1(rsrc_t rs, lptr d, unsigned vo, unsigned so) { __builtin_amdgcn_raw_ptr_buffer_load_lds(rs, (LAS void*)d, 16, vo, so, 0, 16); }
__device__ __forceinline__ void dma4(rsrc_t rs, lptr d, unsigned vo, unsigned so) {
    __builtin_amdgcn_raw_ptr_buffer_load_lds(rs, (LAS void*)d, 16, vo, so, 0, 16); __builtin_amdgcn_raw_ptr_buffer_load_lds(rs, (LAS void*)d, 16, vo, so, 1024, 16);
    __builtin_amdgcn_raw_ptr_buffer_load_lds(rs, (LAS void*)d, 16, vo, so, 2048, 16); __builtin_amdgcn_raw_ptr_buffer_load_lds(rs, (LAS void*)d, 16, vo, so, 3072, 16); }
template <int NP>
__device__ __forceinline__ void dma_mem(rsrc_t rs, unsigned so, const Pc& pc, lptr L) {
    if (NP > 0) __builtin_amdgcn_raw_ptr_buffer_load_lds(rs, (LAS void*)(L + L_A + pc.p1 * 1024), 16, pc.g1, so, 0, 16);
    if (NP > 1) __builtin_amdgcn_raw_ptr_buffer_load_lds(rs, (LAS void*)(L + L_A + pc.p2 * 1024), 16, pc.g2, so, 0, 16);
}
#define FETCH(NP, GATE) do { const unsigned so_ = SO(t > 0 ? t - 1 : 0); \
    if (__builtin_expect(!again && gated && t > 1, 1)) { tagc = t; GATE; ST(0); bar(); ST(5); dma_mem<NP>(rs, so_, pc, L); } \
    else { if (t > 0) gather_mem<NP>(a, rs, so_, !again, pc, Asrc, dead); tagc = -5; } } while (0)
__device__ __forceinline__ void flag(unsigned m, int t, int lane, lptr L) { *(LAS int*)(L + (m == 0xFFFFFFFFu ? L_FAIL : L_DUM + lane * 4)) = t; }
template <int NP>
__device__ __forceinline__ void check_own(int t, int lane, const Pc& pc, lptr A, lptr L) {
    unsigned m = mx4(lrd(A + pc.p1 * 1024), 0);
    if (NP == 2) m = mx4(lrd(A + pc.p2 * 1024), m);
    flag(m, t, lane, L);
}
__device__ __forceinline__ bool failed(int t, lptr L) { return __builtin_expect(__builtin_amdgcn_readfirstlane(*(LAS int*)(L + L_FAIL)) == t, 0); }
#define SB() __builtin_amdgcn_sched_barrier(0)
#define VPT 1
#define QPT 2
#define VE 1
#define VD0 10
#define HAUX 16
#define FQ 6
#define FV 10
#define OM 2
#define OG 1
#define FW0 22
#define FWS 3
#define UBAR 3
#define NCAL 3
#define NB1 8
#define NB2 0
#define PH1 2
template <int B> struct Ic { static constexpr int value = B; };
__device__ __forceinline__ unsigned lds_inc(LAS unsigned* p) { unsigned old; asm volatile("ds_add_rtn_u32 %0, %1, %2" : "=v"(old) : "v"(p), "v"(1u) : "memory"); return old; }
struct Nop { __device__ void operator()() const {} };
template <int KIND, class FN = Nop>
__device__ __forceinline__ void proj2(const u32x8 (&W)[20], lptr Asrc, lptr dstp, int idx, int t, const Pc& pc, int lane, lptr L, bool young, unsigned& dg_, FN mid = Nop()) {
    f32x4 e = {0, 0, 0, 0}, o = {0, 0, 0, 0};
    u32x4 af[10], own, own2;
    asm volatile("" : "+v"(e), "+v"(o));
    if (KIND == 1) { drain(); DG(rt32()); }
    if (KIND == 3) { asm volatile("s_waitcnt vmcnt(1)" ::: "memory"); DG(rt32()); }
    if (KIND == 4) { if (young) asm volatile("s_waitcnt vmcnt(8)" ::: "memory"); else drain(); DG(rt32()); }
    bar();
    #pragma unroll
    for (int ks = 0; ks < 4; ++ks) af[ks] = lrd(Asrc + ks * 1024);
    SB();
    #pragma unroll
    for (int ks = 0; ks < PH1; ++ks) { e = smf(af[ks], W[2 * ks], e, idx); o = smf(af[ks], W[2 * ks + 1], o, idx); }
    asm volatile("" : "+v"(e), "+v"(o));
    if (KIND == 2 || KIND == 3) drain();
    if (KIND == 5) asm volatile("s_waitcnt vmcnt(1)" ::: "memory");
    if (KIND == 2 || KIND == 5) DG(rt32());
    bar();
    if (KIND != 4) own = lrd(Asrc + pc.p1 * 1024);
    if (KIND == 3) own2 = lrd(Asrc + pc.p2 * 1024);
    SB();
    #pragma unroll
    for (int ks = 4; ks < 10; ++ks) af[ks] = lrd(Asrc + ks * 1024);
    SB();
    #pragma unroll
    for (int ks = PH1; ks < 4; ++ks) { e = smf(af[ks], W[2 * ks], e, idx); o = smf(af[ks], W[2 * ks + 1], o, idx); }
    SB();
    if (KIND != 4) { asm volatile("" : "+v"(own)); unsigned m = mx4(own, 0); if (KIND == 3) m = mx4(own2, m); flag(m, t, lane, L); }
    #pragma unroll
    for (int ks = 4; ks < 10; ++ks) { e = smf(af[ks], W[2 * ks], e, idx); o = smf(af[ks], W[2 * ks + 1], o, idx); if (ks == 5) { asm volatile("" : "+v"(e), "+v"(o)); SB(); mid(); SB(); } }
    *(LAS unsigned*)dstp = pk2(e[0] + e[2], o[0] + o[2]); *(LAS unsigned*)(dstp + 16) = pk2(e[1] + e[3], o[1] + o[3]);
}
#define PROJ(KIND, GATE, YOUNG) FETCH((KIND == 3 ? 2 : KIND == 4 ? 0 : 1), GATE); proj2<KIND>(W, Asrc, pdst, sidx, t, pc, lane, L, YOUNG, dg_); ST(2); bar(); ST(3); \
        again = failed(tagc, L); if (again) { --t; continue; } DGL;
__device__ __forceinline__ void max3x2(float a, float b, float c, float& r0, float& r1) { asm("v_maximum3_f32 %0, %2, %3, %4\n\tv_maximum3_f32 %1, %2, %3, %4\n\ts_nop 1" : "=&v"(r0), "=&v"(r1) : "v"(a), "v"(b), "v"(c)); }
__device__ __forceinline__ void addx2(float a, float b, float& r0, float& r1) { asm("v_add_f32 %0, %2, %3\n\tv_add_f32 %1, %2, %3\n\ts_nop 1" : "=&v"(r0), "=&v"(r1) : "v"(a), "v"(b)); }
__device__ __forceinline__ void sw16(float& a, float& b) { auto r = __builtin_amdgcn_permlane16_swap(__float_as_uint(a), __float_as_uint(b), false, false); a = __uint_as_float(r[0]); b = __uint_as_float(r[1]); }
__device__ __forceinline__ void sw32(float& a, float& b) { auto r = __builtin_amdgcn_permlane32_swap(__float_as_uint(a), __float_as_uint(b), false, false); a = __uint_as_float(r[0]); b = __uint_as_float(r[1]); }
__device__ __forceinline__ int proj2_dst(int wq, int lane) { const int c = lane & 15; return (2 * wq + (c >> 3)) * 256 + (4 * (lane >> 4) + 2 * ((c >> 2) & 1)) * 16 + (c & 3) * 4; }
__device__ __forceinline__ int tokoff(int tok) { return (4 * (tok >> 1) + (tok & 1)) * 16; }

__device__ __forceinline__ u32x4 pk8(const f32x16& a, int p) { return (u32x4){pk2(a[4 * p], a[4 * p + 8]), pk2(a[4 * p + 1], a[4 * p + 9]), pk2(a[4 * p + 2], a[4 * p + 10]), pk2(a[4 * p + 3], a[4 * p + 11])}; }

constexpr float CS = 0.08838834764831845f * 1.4426950408889634f;
enum { R_P, R_V, R_C, R_OUT, R_QK, R_VO, R_F1, R_F2, R_SP };
#define FSW 1
#define HM 1
#define RMAP 2
__host__ __device__ inline void rolemap(int b, int& role, int& id) {
    const int grp = b & 7, idx8 = b >> 3;
    if (RMAP == 2) {
        if (grp < 4) {
            if (idx8 < 8) { role = R_P; id = HM ? grp * 8 + idx8 : idx8 * 4 + grp; } else if (idx8 < 16) { role = R_V; id = HM ? grp * 8 + idx8 - 8 : (idx8 - 8) * 4 + grp; } else if (idx8 < 26) { role = R_C; id = grp * 10 + idx8 - 16; }
            else if (FSW) { const int k = (idx8 - 26) * 4 + grp; if (k < 23) { role = R_F1; id = k; } else { role = R_SP; id = 0; } }
            else if (idx8 < 31) { role = R_F2; id = (idx8 - 26) * 4 + grp; } else if (grp < 3) { role = R_F1; id = 24 + grp; } else { role = R_SP; id = 0; }
        } else if (OM == 2) {
            const bool og = (grp & 1) == OG;
            if (og && idx8 < 20) { role = R_OUT; id = (grp >> 1 & 1) * 20 + idx8; }
            else { const int wi = og ? 64 + (idx8 - 20) * 2 + (grp >> 1 & 1) : idx8 * 2 + (grp >> 1 & 1);
                if (wi < 64) { role = (wi & 2) ? R_QK : R_VO; id = (wi >> 2) * 2 + (wi & 1); } else if (wi < 84) { role = R_F2; id = wi - 64; } else { role = R_F1; id = wi - 61; } }
        } else if (idx8 < 10) { role = R_OUT; id = ((grp - 4) ^ OM) * 10 + idx8; }
        else { const int wi = (idx8 - 10) * 4 + grp - 4;
            if (wi < 32) { role = R_VO; id = HM ? (wi & 3) * 8 + (wi >> 2) : wi; } else if (wi < 64) { role = R_QK; id = HM ? (wi & 3) * 8 + ((wi - 32) >> 2) : wi - 32; } else if (!FSW) { role = R_F1; id = wi - 64; }
            else if (wi < 84) { role = R_F2; id = wi - 64; } else { role = R_F1; id = wi - 61; } }
        return;
    }
    if (RMAP == 3) {
        if (idx8 < 16) { role = idx8 < 4 ? R_P : idx8 < 8 ? R_QK : idx8 < 12 ? R_V : R_VO; id = (idx8 & 3) * 8 + grp; }
        else if (idx8 < 21) { role = R_C; id = grp * 5 + idx8 - 16; }
        else if (idx8 < 25) { role = R_OUT; id = grp * 4 + idx8 - 21; }
        else if (idx8 < 27 && grp < 4) { role = R_OUT; id = 32 + grp * 2 + idx8 - 25; }
        else { const int k = idx8 < 27 ? (idx8 - 25) * 4 + grp - 4 : 8 + (idx8 - 27) * 8 + grp;
            if (k < 27) { role = R_F1; id = k; } else if (k < 47) { role = R_F2; id = k - 27; } else { role = R_SP; id = 0; } }
        return;
    }
    if (grp == 0) { role = R_P; id = idx8; return; }
    if (grp == 1) { role = R_V; id = idx8; return; }
    if (RMAP == 0) {
        if (grp == 7) { if (idx8 < 27) { role = R_F1; id = idx8; } else { role = R_F2; id = idx8 - 27; } }
        else if (idx8 < 8) { role = R_C; id = (grp - 2) * 8 + idx8; }
        else if (idx8 < 16) { role = R_OUT; id = (grp - 2) * 8 + idx8 - 8; }
        else { const int wi = (idx8 - 16) * 5 + grp - 2;
            if (wi < 32) { role = R_QK; id = wi; } else if (wi < 64) { role = R_VO; id = wi - 32; } else if (wi < 79) { role = R_F2; id = wi - 59; } else { role = R_SP; id = 0; } }
    } else {
        if (grp < 4) {
            if (idx8 < 20) { role = R_C; id = (grp - 2) * 20 + idx8; }
            else if (idx8 < 30) { role = R_F2; id = (idx8 - 20) * 2 + grp - 2; }
            else { const int k = (idx8 - 30) * 2 + grp - 2; if (k < 3) { role = R_QK; id = 29 + k; } else { role = R_SP; id = 0; } }
        } else if (idx8 < 10) { role = R_OUT; id = (grp - 4) * 10 + idx8; }
        else { const int wi = (idx8 - 10) * 4 + grp - 4;
            if (wi < 32) { role = R_VO; id = wi; } else if (wi < 61) { role = R_QK; id = wi - 32; } else { role = R_F1; id = wi - 61; } }
    }
}
__global__ void __launch_bounds__(512, 2) mega(Args a) {
    extern __shared__ __attribute__((aligned(16))) unsigned char smem[];
    lptr L = (lptr)smem;
    const int tid = threadIdx.x, lane = tid & 63, wave = __builtin_amdgcn_readfirstlane(tid >> 6);
    int role, id; rolemap(blockIdx.x, role, id);
    const int hid = role == R_C ? id : role == R_OUT ? 40 + id : role == R_F1 ? 80 + id : role == R_F2 ? 107 + id : role == R_SP ? 127 : -1;
    const rsrc_t rs = mkrs(a.ws, WS_END);
    const int sidx = (lane & 2) ? 0xEEEE : 0x4444;
    bool dead = false;
    lptr stg = L + L_STG + wave * 4096;
    ST_INIT
    TL(0);
    const unsigned tstart = rt32();
    unsigned long long ep;
    asm volatile("s_load_dwordx2 %0, %1, 0x0\n\ts_waitcnt lgkmcnt(0)" : "=s"(ep) : "s"(a.ws + WS_EP) : "memory");
    ++ep;
    const u32x2 tag = {(unsigned)ep, (unsigned)(ep >> 32)};
    unsigned long long pol = 0;
    if (hid >= 0) {
        const int row = hid * 16 + wave * 2 + (lane >> 5), l32 = lane & 31;
        if (hid < 120) {
            #pragma unroll
            for (int i = 0; i < 2; ++i) { const unsigned u = (i * 120 + hid) * 8192 + tid * 16;
                st128((u32x4){~0u, ~0u, ~0u, ~0u}, rs, u < SL_END ? u : u < 2 * SL_END ? u - SL_END + SLOT : u < 2 * SL_END + 3 * CH ? u - 2 * SL_END + 2 * SLOT + 2 * CH : OOB, WS_SLOT); } }
        if (hid < 96 && tid < 128) st32(~0u, rs, WS_FLG + (hid * 128 + tid) * FS, 0);
        u32x2 xv[5];
        #pragma unroll
        for (int i = 0; i < 5; ++i) xv[i] = *(const u32x2*)(a.x + row * D + (l32 + 32 * i) * 4);
        if (hid < 120) {
            u32x4 wv[5]; float sv[5];
            #pragma unroll
            for (int i = 0; i < 5; ++i) { const int k = (tid >> 2) + 128 * i; wv[i] = *(const u32x4*)(a.wm + k * 3840 + hid * 32 + (tid & 3) * 8); sv[i] = b2f(a.c[k]); }
            float acc[8] = {0, 0, 0, 0, 0, 0, 0, 0};
            #pragma unroll
            for (int i = 0; i < 5; ++i) { const float s = rnd(sv[i] / (1.f + __expf(-sv[i])));
                #pragma unroll
                for (int j = 0; j < 4; ++j) { acc[2 * j] += s * blo(wv[i][j]); acc[2 * j + 1] += s * bhi(wv[i][j]); } }
            #pragma unroll
            for (int j = 0; j < 8; ++j) { float v = acc[j]; v += ror<8>(v); v += ror<4>(v); v = x16sum(v); acc[j] = x32sum(v); }
            if (lane < 4) { *(LAS f32x4*)(L + wave * 128 + lane * 32) = (f32x4){acc[0], acc[1], acc[2], acc[3]}; *(LAS f32x4*)(L + wave * 128 + lane * 32 + 16) = (f32x4){acc[4], acc[5], acc[6], acc[7]}; }
            drain(); bar();
            if (tid < 8) { f32x4 s = {0, 0, 0, 0};
                #pragma unroll
                for (int w = 0; w < 8; ++w) s += *(LAS f32x4*)(L + w * 128 + tid * 16);
                st128((u32x4){pk2(s[0], s[1]), pk2(s[2], s[3]), tag.x, tag.y}, rs, WS_MOD + hid * 128 + tid * 16, 0); }
        }
        float xf[20], s = 0;
        #pragma unroll
        for (int i = 0; i < 5; ++i) { xf[4 * i] = blo(xv[i].x); xf[4 * i + 1] = bhi(xv[i].x); xf[4 * i + 2] = blo(xv[i].y); xf[4 * i + 3] = bhi(xv[i].y); }
        #pragma unroll
        for (int i = 0; i < 20; ++i) s += xf[i];
        const float mean = x16sum(sum16(s)) * (1.f / D);
        float vs = 0;
        #pragma unroll
        for (int i = 0; i < 20; ++i) { const float d = xf[i] - mean; vs += d * d; }
        const float var = x16sum(sum16(vs)) * (1.f / D);
        const float mb = rnd(mean), rd = 1.f / rnd(__builtin_sqrtf(rnd(rnd(var) + 1e-5f)));
        #pragma unroll
        for (int i = 0; i < 20; ++i) xf[i] = rnd(rnd(xf[i] - mb) * rd);
        const bool cal = hid >= 8 && hid < 8 + NPG;
        if (cal) {
            const unsigned vo = lane < 32 ? WS_SLOT + SL_CAL + (wave & 3) * 2048 + (wave >> 2) * CH + lane * 4 : OOB;
            unsigned m[2] = {~0u, ~0u};
            for (int r = 0; r < NCAL; ++r)
                #pragma unroll
                for (int q = 0; q < 2; ++q) { unsigned long long t0, t1;
                    asm volatile("s_waitcnt vmcnt(0)\n\ts_memtime %0\n\ts_waitcnt lgkmcnt(0)" : "=s"(t0) :: "memory");
                    st32(0u, rs, vo, (hid - 8) * SLOT + q * 8192);
                    asm volatile("s_waitcnt vmcnt(0)\n\ts_memtime %0\n\ts_waitcnt lgkmcnt(0)" : "=s"(t1) :: "memory");
                    m[q] = min(m[q], (unsigned)(t1 - t0)); }
            *(LAS u32x2*)(L + 2048 + wave * 8) = (u32x2){m[0], m[1]};
        }
        {   u32x4 mv, mw; const int j = tid < 480 ? tid : 479;
            for (unsigned sp = 0;; ++sp) { asm volatile("" ::: "memory"); mv = ld_sc1(rs, WS_MOD + j * 32, 0); mw = ld_sc1(rs, WS_MOD + j * 32 + 16, 0);
                if (__builtin_amdgcn_ballot_w64(mv.z != tag.x || mv.w != tag.y || mw.z != tag.x || mw.w != tag.y) == 0ull) break;
                if (sp > SPIN_MAX) { dead = true; break; } dly(2); }
            lwr(L + L_MOD + j * 16, (u32x4){mv.x, mv.y, mw.x, mw.y}); }
        bar();
        if (cal && wave == 0) { const u32x2 mm = *(LAS u32x2*)(L + 2048 + (lane & 7) * 8);
            const unsigned bm = (unsigned)__builtin_amdgcn_ballot_w64(mm.y < mm.x);
            st64((u32x2){tag.x, tag.y ^ (__builtin_popcount(bm & 0xFEu) > 3 ? 0x80000000u : 0u)}, rs, lane ? OOB : WS_POL + (hid - 8) * 8, 0); }
        #pragma unroll
        for (int i = 0; i < 5; ++i) { const u32x2 sa = *(LAS u32x2*)(L + L_MOD + (l32 + 32 * i) * 8), ca = *(LAS u32x2*)(L + L_MOD + 1280 + (l32 + 32 * i) * 8);
            u32x2 o;
            o.x = pk2(rnd(xf[4 * i] * rnd(1.f + blo(ca.x))) + blo(sa.x), rnd(xf[4 * i + 1] * rnd(1.f + bhi(ca.x))) + bhi(sa.x));
            o.y = pk2(rnd(xf[4 * i + 2] * rnd(1.f + blo(ca.y))) + blo(sa.y), rnd(xf[4 * i + 3] * rnd(1.f + bhi(ca.y))) + bhi(sa.y));
            *(LAS u32x2*)(L + 4096 + ((l32 >> 1) + 16 * i) * 272 + (row & 15) * 16 + (l32 & 1) * 8) = o; }
        bar();
        #pragma unroll
        for (int i = 0; i < 3; ++i) { const int c = (tid >> 4) + 32 * i;
            st128(lrd(L + 4096 + c * 272 + (tid & 15) * 16), rs, c < 80 ? c * 512 + (tid & 15) * 16 : OOB, WS_H1 + (hid >> 1) * 40960 + (hid & 1) * 256); }
        drain(); bar();
        if (tid == 0) st64(tag, rs, WS_FH1 + hid * 8, 0);
        const u32x2 pv0 = ld_pol(rs, lane);
        TL(1);
        pol = get_pol(rs, lane, dead, tag, pv0);
        TL(4);
    }
    const bool gated = a.tp[0] > 0;
    const unsigned DLT = a.tp[0], DU = a.tp[2];
    int fs0 = 0, fst = 1, fse = a.tp[19];
    int tagc = 0; bool again = false; unsigned nfail = 0, dg_ = 0;
#define DGL
    if (role == R_P) {
        const int h = id;
        u32x8 W[20];
        const Fm fm = ld_mem0(a, wave, lane);
        u32x2 pv0;
        load_w_smf<false>(W, mkrs(wave < 4 ? a.Wq : a.Wk, D * HE * 2), h * 256, wave, lane, L, rs, pv0);
        TL(1);
        if (tid == 0) *(LAS int*)(L + L_FAIL) = -1;
        lptr Asrc = L + L_A + lane * 16;
        lptr pdst = L + L_QK + (wave >> 2) * 2048 + proj2_dst(wave & 3, lane);
        first_mem(fm, wave, Asrc);
        pol = get_pol(rs, lane, dead, tag, pv0);
        asm volatile("" : "+v"(pdst));
        ST(0);
        if (wave == 0) {
            const int rho = lane & 15, g = lane >> 4;
            const Pc pc = mkpc(4, 4, lane);
            const int key = (((rho >> 2) & 1) << 3) | (((rho >> 1) & 1) << 2) | (((rho >> 3) & 1) << 1) | (rho & 1);
            lptr qsrc = L + L_QK + (g >> 1) * 256 + (g & 1) * 32 + tokoff(rho & 7);
            lptr msrc = L + L_QK + 2048 + (g >> 1) * 256 + (g & 1) * 32 + tokoff(2 * (rho >> 2) + (rho & 1));
            lptr ksrc = L + L_KQX + (g * 32 + key) * 16;
            asm volatile("" : "+v"(qsrc), "+v"(msrc));
            const bool ch = (lane & 8) != 0;
            const unsigned vpx = WS_SLOT + SL_PX + (h >> 4) * CH + (h & 15) * 512 + ((lane >> 3) & 1) * 256 + (g >> 1) * 32 + tokoff(lane & 7) + (g & 1) * 8;
            const unsigned vpm = ch ? OOB : WS_SLOT + SL_PM + ((lane >> 2) & 1) * 2048 + (h >> 1) * 128 + tokoff(lane & 3) + (g & 1) * 32 + (h & 1) * 8 + (g >> 1) * 4;
            unsigned long long rt0 = 0;
            for (int t = 0; t < NSEG; ++t) {
                ST_T(t); if (t == 2) { ST_RESET; }
                u32x4 k0[4], k1[4];
                int fl;
                unsigned so = SO(t);
                lptr kp = ksrc + (t & 1) * 16384;
                asm volatile("" : "+s"(so), "+v"(kp));
                auto ldk = [&]() {
                    #pragma unroll
                    for (int s = 0; s < 4; ++s) { k0[s] = lrd(kp + s * 2048); k1[s] = lrd(kp + s * 2048 + 256); } };
                auto scores = [&]() -> bool {
                    u32x4 q[4], km[4];
                    #pragma unroll
                    for (int s = 0; s < 4; ++s) { q[s] = lrd(qsrc + s * 512); SB(); }
                    #pragma unroll
                    for (int s = 0; s < 4; ++s) { km[s] = lrd(msrc + s * 512); SB(); }
                    fl = *(LAS int*)(L + L_FAIL);
                    SB();
                    f32x4 sm = {0, 0, 0, 0}, s0 = sm, s1 = sm;
                    #pragma unroll
                    for (int s = 0; s < 4; ++s) { s0 = mf16(k0[s], q[s], s0); s1 = mf16(k1[s], q[s], s1); }
                    asm volatile("" : "+v"(s0), "+v"(s1), "+v"(km[0]), "+v"(fl));
                    #pragma unroll
                    for (int s = 0; s < 4; ++s) sm = mf16(km[s], q[s], sm);
                    asm volatile("" : "+v"(sm));
                    if (__builtin_expect(__builtin_amdgcn_readfirstlane(fl) == tagc, 0)) return true;
                    const float mx = fmx(fmx(fmx(fmx(s0[0], s0[1]), s0[2]), fmx(fmx(s0[3], s1[0]), s1[1])), fmx(s1[2], s1[3]));
                    float v0 = ch ? s1[0] : s0[0], v1 = ch ? s1[1] : s0[1], v2 = ch ? s1[2] : s0[2], v3 = ch ? s1[3] : s0[3];
                    SB();
                    float ma = fmx(fmx(mx, sm[0]), sm[1]), mb = fmx(fmx(mx, sm[1]), sm[0]), m2, m3;
                    float vm = ch ? sm[1] : sm[0];
                    sw16(ma, mb); max3x2(ma, mb, mb, m2, m3); sw32(m2, m3);
                    const float mc = fmx(m2, m3) * CS;
                    v0 = __builtin_amdgcn_exp2f(v0 * CS - mc); v1 = __builtin_amdgcn_exp2f(v1 * CS - mc); v2 = __builtin_amdgcn_exp2f(v2 * CS - mc); v3 = __builtin_amdgcn_exp2f(v3 * CS - mc); vm = __builtin_amdgcn_exp2f(vm * CS - mc);
                    float sa, sb2, sc, sd;
                    addx2((v0 + v1) + v2, v3 + vm, sa, sb2); sw16(sa, sb2); addx2(sa, sb2, sc, sd); sw32(sc, sd);
                    float sum = sc + sd;
                    const float vmo = ror<8>(vm);
                    sum += ror<8>(sum);
                    const float inv = __builtin_amdgcn_rcpf(sum);
                    u32x2 px; px.x = pk2(v0 * inv, v1 * inv); px.y = pk2(v2 * inv, v3 * inv);
                    st64(px, rs, vpx, so);
                    st32(pk2(vm * inv, vmo * inv), rs, vpm, so);
                    return false; };
                FETCH(1, );
                proj2<2>(W, Asrc, pdst, sidx, t, pc, lane, L, false, dg_, ldk);
                ST(2);
                bar();
                ST(3);
                again = scores();
                if (again) { --t; ++nfail; continue; }
                ST(4); LG(0, rt32()); DGL;
            }
        } else if (wave == 1) {
            const int r32 = lane & 31, hf = lane >> 5;
            const int prow = (r32 & 19) | ((r32 & 4) << 1) | ((r32 & 8) >> 1);
            lptr qsrc = L + L_KQX + 8192 + (hf * 32 + r32) * 16;
            lptr ksrc = L + L_KQX + (hf * 32 + prow) * 16;
            lptr msrc = L + L_QK + 2048 + hf * 32 + tokoff(prow & 7);
            const unsigned vax = WS_AX + (h * 2 + (r32 >> 4)) * 1024 + (hf * 16 + (r32 & 15)) * 16;
            const unsigned vax1 = r32 < 16 ? OOB : vax + 512;
            const unsigned vam = WS_AM + ((h >> 2) * 2 + (r32 >> 4)) * 1024 + ((h & 3) * 16 + (r32 & 15)) * 16;
            const int lim = r32 - 8 * hf;
            const Pc pc = mkpc(5, 5, lane);
            for (int t = 0; t < NSEG; ++t) {
                ST_T(t); if (t == 2) { ST_RESET; }
                FETCH(1, );
                if (tagc < 0) drain(); else asm volatile("s_waitcnt vmcnt(1)" ::: "memory");
                st32(0u, rs, (lane || again || t == 0) ? OOB : WS_PP + h * FS - FSEG, t * FSEG);
                proj2<5>(W, Asrc, pdst, sidx, t, pc, lane, L, false, dg_); bar();
                again = failed(tagc, L); if (again) { --t; continue; } DGL;
                f32x16 sx, sm;
                #pragma unroll
                for (int i = 0; i < 16; ++i) { sx[i] = 0; sm[i] = 0; }
                const int po = (t & 1) * 16384;
                #pragma unroll 2
                for (int s = 0; s < 8; ++s) {
                    u32x4 q = lrd(qsrc + po + s * 1024), k = lrd(ksrc + po + s * 1024), km = lrd(msrc + s * 256);
                    sx = mf32(k, q, sx); sm = mf32(km, q, sm);
                }
                float m = sm[0];
                #pragma unroll
                for (int j = 1; j < 8; ++j) m = fmx(m, sm[j]);
                #pragma unroll
                for (int i = 0; i < 16; ++i) { if (16 * (i >> 3) + (i & 7) > lim) sx[i] = -3.0e38f; m = fmx(m, sx[i]); }
                m = x32max(m);
                const float mc = m * CS; float sum = 0, summ = 0;
                #pragma unroll
                for (int i = 0; i < 16; ++i) { sx[i] = __builtin_amdgcn_exp2f(sx[i] * CS - mc); sum += sx[i]; }
                #pragma unroll
                for (int j = 0; j < 8; ++j) { sm[j] = __builtin_amdgcn_exp2f(sm[j] * CS - mc); summ += sm[j]; }
                sum = x32sum(sum) + summ;
                const float inv = __builtin_amdgcn_rcpf(sum);
                #pragma unroll
                for (int p = 0; p < 2; ++p) { u32x4 o; o.x = pk2(sx[8 * p] * inv, sx[8 * p + 1] * inv); o.y = pk2(sx[8 * p + 2] * inv, sx[8 * p + 3] * inv); o.z = pk2(sx[8 * p + 4] * inv, sx[8 * p + 5] * inv); o.w = pk2(sx[8 * p + 6] * inv, sx[8 * p + 7] * inv);
                    st128(o, rs, p ? vax1 : vax, RI(t) * 65536); }
                if (hf == 0) { u32x4 o; o.x = pk2(sm[0] * inv, sm[1] * inv); o.y = pk2(sm[2] * inv, sm[3] * inv); o.z = pk2(sm[4] * inv, sm[5] * inv); o.w = pk2(sm[6] * inv, sm[7] * inv);
                    st128(o, rs, vam, RI(t) * 16384); }
                ST(4);
            }
            drain();
            if (lane == 0) st32(0u, rs, WS_PP + h * FS, (NSEG - 1) * FSEG);
        } else if (wave < 4) {
            const unsigned vsrc = WS_KQ + h * CH + lane * 16;
            lptr ldst = L + L_KQX + (wave - 2) * 8192;
            int seen = -1; unsigned pv = 1;
            const Pc pc = mkpc(0, 0, lane);
            auto fetch = [&](int t) {
                const int pace = seen >= t ? a.tp[7] : 0;
                wait_fl(rs, WS_PQK + h * FS, t, seen, dead, 2);
                pv = ld_fl(rs, t + 1 >= NSEG ? OOB : WS_PQK + h * FS, t + 1);
                const unsigned so = RO(t, wave - 2);
                for (int i = 0; i < 8; ++i) { dma1(rs, ldst + (t & 1) * 16384 + i * 1024, vsrc, so + i * 1024); dly(pace); } };
            fetch(0);
            for (int t = 0; t < NSEG; ++t) {
                ST_T(t); if (t == 2) { ST_RESET; }
                PROJ(4, , false);
                if (fl_ok(pv)) seen = t + 1;
                if (t + 1 < NSEG) fetch(t + 1);
                ST(4);
            }
        } else {
            unsigned tgt = rt32();
            const Pc pc = mkpc(wave - 4, wave + 2, lane);
            const unsigned vcx = WS_SLOT + SL_PX + (h >> 4) * CH + (h & 15) * 512 + lane * 8, vcm = lane < 16 ? WS_SLOT + SL_PM + (lane >> 3) * 2048 + (h >> 1) * 128 + (lane & 7) * 16 + (h & 1) * 8 : OOB;
            for (int t = 0; t < NSEG; ++t) {
                ST_T(t); if (t == 2) { ST_RESET; }
                PROJ(3, gate(tgt), false);
                if (wave == 7) LG(0, tgt);
                if (wave == 6) { st64((u32x2){~0u, ~0u}, rs, vcx, SO(t + 2)); st64((u32x2){~0u, ~0u}, rs, vcm, SO(t + 2)); }
                tgt = (wave == 7 && gated && t > 0) ? read_G(rs, SO(t - 1), lane, dead) + DLT : rt32();
                ST(4);
            }
        }
    } else if (role == R_V) {
        const int h = id;
        u32x8 W[20];
        if (wave < 4) {
            const Fm fm = ld_mem0(a, wave, lane);
            u32x2 pv0;
            load_w_smf<false>(W, mkrs(a.Wv, D * HE * 2), h * 256, wave, lane, L, rs, pv0);
            TL(1);
            if (tid == 0) *(LAS int*)(L + L_FAIL) = -1;
            lptr pdst = L + L_QK + proj2_dst(wave, lane);
            const Pc pc = mkpc(wave, wave, lane);
            lptr Asrc = L + L_A + lane * 16;
            first_mem(fm, wave, Asrc);
            pol = get_pol(rs, lane, dead, tag, pv0);
            unsigned tgt = rt32();
            for (int t = 0; t < NSEG; ++t) {
                ST_T(t); if (t == 2) { ST_RESET; }
                PROJ(1, gate(tgt), false);
                tgt = (wave == 3 && gated && t > 0) ? read_G(rs, SO(t - 1), lane, dead) + DLT : rt32();
                ST(4);
            }
        } else {
            const Fm fm = ld_mem0(a, wave, lane);
            u32x2 pv0;
            load_w_smf<true>(W, mkrs(a.Wo, D * HE * 2), h * E * D * 2, wave, lane, L, rs, pv0);
            TL(1);
            const unsigned vu = WS_SLOT + SL_U + (10 * (wave - 4) + 8 + ((lane >> 4) & 1)) * CH + h * 256 + (lane & 15) * 16 + (lane >> 5) * 8;
            const unsigned vw = WS_SLOT + SL_U + (10 * (wave - 4) + (lane >> 4)) * CH + h * 256 + (lane & 15) * 16;
            const Pc pc = mkpc(wave, wave + 2, lane);
            lptr Asrc = L + L_A + lane * 16;
            first_mem(fm, wave, Asrc);
            pol = get_pol(rs, lane, dead, tag, pv0);
            ST(0);
            for (int t = 0; t < NSEG; ++t) {
                ST_T(t); if (t == 2) { ST_RESET; }
                f32x4 dd[10];
                int fl;
                unsigned so = SO(t), so2 = so + 4 * CH;
                asm volatile("" : "+s"(so), "+s"(so2));
                auto zero = [&]() {
                    #pragma unroll
                    for (int j = 0; j < 10; ++j) { dd[j] = (f32x4){0, 0, 0, 0}; asm volatile("" : "+v"(dd[j])); } };
                auto ustage = [&]() -> bool {
                    const u32x4 a0 = lrd(L + L_QK + lane * 16); SB();
                    const u32x4 a1 = lrd(L + L_QK + 1024 + lane * 16); SB();
                    fl = *(LAS int*)(L + L_FAIL);
                    SB();
                    unsigned o[10];
                    auto fin = [&](int j) { o[j] = pk2(dd[j][0] + dd[j][2], dd[j][1] + dd[j][3]); };
                    auto s16 = [&](int j) { auto r = __builtin_amdgcn_permlane16_swap(o[j], o[j + 1], false, false); o[j] = r[0]; o[j + 1] = r[1]; };
                    auto s32 = [&](int j) { auto r = __builtin_amdgcn_permlane32_swap(o[j], o[j + 2], false, false); o[j] = r[0]; o[j + 2] = r[1]; };
                    auto m0 = [&](int j) { dd[j] = smf(a0, W[2 * j], dd[j], sidx); };
                    auto m1 = [&](int j) { dd[j] = smf(a1, W[2 * j + 1], dd[j], sidx); };
                    m0(0); m0(1); m0(2); m0(3); SB();
                    m1(0); SB(); const int flu = __builtin_amdgcn_readfirstlane(fl); m1(1); SB(); m1(2); m1(3); asm volatile("" : "+v"(dd[0]), "+v"(dd[1]), "+v"(dd[2]), "+v"(dd[3])); SB();
                    if (__builtin_expect(flu == tagc, 0)) return true;
                    m0(4); fin(0); SB(); m0(5); fin(1); SB(); m0(6); fin(2); SB(); m0(7); s16(0); SB();
                    m1(4); fin(3); SB(); m1(5); SB(); m1(6); s16(2); SB(); m1(7); SB();
                    m0(8); s32(0); s32(1); SB(); m0(9); fin(4); SB();
                    m1(8); st128((u32x4){o[0], o[1], o[2], o[3]}, rs, vw, so); fin(5); SB(); m1(9); fin(6); SB();
                    fin(7); s16(4); fin(8); fin(9); s16(6); s16(8); s32(4); s32(5);
                    st64((u32x2){o[8], o[9]}, rs, vu, so);
                    st128((u32x4){o[4], o[5], o[6], o[7]}, rs, vw, so2);
                    return false; };
                zero();
                if (wave >= 6) { FETCH(2, ); bar(); drain(); bar(); check_own<2>(t, lane, pc, Asrc, L); } else { FETCH(1, ); bar(); drain(); bar(); check_own<1>(t, lane, pc, Asrc, L); }
                bar();
                ST(3);
                again = ustage();
                if (again) { --t; continue; }
                ST(4); LG(0, rt32());
                dly(a.tp[9]);
                const unsigned sc = SO(t + 2);
                st128((u32x4){~0u, ~0u, ~0u, ~0u}, rs, vw, sc); dly(a.tp[13]); st128((u32x4){~0u, ~0u, ~0u, ~0u}, rs, vw, sc + 4 * CH); dly(a.tp[13]);
                st64((u32x2){~0u, ~0u}, rs, vu, sc);
            }
        }
    } else if (role == R_C) {
      const int c = id;
      LAS unsigned* gbox = (LAS unsigned*)(L + 98304);
      constexpr int L_CU = 98560;
      const unsigned cmem = WS_SLOT + SL_MEM + (c >> 5) * CH + (c & 31) * 256;
      if (wave >= 4) {
        const unsigned vcf = WS_CF + ((c % 20) * 4 + wave - 4) * CH + lane * 16;
        int seen = -1;
        const unsigned vpv = WS_PVO + (8 * (wave - 4) + (lane & 7)) * FS;
        lptr cdst = L + (wave - 4) * 8192;
        unsigned pv;
        auto ldcf = [&](int t) {
            const bool known = seen >= t;
            wait_fl(rs, vpv, t, seen, dead, 2);
            pv = ld_fl(rs, vpv, t + 1);
            const unsigned so = CO(t, c >= 20);
            for (int i = 0; i < 8; ++i) { dma1(rs, cdst + (t & 1) * 32768 + i * 1024, vcf, so + i * 1024); if (known && i < 7) dly(a.tp[8]); } };
        const unsigned vclr = wave != 7 ? OOB : lane < 32 ? cmem + lane * 8 : lane == 32 ? WS_SLOT + SL_STP + c * 32 : OOB;
        ldcf(0); drain(); bar();
        for (int t = 0;; ++t) {
            if (fl_ok(pv)) seen = t + 1;
            if (t > 0) st64((u32x2){~0u, ~0u}, rs, vclr, SO(t + 2));
            const bool early = seen > t;
            if (t < NSEG - 2 && early) ldcf(t + 1);
            DG(rt32());
            bar();
            bar();
            if (t == NSEG - 2) break;
            if (!early) ldcf(t + 1);
            drain();
            if (!fl_ok(pv)) pv = ld_fl(rs, vpv, t + 2);
            bar();
            DGL;
        }
      } else if (wave >= 2) {
        const unsigned vu = WS_SLOT + SL_U + c * CH + (wave - 2) * 4096 + lane * 16;
        unsigned G = rt32() - 100000u;
        *gbox = G;
        bar();
        for (int t = 0;; ++t) {
            const unsigned so = SO(t);
            unsigned d2_ = 0;
            DG(rt32());
            gate(G + DU);
            bar();
            dma4(rs, L + L_CU + (wave - 2) * 4096, vu, so);
            drain();
            bar();
            if (t == NSEG - 2) break;
            bar();
            if (wave == 2 && gated) {
                dly(a.tp[11]);
                G = read_G(rs, so, lane, dead);
                *gbox = G; }
            if (wave == 3 && c == 0 && t + 4 >= RS) { int sn = -1; wait_fl(rs, WS_PF2 + (lane < 20 ? lane : 0) * FS, t + 4 - RS, sn, dead, 4); }
        }
      } else {
        const int rh = wave, rho = lane & 15, kg = lane >> 4;
        u32x4 cf[32];
        auto ldcf = [&](int t) {
            #pragma unroll
            for (int i = 0; i < 32; ++i) cf[i] = lrd(L + (t & 1) * 32768 + lane * 16 + i * 1024); };
        bar();
        ldcf(0);
        const unsigned opx = WS_SLOT + SL_PX + (2 * (rho >> 3) + (kg >> 1)) * 512 + (kg & 1) * 256 + rh * 128 + (rho & 7) * 16;
        const unsigned opm = WS_SLOT + SL_PM + rh * 2048 + (4 * (rho >> 3) + kg) * 128 + (rho & 7) * 16;
        const unsigned ou = WS_SLOT + SL_U + c * CH + lane * 16;
        const unsigned vmem = (lane & 1) ? OOB : cmem + rh * 128 + (4 * (kg & 1) + 2 * (rho >> 3) + (lane >> 5)) * 16 + (rho & 7) * 2;
        const unsigned vstp = lane ? OOB : WS_SLOT + SL_STP + c * 32 + rh * 4;
        const bool hi = lane >= 32;
        unsigned nlate = 0;
        for (int t = 0; t < NSEG - 1; ++t) {
            ST_T(t); if (t == 2) { ST_RESET; }
            const unsigned sb = SO(t);
            u32x4 p[10], u[8];
            f32x4 ca, cb;
            unsigned res; float v;
            auto ldu = [&]() {
                #pragma unroll
                for (int i = 0; i < 8; ++i) u[i] = ld_sc1(rs, ou + (i & 3) * 1024, sb + (i >> 2) * 4096); };
            auto ldp = [&]() {
                #pragma unroll
                for (int i = 0; i < 8; ++i) { p[i] = ld_sc1(rs, opx + (i & 1) * 2048, sb + (i >> 2) * CH + ((i >> 1) & 1) * 4096); __builtin_amdgcn_sched_barrier(0); }
                p[8] = ld_sc1(rs, opm, sb); __builtin_amdgcn_sched_barrier(0); p[9] = ld_sc1(rs, opm + 1024, sb); };
            auto comb = [&](bool first) {
                #pragma unroll
                for (int f = 0; f < 8; ++f) {
                    ca = smf(p[f], __builtin_shufflevector(cf[4 * f], cf[4 * f + 1], 0, 1, 2, 3, 4, 5, 6, 7), ca, sidx);
                    cb = smf(p[f], __builtin_shufflevector(cf[4 * f + 2], cf[4 * f + 3], 0, 1, 2, 3, 4, 5, 6, 7), cb, sidx);
                    if (first && f == UBAR) { asm volatile("" : "+v"(ca), "+v"(cb)); bar();
                        #pragma unroll
                        for (int i = 0; i < 8; ++i) u[i] = lrd(L + L_CU + lane * 16 + i * 1024); }
                    __builtin_amdgcn_sched_barrier(0); }
                #pragma unroll
                for (int f = 0; f < 2; ++f) {
                    ca = smf(p[8 + f], __builtin_shufflevector(u[4 * f], u[4 * f + 1], 0, 1, 2, 3, 4, 5, 6, 7), ca, sidx);
                    cb = smf(p[8 + f], __builtin_shufflevector(u[4 * f + 2], u[4 * f + 3], 0, 1, 2, 3, 4, 5, 6, 7), cb, sidx);
                    __builtin_amdgcn_sched_barrier(0); }
                const float t0 = hi ? cb[0] + cb[2] : ca[0] + ca[2], t1 = hi ? cb[1] + cb[3] : ca[1] + ca[3];
                v = fold32(t0, t1);
                res = pk2(v, __int_as_float(__builtin_amdgcn_update_dpp(0, __float_as_int(v), 0xB1, 0xf, 0xf, true)));
                asm volatile("" : "+v"(res)); };
            ca = (f32x4){0, 0, 0, 0}; cb = ca;
            asm volatile("" : "+v"(ca), "+v"(cb));
            bar();
            ST(5);
            ldp();
            const unsigned G = __builtin_amdgcn_readfirstlane(*gbox);
            __builtin_amdgcn_sched_barrier(0);
            comb(true);
            if (__builtin_expect(__builtin_amdgcn_ballot_w64(v != v) != 0ull, 0)) {
                for (unsigned sp = 0;; ++sp) {
                    unsigned m = 0, mu = 0;
                    #pragma unroll
                    for (int i = 0; i < 10; ++i) m = mx4(p[i], m);
                    #pragma unroll
                    for (int i = 0; i < 8; ++i) mu = mx4(u[i], mu);
                    if (all_ok(m > mu ? m : mu) || dead) break;
                    if (sp == 0) nfail += all_ok(mu) ? 1 : 0x10000;
                    if (sp > SPIN_MAX) dead = true;
                    asm volatile("" ::: "memory");
                    if (mu == 0xFFFFFFFFu) ldu();
                    if (m == 0xFFFFFFFFu) ldp(); }
                ca = (f32x4){0, 0, 0, 0}; cb = ca;
                comb(false); }
            ST(0);
            st32(res, rs, vmem, sb);
            ST(3);
            const unsigned now = rt32();
            LG(0, now); LG(1, G);
            unsigned Sn = G + DLT;
            const int lt = (int)(now - Sn);
            if (lt > a.tp[5]) { ++nlate; Sn += max((lt + a.tp[6] + 3) & ~3, a.tp[12]); if (t == 0) Sn &= ~3u; }
            st32(nosent(Sn), rs, vstp, sb);
            if (t == NSEG - 2) break;
            bar();
            ldcf(t + 1);
            ST(4);
            if (t == 0) TL(2);
        }
      }
    } else if (role == R_QK || role == R_VO) {
        const int h = id; const bool vo = role == R_VO, st2 = vo && wave >= 4;
        u32x4 W[40];
        bool allh = false;
        int seen = -1;
        auto waith = [&](int s) {
            if (!allh) for (unsigned sp = 0;; ++sp) { asm volatile("" ::: "memory");
                const u32x4 f = ld_sc1(rs, WS_FH1 + lane * 16, 0);
                const unsigned long long bad = __builtin_amdgcn_ballot_w64(f.x != tag.x || f.y != tag.y || f.z != tag.x || f.w != tag.y);
                allh = bad == 0ull;
                if (!((bad >> s) & 1ull) || dead) break;
                if (sp > SPIN_MAX) { dead = true; break; }
                dly(2); }
            asm volatile("" ::: "memory"); };
        LAS unsigned* cnt = (LAS unsigned*)(L + 122880);
        auto img = [&](int s) { return L + ((s & 1) ? 0 : 81920); };
        auto dmah = [&](int s, int i, unsigned v) { __builtin_amdgcn_raw_ptr_buffer_load_lds(rs, (LAS void*)(img(s) + i * 1024), 16, v, WS_H1 + s * 40960 + i * 1024, 0, HAUX); };
        auto start = [&](auto kind) {
            u32x4 fh; u32x2 pv0;
            bool got;
            auto ldh = [&](unsigned v) {
                #pragma unroll
                for (int i = 0; i < 5; ++i) dmah(0, 5 * wave + i, v); };
            auto hook = [&](int r) {
                if (r == FHR) fh = ld_sc1(rs, WS_FH1 + lane * 16, 0);
                if (r == 6) { const unsigned long long bad = __builtin_amdgcn_ballot_w64(fh.x != tag.x || fh.y != tag.y || fh.z != tag.x || fh.w != tag.y);
                    allh = bad == 0ull; got = (bad & 1ull) == 0ull;
                    pv0 = ld_pol(rs, lane); ldh(got ? lane * 16 : OOB); } };
            if (kind.value == 1) load_w_32<false>(W, mkrs(vo ? a.Wv : wave < 4 ? a.Wq : a.Wk, D * HE * 2), h * 256, wave, lane, L, hook);
            if (kind.value == 2) load_w_32<true>(W, mkrs(a.Wo, D * HE * 2), h * E * D * 2, wave, lane, L, hook);
            TL(1);
            if (!got) { waith(0); ldh(lane * 16); }
            if (wave < 2) cnt[tid] = 0;
            pol = get_pol(rs, lane, dead, tag, pv0);
            __builtin_amdgcn_s_waitcnt(0x0F70);
            bar(); };
        if (!st2) {
            const unsigned vdst = WS_KQ + h * CH + (wave & 3) * 2048 + lane * 16;
            const bool ld = vo || wave >= 4;
            const unsigned vpr = wave != 1 ? OOB : vo ? WS_POUT + (lane < 40 ? lane : 0) * FS : WS_PP + h * FS;
            start(Ic<1>());
            if (!ld) __builtin_amdgcn_s_setprio(1);
            for (int s = 0; s < NSEG; ++s) {
                if (s == 1) { ST_RESET; }
                lptr b = img(s) + lane * 16;
                u32x4 f[FV];
                #pragma unroll
                for (int m = 0; m < FV; ++m) f[m] = lrd(b + m * 1024);
                SB();
                if (ld && (s > 0 || !vo)) { if (s + 1 < NSEG) waith(s + 1);
                    const unsigned vh = s + 1 < NSEG ? lane * 16 : OOB;
                    #pragma unroll
                    for (int i = 0; i < 10; ++i) dmah(s + 1, 10 * (wave & 3) + i, vh);
                    if (!vo) { asm volatile("s_waitcnt vmcnt(10)" ::: "memory");
                        unsigned old = lds_inc(cnt + lane); asm volatile("s_waitcnt lgkmcnt(0)" : "+v"(old));
                        st32(0u, rs, ((old & 3u) | lane * 4u) == 3u ? 0 : OOB, WS_PQK + h * FS + (s - 1) * FSEG);
                        if (s == 1) TL(2); } }
                const unsigned pr = ld_fl(rs, s < RC - 1 ? OOB : vpr - (RC - 1) * FSEG, s);
                SB();
                ST(0);
                f32x16 acc;
                #pragma unroll
                for (int i = 0; i < 16; ++i) acc[i] = 0;
                #pragma unroll
                for (int ks = 0; ks < 40; ++ks) { acc = mf32(W[ks], f[ks % FV], acc); if (ks + FV < 40) f[ks % FV] = lrd(b + (ks + FV) * 1024); SB(); }
                const bool rok = fl_ok(pr);
                const u32x4 o0 = pk8(acc, 0), o1 = pk8(acc, 1);
                if (vo) { lptr vw = L + 40960 + (s & 1) * 8192 + wave * 2048 + lane * 16; lwr(vw, o0); lwr(vw + 1024, o1); }
                else { const unsigned so = RO(s, wave < 4); st128(o0, rs, vdst, so); st128(o1, rs, vdst + 1024, so); }
                if (!rok) wait_ring(rs, vpr, s - (RC - 1), seen, dead);
                ST(1);
                if (wave < 4 || s == NSEG - 1) drain(); else asm volatile("s_waitcnt vmcnt(2)" ::: "memory");
                ST(3);
                bar();
                ST(4);
            }
            if (!vo && tid == 0) st32(0u, rs, 0, WS_PQK + h * FS + (NSEG - 1) * FSEG);
        } else {
            const int r32 = lane & 31, hf = lane >> 5;
            const int prow = (r32 & 19) | ((r32 & 4) << 1) | ((r32 & 8) >> 1);
            lptr asrc = L + 40960 + hf * 512 + prow * 16;
            const unsigned vdst = WS_CF + ((10 * (wave & 1) + (r32 >> 4)) * 4 + (h >> 3)) * CH + (h & 7) * 1024 + (hf * 16 + (r32 & 15)) * 16;
            start(Ic<2>());
            __builtin_amdgcn_s_setprio(1);
            waith(1);
            #pragma unroll
            for (int i = 0; i < 10; ++i) dmah(1, 10 * (wave - 4) + i, lane * 16);
            drain();
            for (int s = 0; s < NSEG; ++s) {
                ST(3);
                bar();
                if (s == 1) { ST_RESET; }
                ST(4);
                u32x4 af[8];
                #pragma unroll
                for (int ks = 0; ks < 8; ++ks) af[ks] = lrd(asrc + (s & 1) * 8192 + ks * 1024);
                SB();
                const unsigned so = CO(s, wave >= 6);
                f32x16 acc[2];
                auto fin = [&](int tt, int p) { const f32x16& c = acc[tt & 1];
                    st128((u32x4){pk2(c[8 * p], c[8 * p + 1]), pk2(c[8 * p + 2], c[8 * p + 3]), pk2(c[8 * p + 4], c[8 * p + 5]), pk2(c[8 * p + 6], c[8 * p + 7])}, rs, vdst + p * 512, so + tt * (8 * CH)); };
                #pragma unroll
                for (int tt = 0; tt < 5; ++tt) {
                    #pragma unroll
                    for (int i = 0; i < 16; ++i) acc[tt & 1][i] = 0;
                    #pragma unroll
                    for (int ks = 0; ks < 8; ++ks) { acc[tt & 1] = mf32(af[ks], W[8 * tt + ks], acc[tt & 1]);
                        asm volatile("" : "+v"(acc[tt & 1]));
                        if (tt > 0 && ks == 2) fin(tt - 1, 0);
                        if (tt > 0 && ks == 4) fin(tt - 1, 1);
                        SB(); }
                }
                fin(4, 0); fin(4, 1);
                ST(1);
                drain();
                unsigned old = lds_inc(cnt + lane); asm volatile("s_waitcnt lgkmcnt(0)" : "+v"(old));
                st32(0u, rs, ((old & 3u) | lane * 4u) == 3u ? 0 : OOB, WS_PVO + h * FS + s * FSEG);
                if (s == 0) TL(2);
            }
        }
        if (id < 27) {
            __builtin_amdgcn_s_setprio(0);
            role = R_F1; fs0 = a.tp[19] + (vo ? 0 : 1); fst = 2; fse = NSEG; }
    } else if (role == R_OUT) {
        const int c = id;
        const unsigned lo = (lane >> 5) * 1024 + (lane & 31) * 16;
        const unsigned vcf = WS_CF + ((c % 20) * 4 + (wave >> 1)) * CH + (wave & 1) * 4096 + lo;
        const unsigned vax = WS_AX + wave * 8192 + (lane >> 5) * 2048 + (lane & 31) * 16, vam = WS_AM + wave * 2048 + lane * 16;
        const unsigned vu = WS_SLOT + SL_U + c * CH + wave * 1024 + lane * 16;
        lptr part = L + wave * 2560 + (lane & 15) * 80 + (lane >> 4) * 16;
        const int tok = 4 * wave + (lane >> 4), col = lane & 15;
        lptr rsrc = L + tok * 80 + col * 4;
        LAS unsigned* cnt = (LAS unsigned*)(L + 49152);
        const unsigned vx = tok * (D * 2) + c * 32 + col * 2, vo = WS_X1 + c * 1024 + wave * 128 + (((col >> 3) ^ (wave >> 1)) & 1) * 64 + (lane >> 4) * 16 + (col & 7) * 2;
        const unsigned vl = col ? OOB : WS_LS + (tok >> 4) * 5120 + c * 128 + (tok & 15) * 8;
        const unsigned vpp = WS_PP + (4 * wave + (lane & 3)) * FS, vpv = WS_PVO + (4 * wave + (lane & 3)) * FS;
        const rsrc_t rx = mkrs(a.x, S * D * 2);
        const float ga = b2f(*(LAS u16*)(L + L_MOD + 2560 + c * 32 + col * 2));
        int seenv = -1, seenp = -1;
        u32x4 cf[4];
        auto ldcf = [&](int s) {
            wait_fl(rs, vpv, s, seenv, dead, 4);
            const unsigned so = CO(s, c >= 20);
            #pragma unroll
            for (int i = 0; i < 4; ++i) cf[i] = ld_sc1(rs, vcf + (i >> 1) * 2048 + (i & 1) * 512, so); };
        auto tick = [&]() { unsigned old = 0; if (lane == 0) old = __hip_atomic_fetch_add(cnt, 1u, __ATOMIC_RELAXED, __HIP_MEMORY_SCOPE_WORKGROUP); return old; };
        auto done = [&](int s) { if (lane == 0 && (__hip_atomic_fetch_add(cnt, 1u, __ATOMIC_RELAXED, __HIP_MEMORY_SCOPE_WORKGROUP) & 7u) == 7u) st32(0u, rs, WS_POUT + id * FS, s * FSEG); };
        if (tid == 0) *cnt = 0;
        ldcf(0);
        bool pub = false;
        unsigned hold = 0;
        for (int s = 0; s < NSEG; ++s) {
            if (s == 2) { ST_RESET; }
            wait_fl(rs, vpp, s, seenp, dead, s < 63 ? a.tp[17] : 2);
            ST(0);
            DG(rt32());
            u32x4 ax[6], am0, am1, u;
            unsigned xr = __builtin_amdgcn_raw_buffer_load_b16(rx, vx, s * (32 * D * 2), 0);
            SB();
            #pragma unroll
            for (int j = 0; j < 2; ++j) { ax[3 * j] = ld_sc1(rs, vax, RI(s) * 65536 + j * 4096); ax[3 * j + 1] = ld_sc1(rs, vax + 1024, RI(s) * 65536 + j * 4096); ax[3 * j + 2] = ld_sc1(rs, vax + 1536, RI(s) * 65536 + j * 4096); }
            u = ld_sc1(rs, vu, SO(s));
            am0 = ld_sc1(rs, vam, RI(s) * 16384); am1 = ld_sc1(rs, vam + 1024, RI(s) * 16384);
            SB();
            const unsigned pw = ld_fl(rs, s + 2 < NSEG ? vpv : OOB, s + 2);
            SB();
            unsigned old = 0;
            if (pub) { asm volatile("s_waitcnt vmcnt(11)" :: "v"(hold) : "memory"); old = tick(); }
            st32(0u, rs, (old & 7u) == 7u ? WS_POUT + id * FS : OOB, (s - 1) * FSEG);
            SB();
            ST(1);
            f32x4 d0 = {0, 0, 0, 0}, d1 = d0;
            #pragma unroll
            for (int j = 0; j < 2; ++j) { d0 = mf16(cf[2 * j], ax[3 * j], d0); d1 = mf16(cf[2 * j], ax[3 * j + 1], d1); d1 = mf16(cf[2 * j + 1], ax[3 * j + 2], d1); }
            if (__builtin_expect(!all_ok(mx4(u, 0)), 0))
                for (unsigned sp = 0;; ++sp) { asm volatile("" ::: "memory");
                    u = ld_sc1(rs, vu, SO(s));
                    if (all_ok(mx4(u, 0)) || dead) break;
                    if (sp > SPIN_MAX) { dead = true; break; }
                    dly(2); }
            d0 = mf16(u, am0, d0); d1 = mf16(u, am1, d1);
            lptr pp = part + (s & 1) * 20480;
            *(LAS f32x4*)pp = d0; *(LAS f32x4*)(pp + 1280) = d1;
            if (fl_ok(pw)) seenv = s + 2;
            unsigned pv; unsigned long long mk;
            asm volatile("buffer_load_dword %0, %2, %3, %4 offen sc1" : "=&v"(pv), "+v"(xr) : "v"(s + 1 < NSEG ? vpp : OOB), "s"(rs), "s"((s + 1) * FSEG) : "memory");
            if (s + 1 < NSEG) ldcf(s + 1);
            ST(3);
            bar();
            ST(4);
            float q[8];
            #pragma unroll
            for (int w = 0; w < 8; ++w) q[w] = *(LAS float*)(rsrc + (s & 1) * 20480 + w * 2560);
            const unsigned o = pk2(blo(xr) + rnd(ga * rnd(((q[0] + q[1]) + (q[2] + q[3])) + ((q[4] + q[5]) + (q[6] + q[7])))), 0.f);
            __builtin_amdgcn_raw_buffer_store_b16((unsigned short)o, rs, vo, RI(s) * (32 * D * 2), 16);
            const float of = blo(o);
            st64((u32x2){__float_as_uint(sum16(of)), __float_as_uint(sum16(of * of))}, rs, vl, RI(s) * 10240);
            if (s + 1 == NSEG) drain();
            unsigned p2;
            asm volatile("buffer_load_dword %1, %3, %4, %5 offen sc1\n\ts_waitcnt vmcnt(7)\n\tv_cmp_ne_u32 %0, 0, %2" : "=s"(mk), "=&v"(p2) : "v"(pv), "v"(s + 1 < NSEG ? vpp : OOB), "s"(rs), "s"((s + 1) * FSEG) : "memory");
            pub = mk == 0ull;
            if (!pub) { asm volatile("s_waitcnt vmcnt(0)\n\tv_cmp_ne_u32 %0, 0, %1" : "=s"(mk) : "v"(p2) : "memory"); done(s); }
            if (mk == 0ull) seenp = s + 1;
            hold = p2;
            LS2(dg_, te_);
            ST(5);
            if (s == 0) TL(2);
        }
        drain();
        done(NSEG - 1);
    } else if (role == R_F2) {
        const int cg = id >> 1, mt = id & 1;
        const int ks0 = wave < 6 ? 7 * wave : 42 + 6 * (wave - 6);
        u32x4 W[28];
        gate(tstart + a.tp[23]);
        {   const rsrc_t rw = mkrs(a.wd, F * D * 2);
            const unsigned vw = (32 * ks0 + 8 * (lane >> 4) + ((lane >> 2) & 3)) * (D * 2) + cg * 128 + (lane & 3) * 16;
            u32x4 v[28];
            #pragma unroll
            for (int i = 0; i < 28; ++i) v[i] = ld_pl(rw, vw, (i >= 24 && wave >= 6) ? 0 : (32 * (i >> 2) + 4 * (i & 1)) * (D * 2) + ((i >> 1) & 1) * 64);
            __builtin_amdgcn_sched_barrier(0);
            lptr wp = stg + lane * 16, rp = stg + (lane >> 2) * 64 + (lane & 3) * 8;
            #pragma unroll
            for (int i = 0; i < 28; ++i) { const int pi = i & 3;
                if (i >= 24 && wave >= 6) v[i] = (u32x4){0, 0, 0, 0};
                lwr(wp + pi * 1024, v[i]);
                const u32x2 r0 = trd(rp + pi * 1024), r1 = trd(rp + pi * 1024 + 32);
                const int i0 = (i & ~3) + (pi & 2);
                W[i0][2 * (pi & 1)] = r0.x; W[i0][2 * (pi & 1) + 1] = r0.y; W[i0 + 1][2 * (pi & 1)] = r1.x; W[i0 + 1][2 * (pi & 1) + 1] = r1.y; } }
        TL(2);
        bar();
        const int ab = mt * 54 + ks0;
        const unsigned vact = WS_ACT + lane * 16;
        const int pf = ((ks0 >> 1) + (lane & 3)) < 27 ? (ks0 >> 1) + (lane & 3) : 26;
        lptr part = L + wave * 4352 + (lane & 15) * 272 + (lane >> 4) * 16;
        lptr rsrc = L + (tid >> 5) * 272 + (tid & 31) * 8;
        const unsigned vx = (mt * 16 + (tid >> 5)) * (D * 2) + cg * 128 + (tid & 31) * 4;
        const unsigned vx1 = WS_X1 + (cg * 4 + ((tid & 31) >> 3)) * 1024 + (mt * 4 + (tid >> 7)) * 128 + (((tid >> 2) ^ (tid >> 8)) & 1) * 64 + ((tid >> 5) & 3) * 16 + (tid & 3) * 4;
        const unsigned gb = *(LAS unsigned*)(L + L_MOD + 6400 + cg * 128 + (tid & 31) * 4);
        const rsrc_t ro = mkrs(a.out, S * D * 2);
        int seen = -1;
        auto vfl = [&](int s) { return s + 2 < NSEG ? WS_PF1 + pf * FS : s < NSEG ? WS_POUT + (4 * cg + (lane & 3)) * FS : OOB; };
        unsigned xr = 0, ts_ = 0;
        auto epi = [&](int s) {
            bar();
            st32(0u, rs, (tid == 0 && s > 0) ? WS_PF2 + id * FS : OOB, (s - 1) * FSEG);
            f32x2 r = {0, 0};
            #pragma unroll
            for (int w = 0; w < 8; ++w) r += *(LAS f32x2*)(rsrc + (s & 1) * 34816 + w * 4352);
            __builtin_amdgcn_raw_buffer_store_b32(pk2(blo(xr) + rnd(blo(gb) * rnd(r[0])), bhi(xr) + rnd(bhi(gb) * rnd(r[1]))), ro, vx, s * (32 * D * 2), 0);
            LS2(ts_, rt32());
            SB(); };
        bool pre = false;
        for (int s = 0; s < NSEG; ++s) {
            if (!pre) {
                if (s > 0) { const unsigned pv = ld_fl(rs, vfl(s), s); epi(s - 1); if (fl_ok(pv)) seen = s; }
                wait_fl(rs, vfl(s), s, seen, dead, s < 60 ? a.tp[18] : 2); }
            DG(rt32());
            u32x4 af[7];
            auto lda = [&]() {
                #pragma unroll
                for (int i = 0; i < 7; ++i) { const int b = ab + ((i == 6 && wave >= 6) ? 0 : i); af[i] = ld_sc1(rs, vact, AO(s) + (b >> 3) * CH + (b & 7) * 1024); } };
            lda();
            const unsigned xn = __builtin_amdgcn_raw_buffer_load_b32(rs, vx1, RI(s) * (32 * D * 2), 16), pe = ld_fl(rs, vfl(s + 1), s + 1);
            SB();
            if (pre) epi(s - 1);
            f32x4 d[4];
            for (unsigned sp = 0;; ++sp) {
                #pragma unroll
                for (int j = 0; j < 4; ++j) d[j] = (f32x4){0, 0, 0, 0};
                unsigned m = 0;
                #pragma unroll
                for (int i = 0; i < 7; ++i) {
                    #pragma unroll
                    for (int j = 0; j < 4; ++j) d[j] = mf16(W[4 * i + j], af[i], d[j]);
                    m = mx4(af[i], m);
                    SB(); }
                if (all_ok(m) || dead) break;
                if (sp > SPIN_MAX) { dead = true; break; }
                asm volatile("" ::: "memory");
                if (m == 0xFFFFFFFFu) lda(); }
            #pragma unroll
            for (int j = 0; j < 4; ++j) *(LAS f32x4*)(part + (s & 1) * 34816 + j * 64) = d[j];
            SB();
            xr = xn; ts_ = dg_;
            pre = fl_ok(pe);
        }
        epi(NSEG - 1);
        if (id == 0 && tid == 0) st64(tag, rs, WS_EP, 0);
    }
    if (role == R_F1) {
        int lane; asm volatile("v_mbcnt_lo_u32_b32 %0, -1, 0\n\tv_mbcnt_hi_u32_b32 %0, -1, %0" : "=v"(lane)); lane &= 63; const int tid = wave * 64 + lane;
        u32x4 W[4][10];
        f32x4 cs[2], bs[2];
        const int kg = lane >> 4, t16 = lane & 15, kh = (wave >> 1) & 1, cw = 2 * (wave & 1) + kh, th = wave >> 2;
        const unsigned vfw = WS_FW + (id * 4 + (wave & 3)) * 45056 + lane * 16;
        if (fs0) {
            int sn = -1;
            wait_fl(rs, WS_PF1 + id * FS, FW0 + 4 * FWS, sn, dead, 8);
            #pragma unroll
            for (int i = 0; i < 40; ++i) { W[i / 10][i % 10] = ld_sc1(rs, vfw, i * 1024); if ((i & 3) == 3) { SB(); dly(a.tp[21]); } }
            #pragma unroll
            for (int i = 0; i < 2; ++i) { cs[i] = __builtin_bit_cast(f32x4, ld_sc1(rs, vfw, (40 + i) * 1024)); bs[i] = __builtin_bit_cast(f32x4, ld_sc1(rs, vfw, (42 + i) * 1024)); }
            drain();
        } else {
        gate(tstart + a.tp[23]);
        #pragma unroll
        for (int mat = 0; mat < 2; ++mat) {
            const rsrc_t rg = mkrs(mat ? a.wu : a.wg, D * F * 2);
            const unsigned vw = (wave * 80 + ((lane >> 1) & 7)) * (F * 2) + id * 128 + (lane >> 4) * 32 + (lane & 1) * 16;
            bar();
            #pragma unroll 1
            for (int i = 0; i < 10; ++i) { __builtin_amdgcn_raw_ptr_buffer_load_lds(rg, (LAS void*)(L + (wave * 10 + i) * 1024), 16, vw, i * 8 * F * 2, 0, 0); }
            drain();  bar();
            lptr rp = L + kg * 1024 + cw * 256 + t16 * 8;
            const u32x4 ones = {0x3F803F80u, 0x3F803F80u, 0x3F803F80u, 0x3F803F80u};
            cs[mat] = (f32x4){0, 0, 0, 0}; bs[mat] = cs[mat];
            #pragma unroll 4
            for (int ks = 0; ks < 20; ++ks) { const u32x2 a0 = trd(rp + ks * 4096), a1 = trd(rp + ks * 4096 + 128);
                bs[mat] = mf16((u32x4){a0.x, a0.y, a1.x, a1.y}, lrd(L + L_MOD + 3840 + (32 * ks + 8 * kg) * 2), bs[mat]); }
            bar();
            #pragma unroll 2
            for (int i = 0; i < 10; ++i) { lptr q = L + (wave * 10 + i) * 1024 + lane * 16;
                u32x4 w = lrd(q);
                const float sc = rnd(1.f + b2f(*(LAS u16*)(L + L_MOD + 5120 + (wave * 80 + i * 8 + ((lane >> 1) & 7)) * 2)));
                #pragma unroll
                for (int j = 0; j < 4; ++j) w[j] = pk2(blo(w[j]) * sc, bhi(w[j]) * sc);
                lwr(q, w); }
            bar();
            #pragma unroll 4
            for (int ks = 0; ks < 20; ++ks) { const u32x2 a0 = trd(rp + ks * 4096), a1 = trd(rp + ks * 4096 + 128);
                cs[mat] = mf16((u32x4){a0.x, a0.y, a1.x, a1.y}, ones, cs[mat]); }
            #pragma unroll
            for (int j = 0; j < 2; ++j) { lptr r2 = L + kh * 40960 + kg * 1024 + (cw ^ j) * 256 + t16 * 8;
                #pragma unroll
                for (int i = 0; i < 10; ++i) { const u32x2 a0 = trd(r2 + i * 4096), a1 = trd(r2 + i * 4096 + 128); W[2 * j + mat][i] = (u32x4){a0.x, a0.y, a1.x, a1.y}; } }
             }
        }
        if (fs0) { TL(6); } else { TL(2); }
        lptr bb = L + kh * 20480 + (kg >> 1) * 1024 + (th * 4 + (t16 >> 2)) * 128 + ((kg ^ (t16 >> 3)) & 1) * 64 + (t16 & 3) * 16;
        lptr stat = L + 81920 + wave * 1280 + lane * 8;
        lptr xw = L + 102400 + wave * 2048 + lane * 16, xr = L + 102400 + (wave ^ 2) * 2048 + lane * 16;
        lptr sw = L + 118784 + wave * 128 + t16 * 8, sr = L + 118784 + th * 512 + t16 * 8;
        volatile LAS int* rdy = (volatile LAS int*)(L + 119808);
        LAS unsigned* cnt = (LAS unsigned*)(L + 119812);
        const int ab = th * 54 + 2 * id + (cw >> 1);
        const unsigned vact = WS_ACT + (ab >> 3) * CH + (ab & 7) * 1024 + ((2 * (cw & 1) + (kg >> 1)) * 16 + t16) * 16 + (kg & 1) * 8;
        int seen = -1;
        const unsigned vpo = wave ? OOB : WS_POUT + (lane < 40 ? lane : 0) * FS;
        const unsigned vx1 = WS_X1 + wave * 5120 + lane * 16, vls = WS_LS + tid * 16;
        auto ldp = [&](int s, int k, int i) {
            if (i < 5) dma1(rs, L + k * 40960 + wave * 5120 + i * 1024, vx1, RI(s) * (32 * D * 2) + i * 1024);
            else if (i == 5) dma1(rs, L + 81920 + k * 10240 + wave * 1024, vls, RI(s) * 10240);
            else if (wave < 2) dma1(rs, L + 81920 + k * 10240 + 8192 + wave * 1024, vls, RI(s) * 10240 + 8192); };
        auto ldh = [&](int s, int k) {
            #pragma unroll
            for (int i = 0; i < 7; ++i) ldp(s, k, i); };
        auto ready = [&](int s) { return __builtin_amdgcn_readfirstlane(*rdy) >= s; };
        if (fs0) st64((u32x2){~0u, ~0u}, rs, vact, AO(NSEG - 2 + (fs0 & 1)));
        if (fs0) { wait_fl(rs, vpo, fs0 - 1, seen, dead, 24); dly(a.tp[20]); }
        wait_fl(rs, vpo, fs0, seen, dead, fs0 ? a.tp[22] : 24);
        if (tid == 0) { *rdy = 0; *cnt = 0; }
        bar();
        ldh(fs0, 0);
        drain();
        bool pend = false;
        auto done = [&](int s) { if (lane == 0) { unsigned old; asm volatile("ds_add_rtn_u32 %0, %1, %2\n\ts_waitcnt lgkmcnt(0)" : "=v"(old) : "v"(cnt), "v"(1u) : "memory");
            st32(0u, rs, (old & 7u) == 7u ? WS_PF1 + id * FS : OOB, s * FSEG); } };
        for (int s = fs0, k = 0; s < fse; s += fst, k ^= 1) {
            const int n1 = s + fst, n2 = n1 + fst;
            #pragma unroll
            for (int j = 0; j < 4; ++j) if (__builtin_expect(s == FW0 + j * FWS && th == (j & 1), 0)) {
                #pragma unroll
                for (int i = 0; i < 10; ++i) { st128(W[j][i], rs, vfw, (10 * j + i) * 1024); dly(a.tp[15]); }
                st128(__builtin_bit_cast(u32x4, j < 2 ? cs[j & 1] : bs[j & 1]), rs, vfw, (40 + j) * 1024); }
            if (s == 2) { ST_RESET; }
            unsigned pv, pw;
            if (wave == 0) asm volatile("buffer_load_dword %0, %2, %4, %5 offen sc1\n\tbuffer_load_dword %1, %3, %4, %6 offen sc1" : "=&v"(pv), "=&v"(pw)
                : "v"((n1 < fse && seen < n1) ? vpo : OOB), "v"((n2 < fse && pend) ? vpo : OOB), "s"(rs), "s"((n1) * FSEG), "s"((n2) * FSEG) : "memory");
            ST(0);
            bar();
            ST(1);
            DG(rt32());
            const int bo = k * 40960;
            f32x2 t2, t3, t4;
            u32x4 f[4];
            const int r0 = *rdy;
            #pragma unroll
            for (int m = 0; m < 4; ++m) f[m] = lrd(bb + bo + m * 2048);
            t2 = *(LAS f32x2*)(stat + k * 10240); t3 = *(LAS f32x2*)(stat + k * 10240 + 512); t4 = *(LAS f32x2*)(stat + k * 10240 + 1024);
            SB();
            const bool early = __builtin_amdgcn_readfirstlane(r0) >= n1 && n1 < fse;
            f32x4 d[4];
            #pragma unroll
            for (int j = 0; j < 4; ++j) d[j] = (f32x4){0, 0, 0, 0};
            #pragma unroll
            for (int i = 0; i < 10; ++i) {
                #pragma unroll
                for (int j = 3; j >= 0; --j) d[j] = mf16(W[j][i], f[i & 3], d[j]);
                asm volatile("" : "+v"(d[0]), "+v"(d[1]), "+v"(d[2]), "+v"(d[3]));
                if (i + 4 < 10) f[i & 3] = lrd(bb + bo + (i + 4) * 2048);
                if (i == 5 && pend) { if (early) asm volatile("s_waitcnt vmcnt(5)" ::: "memory"); else drain(); done(s - fst); }
                if (i < 7 && early) ldp(n1, k ^ 1, i);
                if (i == 4) { t2 += t3; if (lane < 32) t2 += t4; }
                if (i == 5) { t2[0] = x16sum(t2[0]); t2[1] = x16sum(t2[1]); }
                if (i == 6) { t2[0] = x32sum(t2[0]); t2[1] = x32sum(t2[1]); *(LAS f32x2*)sw = t2; }
                SB(); }
            *(LAS f32x4*)(xw + 1024) = d[3]; *(LAS f32x4*)xw = d[2];
            if (wave == 0) {
                unsigned long long mv, mw;
                if (!pend) drain();
                asm volatile("v_cmp_ne_u32 %0, 0, %2\n\tv_cmp_ne_u32 %1, 0, %3" : "=&s"(mv), "=&s"(mw) : "v"(pv), "v"(pw) : "memory");
                if (pend && n2 < fse && mw == 0ull) seen = n2; else if (mv == 0ull) seen = max(seen, n1);
                *rdy = seen; }
            bar();
            const int rv = *rdy;
            t2 = *(LAS f32x2*)sr; t3 = *(LAS f32x2*)(sr + 128); t4 = *(LAS f32x2*)(sr + 256);
            const f32x2 t5 = *(LAS f32x2*)(sr + 384);
            d[2] = *(LAS f32x4*)xr; d[3] = *(LAS f32x4*)(xr + 1024);
            SB();
            bool got = n1 >= fse || early;
            if (!got && __builtin_amdgcn_readfirstlane(rv) >= n1) { ldh(n1, k ^ 1); got = true; }
            ST(2);
            t2 = ((t2 + t3) + (t4 + t5)) * (1.f / D);
            const float rstd = __builtin_amdgcn_rcpf(rnd(__builtin_amdgcn_sqrtf(rnd(rnd(__builtin_fmaxf(t2[1] - t2[0] * t2[0], 0.f)) + 1e-5f)))), mean = rnd(t2[0]);
            const f32x4 g = (d[0] + d[2] - cs[0] * mean) * rstd + bs[0], u = (d[1] + d[3] - cs[1] * mean) * rstd + bs[1];
            float act[4];
            #pragma unroll
            for (int i = 0; i < 4; ++i) act[i] = g[i] * u[i] * __builtin_amdgcn_rcpf(1.f + __expf(-g[i]));
            LS2(dg_, (rt32() & ~1u) | (got ? 1u : 0u));
            st64((u32x2){nosent(pk2(act[0], act[1])), nosent(pk2(act[2], act[3]))}, rs, vact, AO(s));
            ST(4);
            pend = got && n1 < fse;
            if (pend) asm volatile("s_waitcnt vmcnt(1)" ::: "memory");
            else {
                const bool ok = fl_ok(ld_fl(rs, got ? OOB : vpo, n1));
                drain();
                if (wave == 0 && ok) { seen = n1; *rdy = n1; }
                done(s);
                if (!got) {
                    if (wave == 0) { wait_fl(rs, vpo, n1, seen, dead, fs0 ? a.tp[22] : a.tp[16]); *rdy = n1; }
                    while (!ready(n1)) dly(1);
                    ldh(n1, k ^ 1); drain(); } }
            ST(5);
            if (s == 0) TL(3);
        }
    }
    TL(7);
    ST_OUT;
}

#define WOFF 0
extern "C" void kernel_launch(void* const* d_in, const int* in_sizes, int n_in, void* d_out, int out_size, void* d_ws, size_t ws_size, hipStream_t stream) {
    unsigned char* wsb = (unsigned char*)(((size_t)d_ws + WOFF + 0x1FFFFF) & ~(size_t)0x1FFFFF);
    Args a{(const u16*)d_in[0], (const u16*)d_in[1], (const u16*)d_in[2], (const u16*)d_in[3], (const u16*)d_in[4], (const u16*)d_in[5],
           (const u16*)d_in[8], (const u16*)d_in[9], (const u16*)d_in[10], (const u16*)d_in[11], (const u16*)d_in[12], (u16*)d_out, wsb, {184, 0, 112, 0, 0, -4, 56, 3, 1, 8, 0, 6, 96, 3, 0, 0, 8, 4, 4, 57, 16, 4, 2, 1400}};
    hipFuncSetAttribute((const void*)mega, hipFuncAttributeMaxDynamicSharedMemorySize, L_TOT);
    mega<<<256, 512, L_TOT, stream>>>(a);
}
```
